```python
import torch

def _softmax(x, axis=-1):
    return torch.softmax(x, dim=axis)

def _gelu(x):
    return torch.nn.functional.gelu(x, approximate='tanh')

def _silu(x):
    return torch.nn.functional.silu(x)

def _sigmoid(x):
    return torch.sigmoid(x)

def _layernorm(x, axis=-1, eps=1e-05):
    m = x.mean(dim=axis, keepdim=True)
    v = x.var(dim=axis, keepdim=True, unbiased=False)
    return (x - m) / torch.sqrt(v + eps)

def _scatter_add(tgt, idx, src, axis=0):
    return tgt.clone().index_add_(axis, idx, src)

def _segment_sum(src, seg, n):
    out = torch.zeros((n,) + src.shape[1:], dtype=src.dtype)
    return out.index_add_(0, seg, src)

def _pad(x, pw):
    flat = tuple((v for pair in reversed(pw) for v in pair))
    return torch.nn.functional.pad(x, flat)
kernel_name = 'adaLN_cond__spectral_gfnet__mixer_channel_mlp__prompt'

def setup_inputs():

    _w = lambda a: (lambda t: t.to(torch.bfloat16) if t.is_floating_point() else t)(torch.as_tensor(a))

    def _A_setup():
        B, S, D = (1, 2048, 1024)
        Df = D // 2 + 1
        return {'x': _w((lambda *s: torch.randn(*s, dtype=torch.bfloat16) * 0.05)(B, S, D)), 'w_r': _w((lambda *s: torch.randn(*s, dtype=torch.bfloat16) * 1.6)(S, Df) * 0.02), 'w_i': _w((lambda *s: torch.randn(*s, dtype=torch.bfloat16) * 1.6)(S, Df) * 0.02)}
    _A_in = _A_setup()

    def _B_setup():
        B, S, D, F = (1, 2048, 1024, 3584)
        return {'x': _w((lambda *s: torch.randn(*s, dtype=torch.bfloat16) * 0.05)(B, S, D)), 'w1': _w((lambda *s: torch.randn(*s, dtype=torch.bfloat16) * 3.2)(D, F) * D ** (-0.5)), 'b1': _w((lambda *s: torch.randn(*s, dtype=torch.bfloat16) * 0.8)(F)), 'w2': _w((lambda *s: torch.randn(*s, dtype=torch.bfloat16) * 0.05)(F, D) * F ** (-0.5)), 'b2': _w((lambda *s: torch.randn(*s, dtype=torch.bfloat16) * 0.1)(D))}
    _B_in = _B_setup()
    return {'A_x': _A_in['x'], 'A_w_r': _A_in['w_r'], 'A_w_i': _A_in['w_i'], 'B_x': _B_in['x'], 'B_w1': _B_in['w1'], 'B_b1': _B_in['b1'], 'B_w2': _B_in['w2'], 'B_b2': _B_in['b2'], 'c': _w((lambda *s: torch.randn(*s, dtype=torch.bfloat16) * 0.2)(1, 1024) * 8.0), 'w_mod': _w((lambda *s: torch.randn(*s, dtype=torch.bfloat16) * 0.2)(1024, 6 * 1024) * 1024 ** (-0.5) * 8.0)}

def reference(A_x, A_w_r, A_w_i, B_x, B_w1, B_b1, B_w2, B_b2, c, w_mod):

    def _A_ref(x, w_r, w_i):
        B, S, D = x.shape
        X = (lambda x, axes=(-2, -1): torch.fft.rfft2(x.float(), dim=axes))(x, axes=(-2, -1))
        W = w_r + 1j * w_i
        X = X * W
        y = (lambda x, s, axes=(-2, -1): torch.fft.irfft2(x, s=s, dim=axes).to(torch.bfloat16))(X, (S, D), axes=(-2, -1))
        return y

    def _B_ref(x, w1, b1, w2, b2):
        y = _layernorm(x, axis=-1)
        h = _gelu(torch.einsum('bsd,df->bsf', y, w1) + b1)
        return torch.einsum('bsf,fd->bsd', h, w2) + b2
    x = A_x
    mod = (_silu(c) @ w_mod).reshape(c.shape[0], 6, 1, -1)
    sa, ca, ga, sb, cb, gb = (mod[:, 0].clone(), mod[:, 1].clone(), mod[:, 2].clone(), mod[:, 3].clone(), mod[:, 4].clone(), mod[:, 5].clone())
    h = _layernorm(x, axis=-1) * (1 + ca) + sa
    x = x + ga * _A_ref(h, A_w_r, A_w_i)
    h = _layernorm(x, axis=-1) * (1 + cb) + sb
    x = x + gb * _B_ref(h, B_w1, B_b1, B_w2, B_b2)
    return x
```

```cpp
#include <hip/hip_runtime.h>
#include <hip/hip_bf16.h>
#include <stdint.h>

typedef unsigned short u16;
typedef unsigned u32;
typedef _Float16 f16;
typedef f16 h8 __attribute__((ext_vector_type(8)));
typedef f16 h4 __attribute__((ext_vector_type(4)));
typedef f16 h2 __attribute__((ext_vector_type(2)));
typedef short s4v __attribute__((ext_vector_type(4)));
typedef short bf16x8 __attribute__((ext_vector_type(8)));
typedef float f32x4 __attribute__((ext_vector_type(4)));
typedef float f32x2 __attribute__((ext_vector_type(2)));
typedef u32 u32x4 __attribute__((ext_vector_type(4)));
typedef u32 u32x2 __attribute__((ext_vector_type(2)));
typedef __bf16 bf2v __attribute__((ext_vector_type(2)));
#define LAS __attribute__((address_space(3)))
#define DI __device__ __forceinline__
#define MF16(a, b, c) __builtin_amdgcn_mfma_f32_16x16x32_f16((a), (b), (c), 0, 0, 0)

constexpr size_t WS_MOD = 0;
constexpr size_t WS_STAT = 32768;
constexpr size_t WS_XB = 1u << 20;
constexpr size_t WS_XC = 6u << 20;
constexpr size_t WS_WL = 11u << 20;
constexpr size_t WS_A1 = 16u << 20;
constexpr size_t WS_X1 = 20u << 20;
constexpr size_t WS_H = 24u << 20;

DI float bflo(u32 v) { return __uint_as_float(v << 16); }
DI float bfhi(u32 v) { return __uint_as_float(v & 0xffff0000u); }
DI u32 pkbf(float a, float b) { f32x2 v = {a, b}; return __builtin_bit_cast(u32, __builtin_convertvector(v, bf2v)); }
DI float rbf(float a) { return bflo(pkbf(a, a)); }
DI u32 pkh(float a, float b) { f32x2 v = {a, b}; return __builtin_bit_cast(u32, __builtin_convertvector(v, h2)); }
DI float cosr(float rev) { return __builtin_amdgcn_cosf(rev); }
DI float sinr(float rev) { return __builtin_amdgcn_sinf(rev); }
DI h8 mk8(u32 a, u32 b, u32 c, u32 d) { u32x4 v = {a, b, c, d}; return __builtin_bit_cast(h8, v); }
DI float wave_sum(float v) {
#pragma unroll
  for (int o = 32; o > 0; o >>= 1) v += __shfl_xor(v, o);
  return v;
}

DI void role_gemv(int blk, const u16* c, const u16* wmod, float* mod, char* lds) {
  const int t = threadIdx.x, cq = t & 3, rg = t >> 2;
  const int col0 = blk * 32 + cq * 8;
  u32x4 w[8]; float sc[8];
#pragma unroll
  for (int i = 0; i < 8; i++) w[i] = *(const u32x4*)(wmod + (size_t)(rg + 128 * i) * 6144 + col0);
#pragma unroll
  for (int i = 0; i < 8; i++) { float x = bflo(c[rg + 128 * i]); sc[i] = rbf(x / (1.0f + __expf(-x))); }
  float acc[8];
#pragma unroll
  for (int e = 0; e < 8; e++) acc[e] = 0.f;
#pragma unroll
  for (int i = 0; i < 8; i++)
#pragma unroll
    for (int e = 0; e < 4; e++) { acc[2 * e] += sc[i] * bflo(w[i][e]); acc[2 * e + 1] += sc[i] * bfhi(w[i][e]); }
#pragma unroll
  for (int e = 0; e < 8; e++) {
#pragma unroll
    for (int o = 4; o < 64; o <<= 1) acc[e] += __shfl_xor(acc[e], o);
  }
  float* red = (float*)lds;
  if ((t & 63) < 4) {
#pragma unroll
    for (int e = 0; e < 8; e++) red[(t >> 6) * 32 + cq * 8 + e] = acc[e];
  }
  __syncthreads();
  if (t < 32) { float s = 0.f;
#pragma unroll
    for (int wv = 0; wv < 8; wv++) s += red[wv * 32 + t];
    mod[blk * 32 + t] = rbf(s); }
}
DI void role_stats(int s2, const u16* x, f32x2* stat) {
  const int w = threadIdx.x >> 6, lane = threadIdx.x & 63, row = 256 * w + s2;
  const u32x4 a = *(const u32x4*)(x + (size_t)row * 1024 + lane * 8), b = *(const u32x4*)(x + (size_t)row * 1024 + 512 + lane * 8);
  float v[16];
#pragma unroll
  for (int e = 0; e < 4; e++) { v[2 * e] = bflo(a[e]); v[2 * e + 1] = bfhi(a[e]); v[8 + 2 * e] = bflo(b[e]); v[9 + 2 * e] = bfhi(b[e]); }
  float s = 0.f;
#pragma unroll
  for (int e = 0; e < 16; e++) s += v[e];
  const float mean = wave_sum(s) * (1.0f / 1024.0f);
  float q = 0.f;
#pragma unroll
  for (int e = 0; e < 16; e++) { float d = v[e] - mean; q += d * d; }
  const float var = rbf(wave_sum(q) * (1.0f / 1024.0f));
  const float rs = 1.0f / rbf(sqrtf(rbf(var + 1e-5f)));
  if (lane == 0) stat[s2 * 8 + w] = (f32x2){rs, -rbf(mean) * rs};
}
DI int blk_base(int j) { return j == 0 ? 192 : (j == 4 ? 224 : (j - 1) * 64); }
DI void role_wl_simple(int idx, const u16* wr, const u16* wi, u32* wl) {
  const int i = idx & 3, lane = (idx >> 2) & 63, g = (idx >> 8) & 3, unit = idx >> 10;
  const int blk = unit >> 2; int j, k1, pq;
  if (blk < 192) { j = 1 + blk / 64; pq = (blk & 63) >> 3; k1 = (blk & 7) * 4 + (unit & 3); }
  else if (blk < 256) { j = blk < 224 ? 0 : 4; pq = (blk & 31) >> 3; k1 = (blk & 7) * 4 + (unit & 3); }
  else { j = (unit & 1) * 4; pq = 4; k1 = 0; }
  const int ks = j + 8 * ((lane & 15) + 16 * (4 * (lane >> 4) + i)), kd = k1 + 32 * (4 * pq + g);
  const int nk = (2048 - ks) & 2047;
  u32 o;
  if (j == 0 || j == 4) {
    if (kd > 512) o = 0;
    else { float a = bflo(wr[ks * 513 + kd]), b = bflo(wi[ks * 513 + kd]); if (kd == 0 || kd == 512) { a *= 0.5f; b *= 0.5f; } o = pkbf(a, b); }
  } else if (kd == 0 || kd == 512) {
    o = pkbf(0.5f * (bflo(wr[ks * 513 + kd]) + bflo(wr[nk * 513 + kd])), 0.5f * (bflo(wi[ks * 513 + kd]) - bflo(wi[nk * 513 + kd])));
  } else if (kd < 512) o = (u32)wr[ks * 513 + kd] | ((u32)wi[ks * 513 + kd] << 16);
  else o = (u32)wr[nk * 513 + 1024 - kd] | (((u32)wi[nk * 513 + 1024 - kd] ^ 0x8000u) << 16);
  wl[idx] = o;
}
DI int s2_of(int b) { return (b & 7) * 32 + (b >> 3); }

__global__ __launch_bounds__(512) void k0(const u16* x, const u16* wr, const u16* wi, const u16* c, const u16* wmod, char* ws) {
  __shared__ __attribute__((aligned(16))) char lds[1024];
  const int b = blockIdx.x;
  if (b < 192) role_gemv(b, c, wmod, (float*)(ws + WS_MOD), lds);
  else if (b < 448) role_stats(s2_of(b - 192), x, (f32x2*)(ws + WS_STAT));
  else role_wl_simple((b - 448) * 512 + threadIdx.x, wr, wi, (u32*)(ws + WS_WL));
}

DI s4v tr_read(u32 addr) { return __builtin_amdgcn_ds_read_tr16_b64_v4i16((LAS s4v*)(size_t)addr); }

__global__ __launch_bounds__(640) void k1(const u16* x, char* ws) {
  __shared__ __attribute__((aligned(16))) char lds[16384];
  const int s2 = s2_of(blockIdx.x), t = threadIdx.x, w = __builtin_amdgcn_readfirstlane(t >> 6), lane = t & 63;
  const float* mod = (const float*)(ws + WS_MOD);
  u32 xv[8]; f32x2 sa, ca;
  if (w < 8) {
#pragma unroll
    for (int i = 0; i < 8; i++) xv[i] = *(const u32*)(x + (size_t)(256 * i + s2) * 1024 + 2 * t);
    sa = *(const f32x2*)(mod + 2 * t); ca = *(const f32x2*)(mod + 1024 + 2 * t);
  }
  const int j = w >> 1, hf = w & 1, q = lane >> 4, l15 = lane & 15, k1i = 16 * hf + l15;
  h8 Fr, Fi, Fn;
  { u32 a[4], bb[4];
#pragma unroll
    for (int e = 0; e < 4; e++) {
      const float p0 = (float)(((8 * q + 2 * e) * k1i) & 31) * (1.0f / 32.0f), p1 = (float)(((8 * q + 2 * e + 1) * k1i) & 31) * (1.0f / 32.0f);
      a[e] = pkh(0.25f * cosr(p0), 0.25f * cosr(p1)); bb[e] = pkh(-0.25f * sinr(p0), -0.25f * sinr(p1)); }
    Fr = mk8(a[0], a[1], a[2], a[3]); Fi = mk8(bb[0], bb[1], bb[2], bb[3]);
    Fn = mk8(bb[0] ^ 0x80008000u, bb[1] ^ 0x80008000u, bb[2] ^ 0x80008000u, bb[3] ^ 0x80008000u); }
  float twr[2][4], twi[2][4];
#pragma unroll
  for (int ut = 0; ut < 2; ut++)
#pragma unroll
    for (int e = 0; e < 4; e++) { const float p = (float)((16 * ut + 4 * q + e) * k1i) * (1.0f / 1024.0f); twr[ut][e] = cosr(p); twi[ut][e] = -sinr(p); }
  u32 g2r[4], g2i[4];
#pragma unroll
  for (int e = 0; e < 4; e++) { const float p = (float)(((4 * q + e) * l15) & 31) * (1.0f / 32.0f); const float cr = 0.125f * cosr(p), ci = -0.125f * sinr(p);
    g2r[e] = pkh(cr, -ci); g2i[e] = pkh(ci, cr); }
  const u32 sgi = (l15 & 1) ? 0x80008000u : 0u;

  if (w < 8) {
    const f32x2* st = (const f32x2*)(ws + WS_STAT) + s2 * 8;
    f32x2 v[8];
#pragma unroll
    for (int i = 0; i < 8; i++) { const f32x2 s = st[i]; v[i] = (f32x2){bflo(xv[i]) * s.x + s.y, bfhi(xv[i]) * s.x + s.y}; }
    const f32x2 g = (f32x2){rbf(1.0f + ca.x), rbf(1.0f + ca.y)} * 0.25f;
    const f32x2 a0 = v[0] + v[4], a1 = v[1] + v[5], a2 = v[2] + v[6], a3 = v[3] + v[7];
    const f32x2 b0 = v[0] - v[4], b1 = v[1] - v[5], b2 = v[2] - v[6], b3 = v[3] - v[7];
    const f32x2 c0 = a0 + a2, c1 = a1 + a3;
    const f32x2 e1 = (b1 - b3) * 0.70710678f, e2 = (b1 + b3) * 0.70710678f;
    f32x2 p[8];
    p[0] = (c0 + c1) * g + sa * 2.0f;
    p[1] = (c0 - c1) * g;
    p[2] = (b0 + e1) * g; p[3] = (-b2 - e2) * g;
    p[4] = (a0 - a2) * g; p[5] = (a3 - a1) * g;
    p[6] = (b0 - e1) * g; p[7] = (b2 - e2) * g;
#pragma unroll
    for (int i = 0; i < 8; i++) *(u32*)(lds + i * 2048 + t * 4) = pkh(p[i].x, p[i].y);
  }
  __syncthreads();
  const bool cplx = (j >= 1 && j <= 3);
  const int pre = j == 0 ? 0 : (j == 4 ? 1 : 2 * j);
  const u32 lb = (u32)(size_t)lds + pre * 2048 + (8 * q + (l15 >> 2)) * 64 + (l15 & 3) * 8;
  u32 dat[2][4];
#pragma unroll
  for (int ut = 0; ut < 2; ut++) {
    const s4v r0 = tr_read(lb + ut * 32), r1 = tr_read(lb + ut * 32 + 256);
    const h8 ar = __builtin_bit_cast(h8, __builtin_shufflevector(r0, r1, 0, 1, 2, 3, 4, 5, 6, 7));
    f32x4 yr = MF16(ar, Fr, ((f32x4){0, 0, 0, 0})), yi = MF16(ar, Fi, ((f32x4){0, 0, 0, 0}));
    if (cplx) {
      const s4v i0 = tr_read(lb + 2048 + ut * 32), i1 = tr_read(lb + 2048 + ut * 32 + 256);
      const h8 ai = __builtin_bit_cast(h8, __builtin_shufflevector(i0, i1, 0, 1, 2, 3, 4, 5, 6, 7));
      yr = MF16(ai, Fn, yr); yi = MF16(ai, Fr, yi);
    }
#pragma unroll
    for (int e = 0; e < 4; e++) dat[ut][e] = pkh(yr[e] * twr[ut][e] - yi[e] * twi[ut][e], yr[e] * twi[ut][e] + yi[e] * twr[ut][e]);
  }
  const h8 d0 = mk8(dat[0][0], dat[0][1], dat[0][2], dat[0][3]), d1 = mk8(dat[1][0], dat[1][1], dat[1][2], dat[1][3]);
  char* xb = ws + WS_XB;
#pragma unroll
  for (int kt = 0; kt < 2; kt++) {
    if (kt == 1 && !cplx && hf == 1) break;
    const u32 so = kt ? 0x80008000u : 0u;
    const h8 ar0 = mk8(g2r[0], g2r[1] ^ so, g2r[2], g2r[3] ^ so), ai0 = mk8(g2i[0], g2i[1] ^ so, g2i[2], g2i[3] ^ so);
    const h8 ar1 = mk8(g2r[0] ^ sgi, g2r[1] ^ so ^ sgi, g2r[2] ^ sgi, g2r[3] ^ so ^ sgi), ai1 = mk8(g2i[0] ^ sgi, g2i[1] ^ so ^ sgi, g2i[2] ^ sgi, g2i[3] ^ so ^ sgi);
    f32x4 tr = MF16(ar0, d0, ((f32x4){0, 0, 0, 0})), ti = MF16(ai0, d0, ((f32x4){0, 0, 0, 0}));
    tr = MF16(ar1, d1, tr); ti = MF16(ai1, d1, ti);
    const u32x4 o = {pkh(tr[0], ti[0]), pkh(tr[1], ti[1]), pkh(tr[2], ti[2]), pkh(tr[3], ti[3])};
    if (kt == 0 || cplx) {
      const int blk = blk_base(j) + (4 * kt + q) * 8 + (k1i >> 2);
      *(u32x4*)(xb + (size_t)blk * 16384 + s2 * 64 + (k1i & 3) * 16) = o;
    } else if (lane == 0) *(u32x4*)(xb + (size_t)256 * 16384 + s2 * 64 + (j >> 2) * 16) = o;
  }
}

__global__ __launch_bounds__(256) void k2(char* ws) {
  const int blk = blockIdx.x, w = __builtin_amdgcn_readfirstlane(threadIdx.x >> 6), lane = threadIdx.x & 63, q = lane >> 4, l15 = lane & 15;
  if (blk == 256 && w >= 2) return;
  const int j = blk < 192 ? 1 + (blk >> 6) : (blk < 224 ? 0 : (blk < 256 ? 4 : 4 * w));
  const size_t off = (size_t)blk * 16384 + (64 * q + l15) * 64 + w * 16;
  u32x4 L[4], W[4];
#pragma unroll
  for (int i = 0; i < 4; i++) L[i] = *(const u32x4*)(ws + WS_XB + off + i * 1024);
#pragma unroll
  for (int g = 0; g < 4; g++) W[g] = *(const u32x4*)(ws + WS_WL + (size_t)(blk * 4 + w) * 4096 + g * 1024 + lane * 16);
  u32 m1r[4], m1i[4], m2r[4], m2i[4], m3r[4], m3i[4], m4r[4], m4i[4]; float t1r[4], t1i[4], t2r[4], t2i[4];
#pragma unroll
  for (int e = 0; e < 4; e++) {
    const int x = 4 * q + e;
    { const float p = (float)((x * (j + 8 * l15)) & 127) * (1.0f / 128.0f); const float cr = 0.25f * cosr(p), ci = -0.25f * sinr(p); m1r[e] = pkh(cr, -ci); m1i[e] = pkh(ci, cr); }
    { const float p = (float)((x * l15) & 15) * (1.0f / 16.0f); const float cr = 0.25f * cosr(p), ci = -0.25f * sinr(p);
      m2r[e] = pkh(cr, -ci); m2i[e] = pkh(ci, cr); m3r[e] = pkh(cr, ci); m3i[e] = pkh(-ci, cr); }
    { const float p = (float)((l15 * (j + 8 * x)) & 127) * (1.0f / 128.0f); const float cr = 0.25f * cosr(p), ci = 0.25f * sinr(p); m4r[e] = pkh(cr, -ci); m4i[e] = pkh(ci, cr); }
    { const float p = (float)(x * (j + 8 * l15)) * (1.0f / 2048.0f); t1r[e] = cosr(p); t1i[e] = -sinr(p); }
    { const float p = (float)(l15 * (j + 8 * x)) * (1.0f / 2048.0f); t2r[e] = cosr(p); t2i[e] = sinr(p); }
  }
  const h8 M1r = mk8(m1r[0], m1r[1], m1r[2], m1r[3]), M1i = mk8(m1i[0], m1i[1], m1i[2], m1i[3]);
  const h8 M2r = mk8(m2r[0], m2r[1], m2r[2], m2r[3]), M2i = mk8(m2i[0], m2i[1], m2i[2], m2i[3]);
  const h8 M3r = mk8(m3r[0], m3r[1], m3r[2], m3r[3]), M3i = mk8(m3i[0], m3i[1], m3i[2], m3i[3]);
  const h8 M4r = mk8(m4r[0], m4r[1], m4r[2], m4r[3]), M4i = mk8(m4i[0], m4i[1], m4i[2], m4i[3]);
  const f32x4 z = {0, 0, 0, 0};
  u32x4 O[4];
#pragma unroll
  for (int g = 0; g < 4; g++) {
    h8 d = mk8(L[0][g], L[1][g], L[2][g], L[3][g]);
    f32x4 re = MF16(d, M1r, z), im = MF16(d, M1i, z);
    u32 o[4];
#pragma unroll
    for (int e = 0; e < 4; e++) o[e] = pkh(re[e] * t1r[e] - im[e] * t1i[e], re[e] * t1i[e] + im[e] * t1r[e]);
    d = mk8(o[0], o[1], o[2], o[3]);
    re = MF16(M2r, d, z); im = MF16(M2i, d, z);
#pragma unroll
    for (int e = 0; e < 4; e++) { const float a = bflo(W[g][e]), b = bfhi(W[g][e]); o[e] = pkh(re[e] * a - im[e] * b, re[e] * b + im[e] * a); }
    d = mk8(o[0], o[1], o[2], o[3]);
    re = MF16(d, M3r, z); im = MF16(d, M3i, z);
#pragma unroll
    for (int e = 0; e < 4; e++) o[e] = pkh(re[e] * t2r[e] - im[e] * t2i[e], re[e] * t2i[e] + im[e] * t2r[e]);
    d = mk8(o[0], o[1], o[2], o[3]);
    re = MF16(M4r, d, z); im = MF16(M4i, d, z);
#pragma unroll
    for (int e = 0; e < 4; e++) O[e][g] = pkh(re[e], im[e]);
  }
#pragma unroll
  for (int i = 0; i < 4; i++) *(u32x4*)(ws + WS_XC + off + i * 1024) = O[i];
}

constexpr int PL = 32 * 144;
__global__ __launch_bounds__(640) void k3(const u16* x, char* ws) {
  __shared__ __attribute__((aligned(16))) char lds[8 * PL + 16384];
  const int s2 = s2_of(blockIdx.x), t = threadIdx.x, w = __builtin_amdgcn_readfirstlane(t >> 6), lane = t & 63;
  const int j = w >> 1, uh = w & 1, q = lane >> 4, l15 = lane & 15, ui = 16 * uh + l15;
  const bool cplx = (j >= 1 && j <= 3);
  const char* xc = ws + WS_XC;
  u32x4 L[2][2];
#pragma unroll
  for (int kt = 0; kt < 2; kt++)
#pragma unroll
    for (int tl = 0; tl < 2; tl++) {
      const int k1i = 16 * tl + l15;
      if (kt == 0 || cplx) L[tl][kt] = *(const u32x4*)(xc + (size_t)(blk_base(j) + (4 * kt + q) * 8 + (k1i >> 2)) * 16384 + s2 * 64 + (k1i & 3) * 16);
      else { L[tl][kt] = (u32x4){0, 0, 0, 0}; if (tl == 0 && lane == 0) L[0][1] = *(const u32x4*)(xc + (size_t)256 * 16384 + s2 * 64 + (j >> 2) * 16); }
    }
  const float* mod = (const float*)(ws + WS_MOD);
  u32x4 xa, xb2; f32x4 ga[4], sb[4], cb[4];
  if (w < 8) {
    const u16* xr = x + (size_t)(256 * w + s2) * 1024 + lane * 8;
    xa = *(const u32x4*)xr; xb2 = *(const u32x4*)(xr + 512);
#pragma unroll
    for (int i = 0; i < 4; i++) { const int o = (i >> 1) * 512 + lane * 8 + (i & 1) * 4;
      ga[i] = *(const f32x4*)(mod + 2048 + o); sb[i] = *(const f32x4*)(mod + 3072 + o); cb[i] = *(const f32x4*)(mod + 4096 + o); }
  }
  u32 g1r[4], g1i[4], g2r[4], g2i[4];
#pragma unroll
  for (int e = 0; e < 4; e++) {
    { const float p = (float)(((4 * q + e) * ui) & 31) * (1.0f / 32.0f); const float cr = 0.125f * cosr(p), ci = 0.125f * sinr(p); g1r[e] = pkh(cr, -ci); g1i[e] = pkh(ci, cr); }
    { const float p = (float)(((4 * q + e) * l15) & 31) * (1.0f / 32.0f); const float cr = 0.25f * cosr(p), ci = 0.25f * sinr(p); g2r[e] = pkh(cr, -ci); g2i[e] = pkh(ci, cr); }
  }
  float twr[2][4], twi[2][4];
#pragma unroll
  for (int tl = 0; tl < 2; tl++)
#pragma unroll
    for (int e = 0; e < 4; e++) { const float p = (float)((16 * tl + 4 * q + e) * ui) * (1.0f / 1024.0f); twr[tl][e] = cosr(p); twi[tl][e] = sinr(p); }
  const u32 sgi = (l15 & 1) ? 0x80008000u : 0u;
  const f32x4 z = {0, 0, 0, 0};
  h8 d[2];
  { const h8 b0r = mk8(g1r[0], g1r[1], g1r[2], g1r[3]), b0i = mk8(g1i[0], g1i[1], g1i[2], g1i[3]);
    const h8 b1r = mk8(g1r[0] ^ sgi, g1r[1] ^ sgi, g1r[2] ^ sgi, g1r[3] ^ sgi), b1i = mk8(g1i[0] ^ sgi, g1i[1] ^ sgi, g1i[2] ^ sgi, g1i[3] ^ sgi);
#pragma unroll
    for (int tl = 0; tl < 2; tl++) {
      const h8 a0 = __builtin_bit_cast(h8, L[tl][0]), a1 = __builtin_bit_cast(h8, L[tl][1]);
      f32x4 re = MF16(a0, b0r, z), im = MF16(a0, b0i, z);
      re = MF16(a1, b1r, re); im = MF16(a1, b1i, im);
      u32 o[4];
#pragma unroll
      for (int e = 0; e < 4; e++) o[e] = pkh(re[e] * twr[tl][e] - im[e] * twi[tl][e], re[e] * twi[tl][e] + im[e] * twr[tl][e]);
      d[tl] = mk8(o[0], o[1], o[2], o[3]);
    } }
  const int pre = j == 0 ? 0 : (j == 4 ? 1 : 2 * j);
#pragma unroll
  for (int rt = 0; rt < 2; rt++) {
    const u32 so = rt ? 0x80008000u : 0u;
    char* dst = lds + pre * PL + (16 * rt + l15) * 144 + (16 * uh + 4 * q) * 4;
    f32x4 re = MF16(d[0], mk8(g2r[0], g2r[1] ^ so, g2r[2], g2r[3] ^ so), z);
    re = MF16(d[1], mk8(g2r[0] ^ sgi, g2r[1] ^ so ^ sgi, g2r[2] ^ sgi, g2r[3] ^ so ^ sgi), re);
    *(f32x4*)dst = re;
    if (cplx) {
      f32x4 im = MF16(d[0], mk8(g2i[0], g2i[1] ^ so, g2i[2], g2i[3] ^ so), z);
      im = MF16(d[1], mk8(g2i[0] ^ sgi, g2i[1] ^ so ^ sgi, g2i[2] ^ sgi, g2i[3] ^ so ^ sgi), im);
      *(f32x4*)(dst + PL) = im;
    }
  }
  __syncthreads();
  char* yb = lds + 8 * PL;
  if (w < 8) {
    f32x2 p[8];
#pragma unroll
    for (int i = 0; i < 8; i++) p[i] = *(const f32x2*)(lds + i * PL + (t >> 4) * 144 + (t & 15) * 8);
    const f32x2 c0 = p[0] + p[1], c1 = p[0] - p[1];
    const f32x2 a0 = c0 + p[4], a2 = c0 - p[4], a1 = c1 - p[5], a3 = c1 + p[5];
    const f32x2 b0 = p[2] + p[6], b2 = p[7] - p[3];
    const f32x2 e1 = (p[2] - p[6]) * 0.70710678f, e2 = (p[3] + p[7]) * 0.70710678f;
    const f32x2 b1 = e1 - e2, b3 = -e1 - e2;
    const f32x2 y[8] = {a0 + b0, a1 + b1, a2 + b2, a3 + b3, a0 - b0, a1 - b1, a2 - b2, a3 - b3};
#pragma unroll
    for (int i = 0; i < 8; i++) *(u32*)(yb + i * 2048 + t * 4) = pkbf(y[i].x, y[i].y);
  }
  __syncthreads();
  if (w >= 8) return;
  const u32x4 ya = *(const u32x4*)(yb + w * 2048 + lane * 16), yb2 = *(const u32x4*)(yb + w * 2048 + 1024 + lane * 16);
  float v[16]; u32x4 o1[2];
#pragma unroll
  for (int e = 0; e < 8; e++) {
    const u32 xx = e < 4 ? xa[e] : xb2[e - 4], yy = e < 4 ? ya[e] : yb2[e - 4];
    const u32 gy = pkbf(ga[e >> 1][(e & 1) * 2] * bflo(yy), ga[e >> 1][(e & 1) * 2 + 1] * bfhi(yy));
    const u32 r = pkbf(bflo(xx) + bflo(gy), bfhi(xx) + bfhi(gy));
    o1[e >> 2][e & 3] = r; v[2 * e] = bflo(r); v[2 * e + 1] = bfhi(r);
  }
  const size_t ro = (size_t)(8 * s2 + w) * 2048 + lane * 16;
  *(u32x4*)(ws + WS_X1 + ro) = o1[0]; *(u32x4*)(ws + WS_X1 + ro + 1024) = o1[1];
#pragma unroll
  for (int pass = 0; pass < 2; pass++) {
    float s = 0.f;
#pragma unroll
    for (int e = 0; e < 16; e++) s += v[e];
    const float mean = wave_sum(s) * (1.0f / 1024.0f);
    float qq = 0.f;
#pragma unroll
    for (int e = 0; e < 16; e++) { const float dd = v[e] - mean; qq += dd * dd; }
    const float var = rbf(wave_sum(qq) * (1.0f / 1024.0f));
    const float rs = 1.0f / rbf(sqrtf(rbf(var + 1e-5f))), nb = -rbf(mean) * rs;
#pragma unroll
    for (int e = 0; e < 16; e++) {
      v[e] = v[e] * rs + nb;
      if (pass == 0) v[e] = v[e] * rbf(1.0f + cb[e >> 2][e & 3]) + sb[e >> 2][e & 3];
    }
  }
#pragma unroll
  for (int e = 0; e < 8; e++) o1[e >> 2][e & 3] = pkbf(v[2 * e], v[2 * e + 1]);
  *(u32x4*)(ws + WS_A1 + ro) = o1[0]; *(u32x4*)(ws + WS_A1 + ro + 1024) = o1[1];
}

template <int MODE>
__global__ __launch_bounds__(256) void gemm_naive(const u16* A, const u16* B, const u16* bias, int N, int K, char* ws, u16* out) {
  __shared__ float sa[16][65], sbm[16][65];
  const int tx = threadIdx.x & 15, ty = threadIdx.x >> 4, m0 = blockIdx.y * 64, n0 = blockIdx.x * 64;
  float acc[4][4] = {};
  for (int k0 = 0; k0 < K; k0 += 16) {
    for (int i = threadIdx.x; i < 1024; i += 256) { sa[i & 15][i >> 4] = bflo(A[(size_t)(m0 + (i >> 4)) * K + k0 + (i & 15)]); sbm[i >> 6][i & 63] = bflo(B[(size_t)(k0 + (i >> 6)) * N + n0 + (i & 63)]); }
    __syncthreads();
    for (int k = 0; k < 16; k++)
      for (int a = 0; a < 4; a++) for (int b = 0; b < 4; b++) acc[a][b] += sa[k][ty * 4 + a] * sbm[k][tx * 4 + b];
    __syncthreads();
  }
  for (int a = 0; a < 4; a++) for (int b = 0; b < 4; b++) {
    const int m = m0 + ty * 4 + a, n = n0 + tx * 4 + b;
    float v = acc[a][b] + bflo(bias[n]);
    if (MODE == 0) { const float u = 0.7978845608f * (v + 0.044715f * v * v * v); v = v / (1.0f + __expf(-2.0f * u)); out[(size_t)m * N + n] = (u16)(pkbf(v, v) & 0xffff); }
    else { const float gb = ((const float*)(ws + WS_MOD))[5120 + n]; const float x1 = bflo(((const u16*)(ws + WS_X1))[(size_t)m * 1024 + n]);
      v = x1 + rbf(gb * rbf(v)); const int s = 256 * (m & 7) + (m >> 3); out[(size_t)s * 1024 + n] = (u16)(pkbf(v, v) & 0xffff); }
  }
}

extern "C" void kernel_launch(void* const* d_in, const int* in_sizes, int n_in, void* d_out, int out_size, void* d_ws, size_t ws_size, hipStream_t stream) {
  const u16* x = (const u16*)d_in[0]; const u16* wr = (const u16*)d_in[1]; const u16* wi = (const u16*)d_in[2];
  const u16* w1 = (const u16*)d_in[4]; const u16* b1 = (const u16*)d_in[5]; const u16* w2 = (const u16*)d_in[6]; const u16* b2 = (const u16*)d_in[7];
  const u16* c = (const u16*)d_in[8]; const u16* wmod = (const u16*)d_in[9];
  char* ws = (char*)d_ws;
  k0<<<448 + 1026 * 2, 512, 0, stream>>>(x, wr, wi, c, wmod, ws);
  k1<<<256, 640, 0, stream>>>(x, ws);
  k2<<<257, 256, 0, stream>>>(ws);
  k3<<<256, 640, 0, stream>>>(x, ws);
  gemm_naive<0><<<dim3(56, 32), 256, 0, stream>>>((const u16*)(ws + WS_A1), w1, b1, 3584, 1024, ws, (u16*)(ws + WS_H));
  gemm_naive<1><<<dim3(16, 32), 256, 0, stream>>>((const u16*)(ws + WS_H), w2, b2, 1024, 3584, ws, (u16*)d_out);
}
```

```cpp
#define GMAJOR
#define MMAJOR
#include <hip/hip_runtime.h>
#include <hip/hip_bf16.h>
#include <stdint.h>

#define W2SZ (3584 * 2048)
#define HSZ (128 * 114688)
#define ASMINC "mlp_asm.inc"
#define CODEPF 2048
#define NSM (me & 7)
#define MGM ((me >> 3) & 7)
#define SL(lp) ((lp) < 63 ? (lp) % 7 : 6)
#define MG(lp) ((lp) < 63 ? (lp) / 7 : 8)
#define W1SZ (1024 * 7168)
#define CG (me & 63)
#define KS (me >> 6)
#define WSOFF 0
typedef unsigned short u16;
typedef unsigned u32;
typedef _Float16 f16;
typedef f16 h8 __attribute__((ext_vector_type(8)));
typedef f16 h4 __attribute__((ext_vector_type(4)));
typedef f16 h2 __attribute__((ext_vector_type(2)));
typedef short s4v __attribute__((ext_vector_type(4)));
typedef short bf16x8 __attribute__((ext_vector_type(8)));
typedef float f32x4 __attribute__((ext_vector_type(4)));
typedef float f32x2 __attribute__((ext_vector_type(2)));
typedef u32 u32x4 __attribute__((ext_vector_type(4)));
typedef u32 u32x2 __attribute__((ext_vector_type(2)));
typedef __bf16 bf2v __attribute__((ext_vector_type(2)));
#define LAS __attribute__((address_space(3)))
#define DI __device__ __forceinline__
#define MF16(a, b, c) __builtin_amdgcn_mfma_f32_16x16x32_f16((a), (b), (c), 0, 0, 0)

constexpr size_t WS_MOD = 0;
constexpr size_t WS_STAT = 32768;
constexpr size_t WS_XB = 1u << 20;
constexpr size_t WS_XC = 6u << 20;
constexpr size_t WS_WL = 11u << 20;
constexpr size_t WS_A1 = 16u << 20;
constexpr size_t WS_X1 = 20u << 20;
constexpr size_t WS_H = 24u << 20;
constexpr size_t WS_P = 40u << 20;
constexpr size_t WS_END = 64u << 20;
constexpr size_t WS_FLAG = 128u << 10;
constexpr size_t WS_STAMP = 512u << 10;

#define ST_BEGIN
#define ST_MARK(i)
#define ST_WAITV
#define ST_END(k)
DI float bflo(u32 v) { return __uint_as_float(v << 16); }
DI float bfhi(u32 v) { return __uint_as_float(v & 0xffff0000u); }
DI u32 pkbf(float a, float b) { f32x2 v = {a, b}; return __builtin_bit_cast(u32, __builtin_convertvector(v, bf2v)); }
DI float rbf(float a) { return bflo(pkbf(a, a)); }
DI u32 pkh(float a, float b) { f32x2 v = {a, b}; return __builtin_bit_cast(u32, __builtin_convertvector(v, h2)); }
DI float cosr(float rev) { return __builtin_amdgcn_cosf(rev); }
DI float sinr(float rev) { return __builtin_amdgcn_sinf(rev); }
DI h8 mk8(u32 a, u32 b, u32 c, u32 d) { u32x4 v = {a, b, c, d}; return __builtin_bit_cast(h8, v); }
DI float wave_sum(float v) {
#pragma unroll
  for (int o = 32; o > 0; o >>= 1) v += __shfl_xor(v, o);
  return v;
}

typedef int i32x16 __attribute__((ext_vector_type(16)));
typedef int i32x8 __attribute__((ext_vector_type(8)));
typedef int i32x4 __attribute__((ext_vector_type(4)));
typedef int i32x2 __attribute__((ext_vector_type(2)));
#define C8(p, a) p #a "0", p #a "1", p #a "2", p #a "3", p #a "4", p #a "5", p #a "6", p #a "7", p #a "8", p #a "9"
__global__ __launch_bounds__(256, 1) void kmega(const u16* x, const u16* wr, const u16* wi, const u16* cv, const u16* wmod, const u16* w1, const u16* b1, const u16* w2, const u16* b2, char* ws, u16* out) {
  constexpr int OOR = 0x08000000, XBS = 16512;
  i32x16 xr, vin, fin, fs; i32x8 cr, d0, d1, p0, vt; i32x4 pp, am, keep, cnt; i32x2 fx;
  {
    const int b = blockIdx.x, xcd = b & 7, idx = b >> 3, me = xcd * 32 + idx;
    { i32x8 q0; const uint64_t pm = (uint64_t)wmod + CG * 64 + KS * (256 * 12288), pc = (uint64_t)cv + KS * 512;
      q0[0] = (int)pc; q0[1] = (int)(pc >> 32); q0[2] = (int)pm; q0[3] = (int)(pm >> 32);
      q0[4] = (int)(uint64_t)ws; q0[5] = (int)((uint64_t)ws >> 32); const uint64_t px = (uint64_t)x + me * 2048; q0[6] = (int)px; q0[7] = (int)(px >> 32);
      cr[5] = threadIdx.x;
      asm volatile(
"v_readfirstlane_b32 s93, v45\n"
"v_and_b32 v46, 0xc0, v45\n"
"v_bfe_u32 v47, v45, 2, 4\n"
"v_add_u32 v46, v46, v47\n"
"v_mul_u32_u24 v46, 12288, v46\n"
"v_and_b32 v44, 3, v45\n"
"v_lshl_add_u32 v46, v44, 4, v46\n"
"s_lshl_b32 s93, s93, 6\n"
"s_add_u32 m0, s93, 65536\n"
"v_lshlrev_b32 v47, 1, v45\n"
"global_load_ushort v41, v47, s[0:1]\n"
"v_mov_b32 v47, 65536\n"
"global_load_dword v44, v47, s[4:5] sc1\n"
"global_load_lds_dwordx4 v46, s[2:3] offset:0\n"
"s_add_u32 s2, s2, 195584\n"
"s_addc_u32 s3, s3, 0\n"
"global_load_lds_dwordx4 v46, s[2:3] offset:1024\n"
"s_add_u32 s2, s2, 195584\n"
"s_addc_u32 s3, s3, 0\n"
"global_load_lds_dwordx4 v46, s[2:3] offset:2048\n"
"s_add_u32 s2, s2, 195584\n"
"s_addc_u32 s3, s3, 0\n"
"global_load_lds_dwordx4 v46, s[2:3] offset:3072\n"
          : "={v[16:31]}"(xr), "+{v[40:47]}"(cr), "+{s[0:7]}"(q0) : : "memory", "scc", "s92", "s93", "s94"); }
    const int t = threadIdx.x, w = __builtin_amdgcn_readfirstlane(t >> 6), lane = t & 63, q = lane >> 4, m = lane & 15;
    const int kq = xcd >> 1, lp = me & 63, act = lp < 63, j = 7 * kq + SL(lp);
    const int wtr = (8 * q + (m >> 2)) * 256 + ((w ^ (m >> 2)) * 64) + (m & 3) * 16;
    const int fj = me < 192 ? 1 + (me >> 6) : ((me >> 5) & 1) * 4, mir = me < 192 ? (me >> 5) & 1 : 0;
    const int ks0 = fj + 16 * w + 64 * (me & 31);
    vin[1] = lane * 16; vin[2] = mir ? ((2048 - ks0) * 513 + 505 - 8 * lane) * 2 : (ks0 * 513 + 8 * lane) * 2; vin[3] = lane < 32 ? (idx * 16 + 4 * w + (lane >> 3)) * 2048 + (lane & 7) * 128 : (lane < 56 ? (512 + idx * 12 + 3 * w + ((lane - 32) >> 3)) * 2048 + (lane & 7) * 128 : OOR);
    vin[4] = (me == 0 && t < 16) ? (int)WS_FLAG + t * 256 : OOR; vin[6] = (8 * q + (m >> 3)) * 16384 + (m & 7) * 16;
    vin[7] = wtr; vin[8] = wtr + 65536; vin[9] = wtr + 131072; vin[10] = q * 7168 + ((m ^ (q << 2)) * 16);
    vin[11] = (me < 192 && !mir) ? (2048 - ks0) * 1026 : ks0 * 1026 + 1024;
    vin[14] = 20480 + (8 * q + 2 * (lane & 3)) * 528 + w * 32 + (m >> 2) * 4; vin[15] = (8 * q + (m >> 3)) * 64 + (m & 7) * 8;
    const int csw = ((lane & 3) ^ q) * 16;
    fin[0] = t * 8; fin[1] = t * 16; fin[2] = (8 * q + (m >> 2)) * 64 + (m & 3) * 8 + 4096 + (w >> 1) * 8192; fin[3] = q * 512 + m * 16;
    fin[4] = (lane >> 2) * XBS + csw; fin[5] = (64 * q + m) * 64 + ((w ^ (m >> 2)) * 16); fin[6] = (lane >> 2) * 64 + csw; fin[7] = (lane >> 2) * 16384 + (lane & 3) * 16;
    fin[8] = (lane & 7) == 7 ? OOR : ((lane >> 5) * 512 + ((lane >> 3) & 3)) * 7168 + (lane & 7) * 128; fin[9] = m * 144 + q * 16 + (w & 1) * 64; fin[10] = (t >> 3) * 144 + (t & 7) * 16;
    fin[11] = (lane >> 3) * 2048 + (((lane & 7) ^ (4 * (me & 1) + w)) * 16); fin[12] = t == 128 ? 0x3f800000 : 0;
    fin[13] = __float_as_int((float)m); fin[14] = __float_as_int((float)(4 * q)); fin[15] = q == 0 ? ((m & 1) ? (int)0xbf800000 : 0x3f800000) : 0;
    fs[0] = (int)WS_WL + (me & ~31) * 16384 + ((me & 31) ^ mir) * 512 + (w ^ (3 * mir)) * 128; fs[1] = 8208 - mir * 16416;
    fs[2] = (int)WS_XB + me * XBS + (w >> 1) * 8192 + (w & 1) * 256; fs[3] = fs[2] + 4096;
    fs[5] = (int)WS_XB + me * 64 + w * 64 * XBS; fs[6] = -mir; fs[7] = (int)WS_XC + me * 16384 + w * 4096; fs[8] = (int)WS_XC + me * 64 + (w << 20);
    fs[9] = fj; fs[10] = (((me & ~32) + w * 1000) == 192) ? ((me >> 5) & 1) * 8 : -1;
    fs[11] = (int)WS_X1 + me * 16384 + w * 4096; fs[12] = (int)WS_A1 + (me >> 1) * 32768 + (8 * (me & 1) + 2 * w) * 128; fs[13] = w; fs[14] = 4096 - (w >> 1) * 16384;
    const uint64_t pm = (uint64_t)wmod + 4096 + CG * 128 + KS * (256 * 12288), pc = (uint64_t)cv + KS * 512, px = (uint64_t)x + me * 2048;
    p0[0] = (int)pc; p0[1] = (int)(pc >> 32); p0[2] = (int)pm; p0[3] = (int)(pm >> 32);
    p0[4] = (int)(uint64_t)wr; p0[5] = (int)((uint64_t)wr >> 32); p0[6] = (int)(uint64_t)wi; p0[7] = (int)((uint64_t)wi >> 32);
    am[0] = (int)px; am[1] = (int)(px >> 32); am[2] = (192 << 10) + KS * 24576 + 8192 + CG * 256; am[3] = (192 << 10) + KS * 24576 + CG * 128;
    fx[0] = (me < 192 && !mir) ? -8208 : 8208; fx[1] = me * 64;
    i32x16 s0; i32x8 s1;
    s0[2] = j * 256 + 16 * w * 7168; s0[4] = w * 4096;
    s1[3] = (idx * 16 + 4 * w) * 7168 + (kq * 896 + (xcd & 1) * 448) * 2; s1[7] = idx * CODEPF; s1[4] = kq * 896 * 2048 + (xcd & 1) * 1024;
    const uint64_t pw = (uint64_t)ws, p1 = (uint64_t)w1, p2 = (uint64_t)w2, po = (uint64_t)out;
    d0[0] = (int)pw; d0[1] = (int)(pw >> 32); d0[2] = (int)WS_END; d0[3] = 0x20000;
    d0[4] = (int)p1; d0[5] = (int)(p1 >> 32); d0[6] = W1SZ; d0[7] = 0x20000;
    d1[0] = (int)p2; d1[1] = (int)(p2 >> 32); d1[2] = 3584 * 2048; d1[3] = 0x20000; d1[4] = (int)po; d1[5] = (int)(po >> 32);
    pp[0] = (int)(uint64_t)b1; pp[1] = (int)((uint64_t)b1 >> 32); pp[2] = (int)(uint64_t)b2; pp[3] = (int)((uint64_t)b2 >> 32);
    asm volatile(
"s_mov_b32 s94, 0\n"
"v_lshrrev_b32 v46, 6, v45\n"
"v_and_b32 v47, 63, v45\n"
"v_lshlrev_b32 v46, 20, v46\n"
"v_lshl_add_u32 v46, v47, 4, v46\n"
"global_load_dwordx4 v[16:19], v46, s[96:97] offset:0\n"
"global_load_dwordx4 v[20:23], v46, s[96:97] offset:1024\n"
"s_add_u32 s96, s96, 524288\n"
"s_addc_u32 s97, s97, 0\n"
"global_load_dwordx4 v[24:27], v46, s[96:97] offset:0\n"
"global_load_dwordx4 v[28:31], v46, s[96:97] offset:1024\n"
"s_setreg_imm32_b32 hwreg(HW_REG_MODE, 23, 1), 1\n"
"s_mov_b32 s24, 0x80008000\n"
"s_mov_b32 s25, 0xffff0000\n"
"s_mov_b32 s26, 0x3f3504f3\n"
"s_mov_b32 s27, 0x3f3504f3\n"
"v_lshrrev_b32 v245, 4, v97\n"
"v_and_b32 v238, 0xc0, v245\n"
"v_bfe_u32 v245, v245, 3, 3\n"
"v_add_u32 v245, v245, v238\n"
"v_mul_u32_u24 v245, 12288, v245\n"
"v_and_b32 v238, 0x70, v113\n"
"v_add_u32 v245, v245, v238\n"
"s_lshl_b32 s39, s44, 1\n"
"v_lshrrev_b32 v248, 9, v97\n"
"v_mul_u32_u24 v248, 12288, v248\n"
"v_and_b32 v238, 0x1f0, v113\n"
"v_lshl_add_u32 v248, v238, 3, v248\n"
"s_and_b32 s28, s101, 0x7c0\n"
"s_mul_i32 s28, s28, 1534\n"
"v_add_u32 v248, s28, v248\n"
"s_and_b32 s28, s21, 1\n"
"s_mul_i32 s28, s28, 0x41800000\n"
"v_add_f32 v243, s28, v109\n"
"v_lshlrev_b32 v244, 27, v113\n"
"v_ashrrev_i32 v244, 31, v244\n"
"v_and_b32 v244, s24, v244\n"
"v_mul_f32 v239, 0x3d000000, v243\n"
"v_mul_f32 v238, v110, v239\n"
"v_add_f32 v238, v238, v238\n"
"v_cos_f32 v192, v238\n"
"v_sin_f32 v200, v238\n"
"v_add_f32 v240, v238, v239\n"
"v_cos_f32 v193, v240\n"
"v_sin_f32 v201, v240\n"
"v_add_f32 v240, v240, v239\n"
"v_cos_f32 v194, v240\n"
"v_sin_f32 v202, v240\n"
"v_add_f32 v240, v240, v239\n"
"v_cos_f32 v195, v240\n"
"v_sin_f32 v203, v240\n"
"v_add_f32 v240, v240, v239\n"
"v_cos_f32 v196, v240\n"
"v_sin_f32 v204, v240\n"
"v_add_f32 v240, v240, v239\n"
"v_cos_f32 v197, v240\n"
"v_sin_f32 v205, v240\n"
"v_add_f32 v240, v240, v239\n"
"v_cos_f32 v198, v240\n"
"v_sin_f32 v206, v240\n"
"v_add_f32 v240, v240, v239\n"
"v_cos_f32 v199, v240\n"
"v_sin_f32 v207, v240\n"
"v_cvt_pk_f16_f32 v220, v192, v193\n"
"v_cvt_pk_f16_f32 v228, v200, v201\n"
"v_xor_b32 v224, s24, v228\n"
"v_cvt_pk_f16_f32 v221, v194, v195\n"
"v_cvt_pk_f16_f32 v229, v202, v203\n"
"v_xor_b32 v225, s24, v229\n"
"v_cvt_pk_f16_f32 v222, v196, v197\n"
"v_cvt_pk_f16_f32 v230, v204, v205\n"
"v_xor_b32 v226, s24, v230\n"
"v_cvt_pk_f16_f32 v223, v198, v199\n"
"v_cvt_pk_f16_f32 v231, v206, v207\n"
"v_xor_b32 v227, s24, v231\n"
"s_waitcnt vmcnt(9)\n"
"v_lshlrev_b32 v40, 16, v41\n"
"v_mul_f32 v41, 0xbfb8aa3b, v40\n"
"v_exp_f32 v41, v41\n"
"v_lshrrev_b32 v238, 2, v96\n"
"v_add_f32 v41, 1.0, v41\n"
"v_rcp_f32 v41, v41\n"
"v_lshrrev_b32 v249, 4, v113\n"
"v_mul_f32 v40, v40, v41\n"
"v_cvt_pk_bf16_f32 v40, v40, v40\n"
"ds_write_b16 v238, v40 offset:19456\n"
"v_and_b32 v249, 0x30, v249\n"
"s_lshr_b32 s28, s44, 5\n"
"v_add_u32 v238, s28, v249\n"
"s_lshr_b32 s28, s44, 4\n"
"v_add_u32 v249, s28, v249\n"
"s_waitcnt lgkmcnt(0)\n"
"ds_read_b128 v[208:211], v238 offset:19456\n"
"ds_read_b128 v[212:215], v238 offset:19520\n"
"v_and_b32 v144, 0x7ff, v98\n"
"v_and_b32 v251, 0x7c0, v98\n"
"v_add_u32 v251, v251, v144\n"
"s_add_u32 s28, s44, 65536\n"
"v_add_u32 v144, s28, v144\n"
"s_add_u32 s28, s39, 81920\n"
"v_add_u32 v251, s28, v251\n"
"s_waitcnt vmcnt(8)\n"
"v_add_u32 v117, 1, v44\n"
"s_nop 0\n"
"v_readfirstlane_b32 s28, v117\n"
"s_nop 0\n"
"s_and_b32 s38, s28, 0xff\n"
"s_and_b32 s28, s28, 3\n"
"s_mul_i32 s37, s28, 0x8001\n"
"s_and_b32 s37, s37, 0x10001\n"
"v_and_b32 v250, 0xf0, v113\n"
"v_mov_b32 v236, 0xfffefffe\n"
"v_mov_b32 v247, 0xffffff00\n"
"v_mov_b32 v237, 0xffff\n"
"v_cmp_gt_u32 vcc, 0x80, v97\n"
"v_mov_b32 v240, 0x8000000\n"
"s_nop 0\n"
"v_cndmask_b32 v239, v240, v97, vcc\n"
"v_cmp_gt_u32 vcc, 0x100, v97\n"
"v_add_u32 v241, s44, v113\n"
"s_nop 0\n"
"v_cndmask_b32 v246, v240, v97, vcc\n"
"s_waitcnt vmcnt(4)\n"
"ds_read_b64_tr_b16 v[128:129], v144 offset:0\n"
"ds_read_b64_tr_b16 v[130:131], v144 offset:256\n"
"ds_read_b64_tr_b16 v[132:133], v144 offset:32\n"
"ds_read_b64_tr_b16 v[134:135], v144 offset:288\n"
"ds_read_b64_tr_b16 v[136:137], v144 offset:2048\n"
"ds_read_b64_tr_b16 v[138:139], v144 offset:2304\n"
"ds_read_b64_tr_b16 v[140:141], v144 offset:2080\n"
"ds_read_b64_tr_b16 v[142:143], v144 offset:2336\n"
"s_waitcnt lgkmcnt(6)\n"
"v_mfma_f32_16x16x32_bf16 v[128:131], v[128:131], v[208:211], 0\n"
"s_waitcnt lgkmcnt(4)\n"
"v_mfma_f32_16x16x32_bf16 v[132:135], v[132:135], v[208:211], 0\n"
"s_waitcnt lgkmcnt(2)\n"
"v_mfma_f32_16x16x32_bf16 v[128:131], v[136:139], v[212:215], v[128:131]\n"
"s_waitcnt lgkmcnt(0)\n"
"v_mfma_f32_16x16x32_bf16 v[132:135], v[140:143], v[212:215], v[132:135]\n"
"s_mov_b32 exec_lo, 0x00010001\n"
"s_mov_b32 exec_hi, 0x00010001\n"
"s_nop 3\n"
"ds_write_b128 v249, v[128:131] offset:18432\n"
"s_nop 0\n"
"ds_write_b128 v249, v[132:135] offset:18496\n"
"s_mov_b64 exec, -1\n"
"s_waitcnt lgkmcnt(0)\n"
"s_barrier\n"
"ds_read_b128 v[0:3], v250 offset:18432\n"
"ds_read_b128 v[4:7], v250 offset:18688\n"
"ds_read_b128 v[8:11], v250 offset:18944\n"
"ds_read_b128 v[12:15], v250 offset:19200\n"
"s_waitcnt lgkmcnt(2)\n"
"v_pk_add_f32 v[0:1], v[0:1], v[4:5]\n"
"s_waitcnt lgkmcnt(0)\n"
"v_pk_add_f32 v[8:9], v[8:9], v[12:13]\n"
"v_pk_add_f32 v[2:3], v[2:3], v[6:7]\n"
"v_pk_add_f32 v[10:11], v[10:11], v[14:15]\n"
"v_pk_add_f32 v[0:1], v[0:1], v[8:9]\n"
"v_pk_add_f32 v[2:3], v[2:3], v[10:11]\n"
"v_and_or_b32 v0, v0, v247, s38\n"
"v_and_or_b32 v1, v1, v247, s38\n"
"v_and_or_b32 v2, v2, v247, s38\n"
"v_and_or_b32 v3, v3, v247, s38\n"
"buffer_store_dwordx4 v[0:3], v239, s[72:75], s99 offen sc1\n"
"global_load_dwordx4 v[80:83], v114, s[4:5]\n"
"global_load_dwordx4 v[84:87], v114, s[6:7]\n"
"v_add_u32 v114, s9, v114\n"
"v_mul_f32 v239, 0x3a800000, v243\n"
"v_mul_f32 v238, v110, v239\n"
"v_cos_f32 v192, v238\n"
"v_sin_f32 v200, v238\n"
"v_add_f32 v240, v238, v239\n"
"v_cos_f32 v193, v240\n"
"v_sin_f32 v201, v240\n"
"v_add_f32 v240, v240, v239\n"
"v_cos_f32 v194, v240\n"
"v_sin_f32 v202, v240\n"
"global_load_dwordx4 v[88:91], v114, s[4:5]\n"
"global_load_dwordx4 v[92:95], v114, s[6:7]\n"
"v_add_u32 v114, s9, v114\n"
"v_add_f32 v240, v240, v239\n"
"v_cos_f32 v195, v240\n"
"v_sin_f32 v203, v240\n"
"v_fmamk_f32 v238, v239, 0x41800000, v238\n"
"v_cos_f32 v196, v238\n"
"v_sin_f32 v204, v238\n"
"v_add_f32 v240, v238, v239\n"
"v_cos_f32 v197, v240\n"
"global_load_ushort v216, v123, s[4:5]\n"
"global_load_ushort v217, v123, s[6:7]\n"
"v_add_u32 v123, s100, v123\n"
"global_load_ushort v218, v123, s[4:5]\n"
"global_load_ushort v219, v123, s[6:7]\n"
"v_add_u32 v123, s100, v123\n"
"v_sin_f32 v205, v240\n"
"v_add_f32 v240, v240, v239\n"
"v_cos_f32 v198, v240\n"
"v_sin_f32 v206, v240\n"
"v_add_f32 v240, v240, v239\n"
"v_cos_f32 v199, v240\n"
"v_sin_f32 v207, v240\n"
"v_mul_f32 v239, 0x3d000000, v109\n"
"v_mul_f32 v238, v110, v239\n"
"s_add_u32 m0, s39, 81920\n"
"s_nop 0\n"
"global_load_lds_dwordx4 v245, s[2:3] offset:0\n"
"s_add_u32 s2, s2, 97280\n"
"s_addc_u32 s3, s3, 0\n"
"v_cos_f32 v0, v238\n"
"v_sin_f32 v4, v238\n"
"v_add_f32 v8, v238, v239\n"
"v_cos_f32 v1, v8\n"
"v_sin_f32 v5, v8\n"
"v_add_f32 v8, v8, v239\n"
"v_cos_f32 v2, v8\n"
"v_sin_f32 v6, v8\n"
"v_add_f32 v8, v8, v239\n"
"global_load_lds_dwordx4 v245, s[2:3] offset:1024\n"
"s_add_u32 s2, s2, 97280\n"
"s_addc_u32 s3, s3, 0\n"
"v_cos_f32 v3, v8\n"
"v_sin_f32 v7, v8\n"
"v_cvt_pk_f16_f32 v48, v0, v4\n"
"v_cvt_pk_f16_f32 v52, -v4, v0\n"
"v_cvt_pk_f16_f32 v49, v1, v5\n"
"v_cvt_pk_f16_f32 v53, -v5, v1\n"
"v_cvt_pk_f16_f32 v50, v2, v6\n"
"v_cvt_pk_f16_f32 v54, -v6, v2\n"
"v_cvt_pk_f16_f32 v51, v3, v7\n"
"v_cvt_pk_f16_f32 v55, -v7, v3\n"
"global_load_lds_dwordx4 v245, s[2:3] offset:2048\n"
"s_add_u32 s2, s2, 97280\n"
"s_addc_u32 s3, s3, 0\n"
"v_xor_b32 v56, v244, v48\n"
"v_xor_b32 v57, v244, v49\n"
"v_xor_b32 v58, v244, v50\n"
"v_xor_b32 v59, v244, v51\n"
"v_xor_b32 v60, v244, v52\n"
"v_xor_b32 v61, v244, v53\n"
"v_xor_b32 v62, v244, v54\n"
"v_xor_b32 v63, v244, v55\n"
"v_mov_b32 v64, v48\n"
"v_xor_b32 v65, s24, v49\n"
"v_mov_b32 v66, v50\n"
"v_xor_b32 v67, s24, v51\n"
"global_load_lds_dwordx4 v245, s[2:3] offset:3072\n"
"s_add_u32 s2, s2, 101376\n"
"s_addc_u32 s3, s3, 0\n"
"v_mov_b32 v68, v52\n"
"v_xor_b32 v69, s24, v53\n"
"v_mov_b32 v70, v54\n"
"v_xor_b32 v71, s24, v55\n"
"v_xor_b32 v72, v244, v48\n"
"v_bitop3_b32 v73, v49, v244, s24 bitop3:0x96\n"
"v_xor_b32 v74, v244, v50\n"
"v_bitop3_b32 v75, v51, v244, s24 bitop3:0x96\n"
"v_xor_b32 v76, v244, v52\n"
"v_bitop3_b32 v77, v53, v244, s24 bitop3:0x96\n"
"v_xor_b32 v78, v244, v54\n"
"v_bitop3_b32 v79, v55, v244, s24 bitop3:0x96\n"
"v_add_u32 v123, 27648, v122\n"
"v_add_u32 v124, 55296, v122\n"
"v_add_u32 v125, 82944, v122\n"
"s_getpc_b64 s[28:29]\n"
"Lpc_K:\n"
"s_min_u32 s63, s63, Lend_K-Lpc_K-2048\n"
"s_add_u32 s28, s28, s63\n"
"s_addc_u32 s29, s29, 0\n"
"global_load_dwordx2 a[0:1], v96, s[28:29]\n"
"s_add_u32 m0, s39, 86016\n"
"s_nop 0\n"
"global_load_lds_dwordx4 v245, s[2:3] offset:0\n"
"s_add_u32 s2, s2, 97280\n"
"s_addc_u32 s3, s3, 0\n"
"s_waitcnt vmcnt(18)\n"
"ds_write_b128 v241, v[16:19] offset:49152\n"
"s_waitcnt vmcnt(17)\n"
"ds_write_b128 v241, v[20:23] offset:50176\n"
"s_waitcnt vmcnt(16)\n"
"ds_write_b128 v241, v[24:27] offset:51200\n"
"s_waitcnt vmcnt(15)\n"
"ds_write_b128 v241, v[28:31] offset:52224\n"
"global_load_lds_dwordx4 v245, s[2:3] offset:1024\n"
"s_add_u32 s2, s2, 97280\n"
"s_addc_u32 s3, s3, 0\n"
"v_mov_b32 v239, 0x3f803f80\n"
"v_mov_b32 v32, 0\n"
"v_mov_b32 v33, 0\n"
"v_mov_b32 v34, 0\n"
"v_mov_b32 v35, 0\n"
"v_dot2c_f32_bf16 v32, v16, v239\n"
"v_dot2c_f32_bf16 v34, v16, v16\n"
"v_dot2c_f32_bf16 v33, v24, v239\n"
"v_dot2c_f32_bf16 v35, v24, v24\n"
"v_dot2c_f32_bf16 v32, v17, v239\n"
"v_dot2c_f32_bf16 v34, v17, v17\n"
"v_dot2c_f32_bf16 v33, v25, v239\n"
"v_dot2c_f32_bf16 v35, v25, v25\n"
"v_dot2c_f32_bf16 v32, v18, v239\n"
"v_dot2c_f32_bf16 v34, v18, v18\n"
"v_dot2c_f32_bf16 v33, v26, v239\n"
"v_dot2c_f32_bf16 v35, v26, v26\n"
"v_dot2c_f32_bf16 v32, v19, v239\n"
"v_dot2c_f32_bf16 v34, v19, v19\n"
"v_dot2c_f32_bf16 v33, v27, v239\n"
"v_dot2c_f32_bf16 v35, v27, v27\n"
"global_load_lds_dwordx4 v245, s[2:3] offset:2048\n"
"s_add_u32 s2, s2, 97280\n"
"s_addc_u32 s3, s3, 0\n"
"v_dot2c_f32_bf16 v32, v20, v239\n"
"v_dot2c_f32_bf16 v34, v20, v20\n"
"v_dot2c_f32_bf16 v33, v28, v239\n"
"v_dot2c_f32_bf16 v35, v28, v28\n"
"v_dot2c_f32_bf16 v32, v21, v239\n"
"v_dot2c_f32_bf16 v34, v21, v21\n"
"v_dot2c_f32_bf16 v33, v29, v239\n"
"v_dot2c_f32_bf16 v35, v29, v29\n"
"v_dot2c_f32_bf16 v32, v22, v239\n"
"v_dot2c_f32_bf16 v34, v22, v22\n"
"v_dot2c_f32_bf16 v33, v30, v239\n"
"v_dot2c_f32_bf16 v35, v30, v30\n"
"v_dot2c_f32_bf16 v32, v23, v239\n"
"v_dot2c_f32_bf16 v34, v23, v23\n"
"v_dot2c_f32_bf16 v33, v31, v239\n"
"v_dot2c_f32_bf16 v35, v31, v31\n"
"v_lshrrev_b32 v238, 6, v97\n"
"s_nop 0\n"
"s_nop 0\n"
"v_permlane32_swap_b32 v32, v33\n"
"v_permlane32_swap_b32 v34, v35\n"
"v_add_f32 v32, v32, v33\n"
"global_load_lds_dwordx4 v245, s[2:3] offset:3072\n"
"s_add_u32 s2, s2, 97280\n"
"s_addc_u32 s3, s3, 0\n"
"v_add_f32 v34, v34, v35\n"
"s_nop 1\n"
"v_permlane16_swap_b32 v32, v34\n"
"v_add_f32 v32, v32, v34\n"
"s_nop 1\n"
"v_add_f32_dpp v32, v32, v32 quad_perm:[1,0,3,2] row_mask:0xf bank_mask:0xf\n"
"s_nop 1\n"
"v_add_f32_dpp v32, v32, v32 quad_perm:[2,3,0,1] row_mask:0xf bank_mask:0xf\n"
"s_nop 1\n"
"v_add_f32_dpp v32, v32, v32 row_half_mirror row_mask:0xf bank_mask:0xf\n"
"s_nop 1\n"
"v_add_f32_dpp v32, v32, v32 row_mirror row_mask:0xf bank_mask:0xf\n"
"v_mul_f32 v32, 0x3a800000, v32\n"
"v_mov_b32 v33, v32\n"
"s_nop 1\n"
"v_permlane16_swap_b32 v32, v33\n"
"v_fma_f32 v33, -v32, v32, v33\n"
"v_max_f32 v33, 0, v33\n"
"v_add_f32 v33, 0x3727c5ac, v33\n"
"v_rsq_f32 v33, v33\n"
"s_nop 0\n"
"v_mul_f32_e64 v32, -v32, v33\n"
"s_mov_b32 exec_lo, 1\n"
"s_mov_b32 exec_hi, 1\n"
"ds_write_b64 v238, v[32:33] offset:16896\n"
"s_mov_b64 exec, -1\n"
"v_mov_b32 v240, 0\n"
"buffer_store_dword v240, v116, s[72:75], 0 offen sc1\n"
"s_waitcnt lgkmcnt(0)\n"
"s_barrier\n"
"v_mov_b32 v239, 0\n"
"ds_read_b64 v[160:161], v96 offset:49152\n"
"ds_read_b64 v[162:163], v96 offset:51200\n"
"ds_read_b64 v[164:165], v96 offset:53248\n"
"ds_read_b64 v[166:167], v96 offset:55296\n"
"ds_read_b64 v[168:169], v96 offset:57344\n"
"ds_read_b64 v[170:171], v96 offset:59392\n"
"ds_read_b64 v[172:173], v96 offset:61440\n"
"ds_read_b64 v[174:175], v96 offset:63488\n"
"ds_read_b128 v[32:35], v239 offset:16896\n"
"ds_read_b128 v[36:39], v239 offset:16912\n"
"ds_read_b128 v[40:43], v239 offset:16928\n"
"ds_read_b128 v[44:47], v239 offset:16944\n"
"s_mov_b32 s36, 196608\n"
"buffer_load_dwordx4 v[128:131], v97, s[72:75], s36 offen sc1\n"
"s_waitcnt lgkmcnt(11)\n"
"v_lshlrev_b32 v0, 16, v160\n"
"v_and_b32 v1, s25, v160\n"
"v_lshlrev_b32 v2, 16, v161\n"
"v_and_b32 v3, s25, v161\n"
"s_mov_b32 s36, 200704\n"
"buffer_load_dwordx4 v[132:135], v97, s[72:75], s36 offen sc1\n"
"s_waitcnt lgkmcnt(10)\n"
"v_lshlrev_b32 v4, 16, v162\n"
"v_and_b32 v5, s25, v162\n"
"v_lshlrev_b32 v6, 16, v163\n"
"s_mov_b32 s36, 221184\n"
"buffer_load_dwordx4 v[136:139], v97, s[72:75], s36 offen sc1\n"
"v_and_b32 v7, s25, v163\n"
"s_waitcnt lgkmcnt(9)\n"
"v_lshlrev_b32 v8, 16, v164\n"
"v_and_b32 v9, s25, v164\n"
"s_mov_b32 s36, 225280\n"
"buffer_load_dwordx4 v[140:143], v97, s[72:75], s36 offen sc1\n"
"v_lshlrev_b32 v10, 16, v165\n"
"v_and_b32 v11, s25, v165\n"
"s_waitcnt lgkmcnt(8)\n"
"v_lshlrev_b32 v12, 16, v166\n"
"v_and_b32 v13, s25, v166\n"
"s_mov_b32 s36, 245760\n"
"buffer_load_dwordx4 v[144:147], v97, s[72:75], s36 offen sc1\n"
"v_lshlrev_b32 v14, 16, v167\n"
"v_and_b32 v15, s25, v167\n"
"s_waitcnt lgkmcnt(7)\n"
"v_lshlrev_b32 v16, 16, v168\n"
"s_mov_b32 s36, 249856\n"
"buffer_load_dwordx4 v[148:151], v97, s[72:75], s36 offen sc1\n"
"v_and_b32 v17, s25, v168\n"
"v_lshlrev_b32 v18, 16, v169\n"
"v_and_b32 v19, s25, v169\n"
"s_mov_b32 s36, 270336\n"
"buffer_load_dwordx4 v[152:155], v97, s[72:75], s36 offen sc1\n"
"s_waitcnt lgkmcnt(6)\n"
"v_lshlrev_b32 v20, 16, v170\n"
"v_and_b32 v21, s25, v170\n"
"v_lshlrev_b32 v22, 16, v171\n"
"v_and_b32 v23, s25, v171\n"
"s_mov_b32 s36, 274432\n"
"buffer_load_dwordx4 v[156:159], v97, s[72:75], s36 offen sc1\n"
"s_waitcnt lgkmcnt(5)\n"
"v_lshlrev_b32 v24, 16, v172\n"
"v_and_b32 v25, s25, v172\n"
"v_lshlrev_b32 v26, 16, v173\n"
"v_and_b32 v27, s25, v173\n"
"s_waitcnt lgkmcnt(4)\n"
"v_lshlrev_b32 v28, 16, v174\n"
"v_and_b32 v29, s25, v174\n"
"v_lshlrev_b32 v30, 16, v175\n"
"v_and_b32 v31, s25, v175\n"
"s_waitcnt lgkmcnt(3)\n"
"v_pk_fma_f32 v[0:1], v[0:1], v[32:33], v[32:33] op_sel:[0,1,0] op_sel_hi:[1,1,0]\n"
"v_pk_fma_f32 v[2:3], v[2:3], v[32:33], v[32:33] op_sel:[0,1,0] op_sel_hi:[1,1,0]\n"
"v_pk_fma_f32 v[4:5], v[4:5], v[34:35], v[34:35] op_sel:[0,1,0] op_sel_hi:[1,1,0]\n"
"v_pk_fma_f32 v[6:7], v[6:7], v[34:35], v[34:35] op_sel:[0,1,0] op_sel_hi:[1,1,0]\n"
"s_waitcnt lgkmcnt(2)\n"
"v_pk_fma_f32 v[8:9], v[8:9], v[36:37], v[36:37] op_sel:[0,1,0] op_sel_hi:[1,1,0]\n"
"v_pk_fma_f32 v[10:11], v[10:11], v[36:37], v[36:37] op_sel:[0,1,0] op_sel_hi:[1,1,0]\n"
"v_pk_fma_f32 v[12:13], v[12:13], v[38:39], v[38:39] op_sel:[0,1,0] op_sel_hi:[1,1,0]\n"
"v_pk_fma_f32 v[14:15], v[14:15], v[38:39], v[38:39] op_sel:[0,1,0] op_sel_hi:[1,1,0]\n"
"s_waitcnt lgkmcnt(1)\n"
"v_pk_fma_f32 v[16:17], v[16:17], v[40:41], v[40:41] op_sel:[0,1,0] op_sel_hi:[1,1,0]\n"
"v_pk_fma_f32 v[18:19], v[18:19], v[40:41], v[40:41] op_sel:[0,1,0] op_sel_hi:[1,1,0]\n"
"v_pk_fma_f32 v[20:21], v[20:21], v[42:43], v[42:43] op_sel:[0,1,0] op_sel_hi:[1,1,0]\n"
"v_pk_fma_f32 v[22:23], v[22:23], v[42:43], v[42:43] op_sel:[0,1,0] op_sel_hi:[1,1,0]\n"
"s_waitcnt lgkmcnt(0)\n"
"v_pk_fma_f32 v[24:25], v[24:25], v[44:45], v[44:45] op_sel:[0,1,0] op_sel_hi:[1,1,0]\n"
"v_pk_fma_f32 v[26:27], v[26:27], v[44:45], v[44:45] op_sel:[0,1,0] op_sel_hi:[1,1,0]\n"
"v_pk_fma_f32 v[28:29], v[28:29], v[46:47], v[46:47] op_sel:[0,1,0] op_sel_hi:[1,1,0]\n"
"v_pk_fma_f32 v[30:31], v[30:31], v[46:47], v[46:47] op_sel:[0,1,0] op_sel_hi:[1,1,0]\n"
"v_pk_add_f32 v[32:33], v[0:1], v[16:17]\n"
"v_pk_add_f32 v[34:35], v[2:3], v[18:19]\n"
"v_pk_add_f32 v[36:37], v[4:5], v[20:21]\n"
"v_pk_add_f32 v[38:39], v[6:7], v[22:23]\n"
"v_pk_add_f32 v[40:41], v[8:9], v[24:25]\n"
"v_pk_add_f32 v[42:43], v[10:11], v[26:27]\n"
"v_pk_add_f32 v[44:45], v[12:13], v[28:29]\n"
"v_pk_add_f32 v[46:47], v[14:15], v[30:31]\n"
"v_pk_add_f32 v[0:1], v[0:1], v[16:17] neg_lo:[0,1] neg_hi:[0,1]\n"
"v_pk_add_f32 v[2:3], v[2:3], v[18:19] neg_lo:[0,1] neg_hi:[0,1]\n"
"v_pk_add_f32 v[4:5], v[4:5], v[20:21] neg_lo:[0,1] neg_hi:[0,1]\n"
"v_pk_add_f32 v[6:7], v[6:7], v[22:23] neg_lo:[0,1] neg_hi:[0,1]\n"
"v_pk_add_f32 v[8:9], v[8:9], v[24:25] neg_lo:[0,1] neg_hi:[0,1]\n"
"v_pk_add_f32 v[10:11], v[10:11], v[26:27] neg_lo:[0,1] neg_hi:[0,1]\n"
"v_pk_add_f32 v[12:13], v[12:13], v[28:29] neg_lo:[0,1] neg_hi:[0,1]\n"
"v_pk_add_f32 v[14:15], v[14:15], v[30:31] neg_lo:[0,1] neg_hi:[0,1]\n"
"v_pk_add_f32 v[16:17], v[32:33], v[40:41] neg_lo:[0,1] neg_hi:[0,1]\n"
"v_pk_add_f32 v[20:21], v[44:45], v[36:37] neg_lo:[0,1] neg_hi:[0,1]\n"
"v_pk_add_f32 v[18:19], v[34:35], v[42:43] neg_lo:[0,1] neg_hi:[0,1]\n"
"v_pk_add_f32 v[22:23], v[46:47], v[38:39] neg_lo:[0,1] neg_hi:[0,1]\n"
"v_pk_add_f32 v[32:33], v[32:33], v[40:41]\n"
"v_pk_add_f32 v[36:37], v[36:37], v[44:45]\n"
"v_pk_add_f32 v[34:35], v[34:35], v[42:43]\n"
"v_pk_add_f32 v[38:39], v[38:39], v[46:47]\n"
"v_pk_add_f32 v[40:41], v[32:33], v[36:37]\n"
"v_pk_add_f32 v[44:45], v[32:33], v[36:37] neg_lo:[0,1] neg_hi:[0,1]\n"
"v_pk_add_f32 v[42:43], v[34:35], v[38:39]\n"
"v_pk_add_f32 v[46:47], v[34:35], v[38:39] neg_lo:[0,1] neg_hi:[0,1]\n"
"v_pk_add_f32 v[32:33], v[4:5], v[12:13] neg_lo:[0,1] neg_hi:[0,1]\n"
"v_pk_add_f32 v[36:37], v[4:5], v[12:13]\n"
"v_pk_add_f32 v[34:35], v[6:7], v[14:15] neg_lo:[0,1] neg_hi:[0,1]\n"
"v_pk_add_f32 v[38:39], v[6:7], v[14:15]\n"
"v_pk_fma_f32 v[24:25], v[32:33], s[26:27], v[0:1]\n"
"v_pk_fma_f32 v[28:29], v[36:37], s[26:27], v[8:9] neg_lo:[1,0,1] neg_hi:[1,0,1]\n"
"v_pk_fma_f32 v[4:5], v[32:33], s[26:27], v[0:1] neg_lo:[1,0,0] neg_hi:[1,0,0]\n"
"v_pk_fma_f32 v[12:13], v[36:37], s[26:27], v[8:9] neg_lo:[1,0,0] neg_hi:[1,0,0]\n"
"v_pk_fma_f32 v[26:27], v[34:35], s[26:27], v[2:3]\n"
"v_pk_fma_f32 v[30:31], v[38:39], s[26:27], v[10:11] neg_lo:[1,0,1] neg_hi:[1,0,1]\n"
"v_pk_fma_f32 v[6:7], v[34:35], s[26:27], v[2:3] neg_lo:[1,0,0] neg_hi:[1,0,0]\n"
"v_pk_fma_f32 v[14:15], v[38:39], s[26:27], v[10:11] neg_lo:[1,0,0] neg_hi:[1,0,0]\n"
"s_mov_b32 s28, 0x41800000\n"
"s_mov_b32 s29, 0x41800000\n"
"s_waitcnt vmcnt(21)\n"
"v_lshlrev_b32 v216, 16, v216\n"
"v_mul_f32 v216, 0x41000000, v216\n"
"s_waitcnt vmcnt(20)\n"
"v_lshlrev_b32 v217, 16, v217\n"
"v_mul_f32 v217, 0x41000000, v217\n"
"s_waitcnt vmcnt(19)\n"
"v_lshlrev_b32 v218, 16, v218\n"
"v_mul_f32 v218, 0x41000000, v218\n"
"s_waitcnt vmcnt(18)\n"
"v_lshlrev_b32 v219, 16, v219\n"
"v_mul_f32 v219, 0x41000000, v219\n"
"v_cvt_pk_f16_f32 v232, v216, v217\n"
"v_mov_b32 v233, v117\n"
"v_cvt_pk_f16_f32 v234, v218, v219\n"
"v_mov_b32 v235, v117\n"
"v_min_u32 v238, 16, v113\n"
"v_lshrrev_b32 v239, 6, v97\n"
"v_lshl_or_b32 v238, v238, 23, v239\n"
"s_add_u32 s30, s101, 110592\n"
"s_cmp_lt_u32 s101, 12288\n"
"s_cselect_b32 s30, 0x8000000, s30\n"
"buffer_store_dwordx4 v[232:235], v238, s[72:75], s30 offen sc1\n"
"s_and_b32 s30, s17, 3\n"
"s_cselect_b32 s30, 1.0, 0\n"
"v_mul_f32 v216, s30, v216\n"
"v_mul_f32 v217, s30, v217\n"
"v_mul_f32 v218, s30, v218\n"
"v_mul_f32 v219, s30, v219\n"
"v_lshlrev_b32 v176, 16, v80\n"
"v_lshlrev_b32 v177, 16, v84\n"
"v_pk_mul_f32 v[176:177], v[176:177], s[28:29]\n"
"v_lshlrev_b32 v178, 16, v88\n"
"v_lshlrev_b32 v179, 16, v92\n"
"v_pk_mul_f32 v[178:179], v[178:179], s[28:29]\n"
"s_andn2_b32 exec_lo, 1, s14\n"
"s_mov_b32 exec_hi, 0\n"
"v_fma_f32 v176, 0.5, v176, v216\n"
"v_fma_f32 v177, 0.5, v177, -v217\n"
"v_fma_f32 v178, 0.5, v178, v218\n"
"v_fma_f32 v179, 0.5, v179, -v219\n"
"s_mov_b64 exec, -1\n"
"v_cvt_pk_f16_f32 v176, v176, v177\n"
"v_cvt_pk_f16_f32 v178, v178, v179\n"
"v_and_or_b32 v176, v176, v236, s37\n"
"v_and_or_b32 v178, v178, v236, s37\n"
"ds_write2_b32 v126, v176, v178 offset0:0 offset1:4\n"
"v_and_b32 v180, s25, v80\n"
"v_and_b32 v181, s25, v84\n"
"v_pk_mul_f32 v[180:181], v[180:181], s[28:29]\n"
"v_and_b32 v182, s25, v88\n"
"v_and_b32 v183, s25, v92\n"
"v_pk_mul_f32 v[182:183], v[182:183], s[28:29]\n"
"v_cvt_pk_f16_f32 v180, v180, v181\n"
"v_cvt_pk_f16_f32 v182, v182, v183\n"
"v_and_or_b32 v180, v180, v236, s37\n"
"v_and_or_b32 v182, v182, v236, s37\n"
"ds_write2_b32 v126, v180, v182 offset0:32 offset1:36\n"
"v_lshlrev_b32 v184, 16, v81\n"
"v_lshlrev_b32 v185, 16, v85\n"
"v_pk_mul_f32 v[184:185], v[184:185], s[28:29]\n"
"v_lshlrev_b32 v186, 16, v89\n"
"v_lshlrev_b32 v187, 16, v93\n"
"v_pk_mul_f32 v[186:187], v[186:187], s[28:29]\n"
"v_cvt_pk_f16_f32 v184, v184, v185\n"
"v_cvt_pk_f16_f32 v186, v186, v187\n"
"v_and_or_b32 v184, v184, v236, s37\n"
"v_and_or_b32 v186, v186, v236, s37\n"
"ds_write2_b32 v126, v184, v186 offset0:64 offset1:68\n"
"v_and_b32 v188, s25, v81\n"
"v_and_b32 v189, s25, v85\n"
"v_pk_mul_f32 v[188:189], v[188:189], s[28:29]\n"
"v_and_b32 v190, s25, v89\n"
"v_and_b32 v191, s25, v93\n"
"v_pk_mul_f32 v[190:191], v[190:191], s[28:29]\n"
"v_cvt_pk_f16_f32 v188, v188, v189\n"
"v_cvt_pk_f16_f32 v190, v190, v191\n"
"v_and_or_b32 v188, v188, v236, s37\n"
"v_and_or_b32 v190, v190, v236, s37\n"
"ds_write2_b32 v126, v188, v190 offset0:96 offset1:100\n"
"v_lshlrev_b32 v176, 16, v82\n"
"v_lshlrev_b32 v177, 16, v86\n"
"v_pk_mul_f32 v[176:177], v[176:177], s[28:29]\n"
"v_lshlrev_b32 v178, 16, v90\n"
"v_lshlrev_b32 v179, 16, v94\n"
"v_pk_mul_f32 v[178:179], v[178:179], s[28:29]\n"
"v_cvt_pk_f16_f32 v176, v176, v177\n"
"v_cvt_pk_f16_f32 v178, v178, v179\n"
"v_and_or_b32 v176, v176, v236, s37\n"
"v_and_or_b32 v178, v178, v236, s37\n"
"ds_write2_b32 v126, v176, v178 offset0:132 offset1:136\n"
"v_and_b32 v180, s25, v82\n"
"v_and_b32 v181, s25, v86\n"
"v_pk_mul_f32 v[180:181], v[180:181], s[28:29]\n"
"v_and_b32 v182, s25, v90\n"
"v_and_b32 v183, s25, v94\n"
"v_pk_mul_f32 v[182:183], v[182:183], s[28:29]\n"
"v_cvt_pk_f16_f32 v180, v180, v181\n"
"v_cvt_pk_f16_f32 v182, v182, v183\n"
"v_and_or_b32 v180, v180, v236, s37\n"
"v_and_or_b32 v182, v182, v236, s37\n"
"ds_write2_b32 v126, v180, v182 offset0:164 offset1:168\n"
"v_lshlrev_b32 v184, 16, v83\n"
"v_lshlrev_b32 v185, 16, v87\n"
"v_pk_mul_f32 v[184:185], v[184:185], s[28:29]\n"
"v_lshlrev_b32 v186, 16, v91\n"
"v_lshlrev_b32 v187, 16, v95\n"
"v_pk_mul_f32 v[186:187], v[186:187], s[28:29]\n"
"v_cvt_pk_f16_f32 v184, v184, v185\n"
"v_cvt_pk_f16_f32 v186, v186, v187\n"
"v_and_or_b32 v184, v184, v236, s37\n"
"v_and_or_b32 v186, v186, v236, s37\n"
"ds_write2_b32 v126, v184, v186 offset0:196 offset1:200\n"
"v_and_b32 v188, s25, v83\n"
"v_and_b32 v189, s25, v87\n"
"v_pk_mul_f32 v[188:189], v[188:189], s[28:29]\n"
"v_and_b32 v190, s25, v91\n"
"v_and_b32 v191, s25, v95\n"
"v_pk_mul_f32 v[190:191], v[190:191], s[28:29]\n"
"s_and_b32 exec_lo, 1, s14\n"
"s_mov_b32 exec_hi, 0\n"
"v_fma_f32 v188, 0.5, v188, v216\n"
"v_fma_f32 v189, 0.5, v189, -v217\n"
"v_fma_f32 v190, 0.5, v190, v218\n"
"v_fma_f32 v191, 0.5, v191, -v219\n"
"s_mov_b64 exec, -1\n"
"v_cvt_pk_f16_f32 v188, v188, v189\n"
"v_cvt_pk_f16_f32 v190, v190, v191\n"
"v_and_or_b32 v188, v188, v236, s37\n"
"v_and_or_b32 v190, v190, v236, s37\n"
"ds_write2_b32 v126, v188, v190 offset0:228 offset1:232\n"
"v_lshrrev_b32 v240, 9, v241\n"
"v_lshl_add_u32 v240, v240, 4, v241\n"
"s_add_u32 s28, s15, 5242880\n"
"s_waitcnt lgkmcnt(0)\n"
"s_barrier\n"
"ds_read_b128 v[80:83], v240 offset:20480\n"
"ds_read_b128 v[84:87], v240 offset:21536\n"
"ds_read_b128 v[88:91], v240 offset:22592\n"
"ds_read_b128 v[92:95], v240 offset:23648\n"
"s_waitcnt lgkmcnt(3)\n"
"buffer_store_dwordx4 v[80:83], v113, s[72:75], s28 offen offset:0 sc1\n"
"s_waitcnt lgkmcnt(2)\n"
"buffer_store_dwordx4 v[84:87], v113, s[72:75], s28 offen offset:1024 sc1\n"
"s_waitcnt lgkmcnt(1)\n"
"buffer_store_dwordx4 v[88:91], v113, s[72:75], s28 offen offset:2048 sc1\n"
"s_waitcnt lgkmcnt(0)\n"
"buffer_store_dwordx4 v[92:95], v113, s[72:75], s28 offen offset:3072 sc1\n"
"s_waitcnt vmcnt(12)\n"
"v_xor_b32 v128, s38, v128\n"
"v_xor_b32 v129, s38, v129\n"
"v_xor_b32 v130, s38, v130\n"
"v_xor_b32 v131, s38, v131\n"
"s_waitcnt vmcnt(11)\n"
"v_xor_b32 v132, s38, v132\n"
"v_xor_b32 v133, s38, v133\n"
"v_xor_b32 v134, s38, v134\n"
"v_xor_b32 v135, s38, v135\n"
"v_or3_b32 v239, v128, v129, v130\n"
"v_or3_b32 v239, v239, v131, v132\n"
"v_or3_b32 v239, v239, v133, v134\n"
"v_bitop3_b32 v239, v239, v135, v247 bitop3:0x54\n"
"v_cmp_ne_u32 s[28:29], 0, v239\n"
"s_waitcnt vmcnt(10)\n"
"v_xor_b32 v136, s38, v136\n"
"v_xor_b32 v137, s38, v137\n"
"v_xor_b32 v138, s38, v138\n"
"v_xor_b32 v139, s38, v139\n"
"s_waitcnt vmcnt(9)\n"
"v_xor_b32 v140, s38, v140\n"
"v_xor_b32 v141, s38, v141\n"
"v_xor_b32 v142, s38, v142\n"
"v_xor_b32 v143, s38, v143\n"
"v_or3_b32 v239, v136, v137, v138\n"
"v_or3_b32 v239, v239, v139, v140\n"
"v_or3_b32 v239, v239, v141, v142\n"
"v_bitop3_b32 v239, v239, v143, v247 bitop3:0x54\n"
"v_cmp_ne_u32 s[30:31], 0, v239\n"
"s_waitcnt vmcnt(8)\n"
"v_xor_b32 v144, s38, v144\n"
"v_xor_b32 v145, s38, v145\n"
"v_xor_b32 v146, s38, v146\n"
"v_xor_b32 v147, s38, v147\n"
"s_waitcnt vmcnt(7)\n"
"v_xor_b32 v148, s38, v148\n"
"v_xor_b32 v149, s38, v149\n"
"v_xor_b32 v150, s38, v150\n"
"v_xor_b32 v151, s38, v151\n"
"v_or3_b32 v239, v144, v145, v146\n"
"v_or3_b32 v239, v239, v147, v148\n"
"v_or3_b32 v239, v239, v149, v150\n"
"v_bitop3_b32 v239, v239, v151, v247 bitop3:0x54\n"
"v_cmp_ne_u32 s[32:33], 0, v239\n"
"s_waitcnt vmcnt(6)\n"
"v_xor_b32 v152, s38, v152\n"
"v_xor_b32 v153, s38, v153\n"
"v_xor_b32 v154, s38, v154\n"
"v_xor_b32 v155, s38, v155\n"
"s_waitcnt vmcnt(5)\n"
"v_xor_b32 v156, s38, v156\n"
"v_xor_b32 v157, s38, v157\n"
"v_xor_b32 v158, s38, v158\n"
"v_xor_b32 v159, s38, v159\n"
"v_or3_b32 v239, v152, v153, v154\n"
"v_or3_b32 v239, v239, v155, v156\n"
"v_or3_b32 v239, v239, v157, v158\n"
"v_bitop3_b32 v239, v239, v159, v247 bitop3:0x54\n"
"v_cmp_ne_u32 s[34:35], 0, v239\n"
"s_or_b64 vcc, s[28:29], s[30:31]\n"
"s_or_b64 vcc, vcc, s[32:33]\n"
"s_or_b64 vcc, vcc, s[34:35]\n"
"s_cbranch_scc1 Lslow_g1_K\n"
"Lret_g1_K:\n"
"v_pk_add_f32 v[128:129], v[128:129], v[136:137]\n"
"v_pk_add_f32 v[144:145], v[144:145], v[152:153]\n"
"v_pk_add_f32 v[130:131], v[130:131], v[138:139]\n"
"v_pk_add_f32 v[146:147], v[146:147], v[154:155]\n"
"v_pk_add_f32 v[128:129], v[128:129], v[144:145]\n"
"v_pk_add_f32 v[130:131], v[130:131], v[146:147]\n"
"v_cvt_pk_bf16_f32 v128, 0, v128\n"
"v_cvt_pk_bf16_f32 v129, 0, v129\n"
"v_cvt_pk_bf16_f32 v130, 0, v130\n"
"v_cvt_pk_bf16_f32 v131, 0, v131\n"
"v_pk_add_f32 v[132:133], v[132:133], v[140:141]\n"
"v_pk_add_f32 v[148:149], v[148:149], v[156:157]\n"
"v_pk_add_f32 v[134:135], v[134:135], v[142:143]\n"
"v_pk_add_f32 v[150:151], v[150:151], v[158:159]\n"
"v_pk_add_f32 v[132:133], v[132:133], v[148:149]\n"
"v_pk_add_f32 v[134:135], v[134:135], v[150:151]\n"
"v_cvt_pk_bf16_f32 v132, 0, v132\n"
"v_cvt_pk_bf16_f32 v133, 0, v133\n"
"v_cvt_pk_bf16_f32 v134, 0, v134\n"
"v_cvt_pk_bf16_f32 v135, 0, v135\n"
"v_add_f32 v132, 1.0, v132\n"
"v_add_f32 v133, 1.0, v133\n"
"v_add_f32 v134, 1.0, v134\n"
"v_add_f32 v135, 1.0, v135\n"
"v_mul_f32 v128, 0x3c800000, v128\n"
"v_mul_f32 v129, 0x3c800000, v129\n"
"v_mul_f32 v130, 0x3c800000, v130\n"
"v_mul_f32 v131, 0x3c800000, v131\n"
"v_cvt_pk_bf16_f32 v132, 0, v132\n"
"v_cvt_pk_bf16_f32 v133, 0, v133\n"
"v_cvt_pk_bf16_f32 v134, 0, v134\n"
"v_cvt_pk_bf16_f32 v135, 0, v135\n"
"v_mul_f32 v132, 0x3b000000, v132\n"
"v_mul_f32 v133, 0x3b000000, v133\n"
"v_mul_f32 v134, 0x3b000000, v134\n"
"v_mul_f32 v135, 0x3b000000, v135\n"
"s_mov_b32 exec_hi, 0\n"
"buffer_load_dword a255, v104, s[76:79], s59 offen\n"
"s_mov_b64 exec, -1\n"
"v_pk_mul_f32 v[24:25], v[24:25], v[132:133]\n"
"v_pk_mul_f32 v[26:27], v[26:27], v[134:135]\n"
"v_cvt_pk_f16_f32 v24, v24, v25\n"
"v_cvt_pk_f16_f32 v25, v26, v27\n"
"ds_write_b64 v96, v[24:25] offset:4096\n"
"v_pk_mul_f32 v[28:29], v[28:29], v[132:133]\n"
"v_pk_mul_f32 v[30:31], v[30:31], v[134:135]\n"
"v_cvt_pk_f16_f32 v28, v28, v29\n"
"v_cvt_pk_f16_f32 v29, v30, v31\n"
"ds_write_b64 v96, v[28:29] offset:6144\n"
"v_pk_mul_f32 v[4:5], v[4:5], v[132:133]\n"
"v_pk_mul_f32 v[6:7], v[6:7], v[134:135]\n"
"v_cvt_pk_f16_f32 v4, v4, v5\n"
"v_cvt_pk_f16_f32 v5, v6, v7\n"
"ds_write_b64 v96, v[4:5] offset:12288\n"
"v_pk_mul_f32 v[12:13], v[12:13], v[132:133]\n"
"v_pk_mul_f32 v[14:15], v[14:15], v[134:135]\n"
"v_cvt_pk_f16_f32 v12, v12, v13\n"
"v_cvt_pk_f16_f32 v13, v14, v15\n"
"ds_write_b64 v96, v[12:13] offset:14336\n"
"v_pk_mul_f32 v[16:17], v[16:17], v[132:133]\n"
"v_pk_mul_f32 v[18:19], v[18:19], v[134:135]\n"
"v_cvt_pk_f16_f32 v16, v16, v17\n"
"v_cvt_pk_f16_f32 v17, v18, v19\n"
"ds_write_b64 v96, v[16:17] offset:8192\n"
"v_pk_mul_f32 v[20:21], v[20:21], v[132:133]\n"
"v_pk_mul_f32 v[22:23], v[22:23], v[134:135]\n"
"v_cvt_pk_f16_f32 v20, v20, v21\n"
"v_cvt_pk_f16_f32 v21, v22, v23\n"
"ds_write_b64 v96, v[20:21] offset:10240\n"
"v_pk_fma_f32 v[40:41], v[40:41], v[132:133], v[128:129]\n"
"v_pk_fma_f32 v[42:43], v[42:43], v[134:135], v[130:131]\n"
"v_cvt_pk_f16_f32 v40, v40, v41\n"
"v_cvt_pk_f16_f32 v41, v42, v43\n"
"ds_write_b64 v96, v[40:41] offset:0\n"
"v_pk_mul_f32 v[44:45], v[44:45], v[132:133]\n"
"v_pk_mul_f32 v[46:47], v[46:47], v[134:135]\n"
"v_cvt_pk_f16_f32 v44, v44, v45\n"
"v_cvt_pk_f16_f32 v45, v46, v47\n"
"ds_write_b64 v96, v[44:45] offset:2048\n"
"s_waitcnt lgkmcnt(0)\n"
"s_barrier\n"
"v_subrev_u32 v238, 65536, v116\n"
"buffer_store_dword v117, v238, s[72:75], 0 offen sc1\n"
"v_add_u32 v242, s22, v98\n"
"ds_read_b64_tr_b16 v[0:1], v98 offset:0\n"
"ds_read_b64_tr_b16 v[2:3], v98 offset:256\n"
"ds_read_b64_tr_b16 v[8:9], v98 offset:2048\n"
"ds_read_b64_tr_b16 v[10:11], v98 offset:2304\n"
"ds_read_b64_tr_b16 v[4:5], v98 offset:32\n"
"ds_read_b64_tr_b16 v[6:7], v98 offset:288\n"
"ds_read_b64_tr_b16 v[12:13], v98 offset:2080\n"
"ds_read_b64_tr_b16 v[14:15], v98 offset:2336\n"
"ds_read_b64_tr_b16 v[16:17], v242 offset:0\n"
"ds_read_b64_tr_b16 v[18:19], v242 offset:256\n"
"ds_read_b64_tr_b16 v[24:25], v242 offset:2048\n"
"ds_read_b64_tr_b16 v[26:27], v242 offset:2304\n"
"ds_read_b64_tr_b16 v[20:21], v242 offset:32\n"
"ds_read_b64_tr_b16 v[22:23], v242 offset:288\n"
"ds_read_b64_tr_b16 v[28:29], v242 offset:2080\n"
"s_waitcnt lgkmcnt(11)\n"
"ds_read_b64_tr_b16 v[30:31], v242 offset:2336\n"
"v_mfma_f32_16x16x32_f16 v[128:131], v[0:3], v[220:223], 0\n"
"v_mfma_f32_16x16x32_f16 v[132:135], v[0:3], v[224:227], 0\n"
"s_waitcnt lgkmcnt(10)\n"
"v_mfma_f32_16x16x32_f16 v[136:139], v[4:7], v[220:223], 0\n"
"v_mfma_f32_16x16x32_f16 v[140:143], v[4:7], v[224:227], 0\n"
"v_mfma_f32_16x16x32_f16 v[128:131], v[8:11], v[228:231], v[128:131]\n"
"v_mfma_f32_16x16x32_f16 v[132:135], v[8:11], v[220:223], v[132:135]\n"
"s_waitcnt lgkmcnt(8)\n"
"v_mfma_f32_16x16x32_f16 v[136:139], v[12:15], v[228:231], v[136:139]\n"
"v_mfma_f32_16x16x32_f16 v[140:143], v[12:15], v[220:223], v[140:143]\n"
"s_waitcnt lgkmcnt(0)\n"
"s_cmp_ge_u32 s21, 2\n"
"s_cbranch_scc1 Lk1real_K\n"
"v_mfma_f32_16x16x32_f16 v[144:147], v[16:19], v[220:223], 0\n"
"v_mfma_f32_16x16x32_f16 v[148:151], v[16:19], v[224:227], 0\n"
"v_mfma_f32_16x16x32_f16 v[152:155], v[20:23], v[220:223], 0\n"
"v_mfma_f32_16x16x32_f16 v[156:159], v[20:23], v[224:227], 0\n"
"v_mfma_f32_16x16x32_f16 v[144:147], v[24:27], v[228:231], v[144:147]\n"
"v_mfma_f32_16x16x32_f16 v[148:151], v[24:27], v[220:223], v[148:151]\n"
"v_mfma_f32_16x16x32_f16 v[152:155], v[28:31], v[228:231], v[152:155]\n"
"v_mfma_f32_16x16x32_f16 v[156:159], v[28:31], v[220:223], v[156:159]\n"
"v_pk_mul_f32 v[32:33], v[128:129], v[192:193]\n"
"v_pk_mul_f32 v[34:35], v[130:131], v[194:195]\n"
"v_pk_fma_f32 v[32:33], v[132:133], v[200:201], v[32:33]\n"
"v_pk_fma_f32 v[34:35], v[134:135], v[202:203], v[34:35]\n"
"v_pk_mul_f32 v[132:133], v[132:133], v[192:193]\n"
"v_pk_mul_f32 v[134:135], v[134:135], v[194:195]\n"
"v_pk_fma_f32 v[132:133], v[128:129], v[200:201], v[132:133] neg_lo:[1,0,0] neg_hi:[1,0,0]\n"
"v_pk_fma_f32 v[134:135], v[130:131], v[202:203], v[134:135] neg_lo:[1,0,0] neg_hi:[1,0,0]\n"
"v_cvt_pk_f16_f32 v32, v32, v132\n"
"v_cvt_pk_f16_f32 v33, v33, v133\n"
"v_cvt_pk_f16_f32 v34, v34, v134\n"
"v_cvt_pk_f16_f32 v35, v35, v135\n"
"v_pk_mul_f32 v[36:37], v[136:137], v[196:197]\n"
"v_pk_mul_f32 v[38:39], v[138:139], v[198:199]\n"
"v_pk_fma_f32 v[36:37], v[140:141], v[204:205], v[36:37]\n"
"v_pk_fma_f32 v[38:39], v[142:143], v[206:207], v[38:39]\n"
"v_pk_mul_f32 v[140:141], v[140:141], v[196:197]\n"
"v_pk_mul_f32 v[142:143], v[142:143], v[198:199]\n"
"v_pk_fma_f32 v[140:141], v[136:137], v[204:205], v[140:141] neg_lo:[1,0,0] neg_hi:[1,0,0]\n"
"v_pk_fma_f32 v[142:143], v[138:139], v[206:207], v[142:143] neg_lo:[1,0,0] neg_hi:[1,0,0]\n"
"v_cvt_pk_f16_f32 v36, v36, v140\n"
"v_cvt_pk_f16_f32 v37, v37, v141\n"
"v_cvt_pk_f16_f32 v38, v38, v142\n"
"v_cvt_pk_f16_f32 v39, v39, v143\n"
"v_mfma_f32_16x16x32_f16 v[128:131], v[48:51], v[32:35], 0\n"
"v_mfma_f32_16x16x32_f16 v[132:135], v[52:55], v[32:35], 0\n"
"v_mfma_f32_16x16x32_f16 v[128:131], v[56:59], v[36:39], v[128:131]\n"
"v_mfma_f32_16x16x32_f16 v[132:135], v[60:63], v[36:39], v[132:135]\n"
"v_mfma_f32_16x16x32_f16 v[136:139], v[64:67], v[32:35], 0\n"
"v_mfma_f32_16x16x32_f16 v[140:143], v[68:71], v[32:35], 0\n"
"v_mfma_f32_16x16x32_f16 v[136:139], v[72:75], v[36:39], v[136:139]\n"
"v_mfma_f32_16x16x32_f16 v[140:143], v[76:79], v[36:39], v[140:143]\n"
"v_pk_mul_f32 v[40:41], v[144:145], v[192:193]\n"
"v_pk_mul_f32 v[42:43], v[146:147], v[194:195]\n"
"v_pk_fma_f32 v[40:41], v[148:149], v[200:201], v[40:41]\n"
"v_pk_fma_f32 v[42:43], v[150:151], v[202:203], v[42:43]\n"
"v_pk_mul_f32 v[148:149], v[148:149], v[192:193]\n"
"v_pk_mul_f32 v[150:151], v[150:151], v[194:195]\n"
"v_pk_fma_f32 v[148:149], v[144:145], v[200:201], v[148:149] neg_lo:[1,0,0] neg_hi:[1,0,0]\n"
"v_pk_fma_f32 v[150:151], v[146:147], v[202:203], v[150:151] neg_lo:[1,0,0] neg_hi:[1,0,0]\n"
"v_cvt_pk_f16_f32 v40, v40, v148\n"
"v_cvt_pk_f16_f32 v41, v41, v149\n"
"v_cvt_pk_f16_f32 v42, v42, v150\n"
"v_cvt_pk_f16_f32 v43, v43, v151\n"
"v_pk_mul_f32 v[44:45], v[152:153], v[196:197]\n"
"v_pk_mul_f32 v[46:47], v[154:155], v[198:199]\n"
"v_pk_fma_f32 v[44:45], v[156:157], v[204:205], v[44:45]\n"
"v_pk_fma_f32 v[46:47], v[158:159], v[206:207], v[46:47]\n"
"v_pk_mul_f32 v[156:157], v[156:157], v[196:197]\n"
"v_pk_mul_f32 v[158:159], v[158:159], v[198:199]\n"
"v_pk_fma_f32 v[156:157], v[152:153], v[204:205], v[156:157] neg_lo:[1,0,0] neg_hi:[1,0,0]\n"
"v_pk_fma_f32 v[158:159], v[154:155], v[206:207], v[158:159] neg_lo:[1,0,0] neg_hi:[1,0,0]\n"
"v_cvt_pk_f16_f32 v44, v44, v156\n"
"v_cvt_pk_f16_f32 v45, v45, v157\n"
"v_cvt_pk_f16_f32 v46, v46, v158\n"
"v_cvt_pk_f16_f32 v47, v47, v159\n"
"v_mfma_f32_16x16x32_f16 v[144:147], v[48:51], v[40:43], 0\n"
"v_cvt_pk_f16_f32 v80, v128, v132\n"
"v_cvt_pk_f16_f32 v81, v129, v133\n"
"v_mfma_f32_16x16x32_f16 v[148:151], v[52:55], v[40:43], 0\n"
"v_cvt_pk_f16_f32 v82, v130, v134\n"
"v_cvt_pk_f16_f32 v83, v131, v135\n"
"v_mfma_f32_16x16x32_f16 v[144:147], v[56:59], v[44:47], v[144:147]\n"
"v_and_or_b32 v80, v80, v236, s37\n"
"v_and_or_b32 v81, v81, v236, s37\n"
"v_mfma_f32_16x16x32_f16 v[148:151], v[60:63], v[44:47], v[148:151]\n"
"v_and_or_b32 v82, v82, v236, s37\n"
"v_and_or_b32 v83, v83, v236, s37\n"
"v_mfma_f32_16x16x32_f16 v[152:155], v[64:67], v[40:43], 0\n"
"buffer_store_dwordx4 v[80:83], v99, s[72:75], s10 offen offset:0 sc1\n"
"v_cvt_pk_f16_f32 v84, v136, v140\n"
"v_mfma_f32_16x16x32_f16 v[156:159], v[68:71], v[40:43], 0\n"
"v_cvt_pk_f16_f32 v85, v137, v141\n"
"v_cvt_pk_f16_f32 v86, v138, v142\n"
"v_mfma_f32_16x16x32_f16 v[152:155], v[72:75], v[44:47], v[152:155]\n"
"v_cvt_pk_f16_f32 v87, v139, v143\n"
"v_and_or_b32 v84, v84, v236, s37\n"
"v_mfma_f32_16x16x32_f16 v[156:159], v[76:79], v[44:47], v[156:159]\n"
"v_and_or_b32 v85, v85, v236, s37\n"
"v_and_or_b32 v86, v86, v236, s37\n"
"v_and_or_b32 v87, v87, v236, s37\n"
"buffer_store_dwordx4 v[84:87], v99, s[72:75], s10 offen offset:2048 sc1\n"
"v_cvt_pk_f16_f32 v32, v144, v148\n"
"v_cvt_pk_f16_f32 v33, v145, v149\n"
"v_cvt_pk_f16_f32 v34, v146, v150\n"
"v_cvt_pk_f16_f32 v35, v147, v151\n"
"v_and_or_b32 v32, v32, v236, s37\n"
"v_and_or_b32 v33, v33, v236, s37\n"
"v_and_or_b32 v34, v34, v236, s37\n"
"v_and_or_b32 v35, v35, v236, s37\n"
"buffer_store_dwordx4 v[32:35], v99, s[72:75], s11 offen offset:0 sc1\n"
"v_cvt_pk_f16_f32 v36, v152, v156\n"
"v_cvt_pk_f16_f32 v37, v153, v157\n"
"v_cvt_pk_f16_f32 v38, v154, v158\n"
"v_cvt_pk_f16_f32 v39, v155, v159\n"
"v_and_or_b32 v36, v36, v236, s37\n"
"v_and_or_b32 v37, v37, v236, s37\n"
"v_and_or_b32 v38, v38, v236, s37\n"
"v_and_or_b32 v39, v39, v236, s37\n"
"buffer_store_dwordx4 v[36:39], v99, s[72:75], s11 offen offset:2048 sc1\n"
"s_branch Lk1done_K\n"
"Lk1real_K:\n"
"v_mfma_f32_16x16x32_f16 v[144:147], v[16:19], v[220:223], 0\n"
"v_mfma_f32_16x16x32_f16 v[148:151], v[16:19], v[224:227], 0\n"
"v_mfma_f32_16x16x32_f16 v[152:155], v[20:23], v[220:223], 0\n"
"v_mfma_f32_16x16x32_f16 v[156:159], v[20:23], v[224:227], 0\n"
"v_mfma_f32_16x16x32_f16 v[0:3], v[24:27], v[220:223], 0\n"
"v_mfma_f32_16x16x32_f16 v[4:7], v[24:27], v[224:227], 0\n"
"v_mfma_f32_16x16x32_f16 v[8:11], v[28:31], v[220:223], 0\n"
"v_mfma_f32_16x16x32_f16 v[12:15], v[28:31], v[224:227], 0\n"
"v_pk_mul_f32 v[32:33], v[128:129], v[192:193]\n"
"v_pk_mul_f32 v[34:35], v[130:131], v[194:195]\n"
"v_pk_fma_f32 v[32:33], v[132:133], v[200:201], v[32:33]\n"
"v_pk_fma_f32 v[34:35], v[134:135], v[202:203], v[34:35]\n"
"v_pk_mul_f32 v[132:133], v[132:133], v[192:193]\n"
"v_pk_mul_f32 v[134:135], v[134:135], v[194:195]\n"
"v_pk_fma_f32 v[132:133], v[128:129], v[200:201], v[132:133] neg_lo:[1,0,0] neg_hi:[1,0,0]\n"
"v_pk_fma_f32 v[134:135], v[130:131], v[202:203], v[134:135] neg_lo:[1,0,0] neg_hi:[1,0,0]\n"
"v_cvt_pk_f16_f32 v32, v32, v132\n"
"v_cvt_pk_f16_f32 v33, v33, v133\n"
"v_cvt_pk_f16_f32 v34, v34, v134\n"
"v_cvt_pk_f16_f32 v35, v35, v135\n"
"v_pk_mul_f32 v[36:37], v[136:137], v[196:197]\n"
"v_pk_mul_f32 v[38:39], v[138:139], v[198:199]\n"
"v_pk_fma_f32 v[36:37], v[140:141], v[204:205], v[36:37]\n"
"v_pk_fma_f32 v[38:39], v[142:143], v[206:207], v[38:39]\n"
"v_pk_mul_f32 v[140:141], v[140:141], v[196:197]\n"
"v_pk_mul_f32 v[142:143], v[142:143], v[198:199]\n"
"v_pk_fma_f32 v[140:141], v[136:137], v[204:205], v[140:141] neg_lo:[1,0,0] neg_hi:[1,0,0]\n"
"v_pk_fma_f32 v[142:143], v[138:139], v[206:207], v[142:143] neg_lo:[1,0,0] neg_hi:[1,0,0]\n"
"v_cvt_pk_f16_f32 v36, v36, v140\n"
"v_cvt_pk_f16_f32 v37, v37, v141\n"
"v_cvt_pk_f16_f32 v38, v38, v142\n"
"v_cvt_pk_f16_f32 v39, v39, v143\n"
"v_mfma_f32_16x16x32_f16 v[128:131], v[48:51], v[32:35], 0\n"
"v_mfma_f32_16x16x32_f16 v[132:135], v[52:55], v[32:35], 0\n"
"v_mfma_f32_16x16x32_f16 v[128:131], v[56:59], v[36:39], v[128:131]\n"
"v_mfma_f32_16x16x32_f16 v[132:135], v[60:63], v[36:39], v[132:135]\n"
"v_mfma_f32_16x16x32_f16 v[136:139], v[64:67], v[32:35], 0\n"
"v_mfma_f32_16x16x32_f16 v[140:143], v[68:71], v[32:35], 0\n"
"v_mfma_f32_16x16x32_f16 v[136:139], v[72:75], v[36:39], v[136:139]\n"
"v_mfma_f32_16x16x32_f16 v[140:143], v[76:79], v[36:39], v[140:143]\n"
"v_pk_mul_f32 v[40:41], v[144:145], v[192:193]\n"
"v_pk_mul_f32 v[42:43], v[146:147], v[194:195]\n"
"v_pk_fma_f32 v[40:41], v[148:149], v[200:201], v[40:41]\n"
"v_pk_fma_f32 v[42:43], v[150:151], v[202:203], v[42:43]\n"
"v_pk_mul_f32 v[148:149], v[148:149], v[192:193]\n"
"v_pk_mul_f32 v[150:151], v[150:151], v[194:195]\n"
"v_pk_fma_f32 v[148:149], v[144:145], v[200:201], v[148:149] neg_lo:[1,0,0] neg_hi:[1,0,0]\n"
"v_pk_fma_f32 v[150:151], v[146:147], v[202:203], v[150:151] neg_lo:[1,0,0] neg_hi:[1,0,0]\n"
"v_cvt_pk_f16_f32 v40, v40, v148\n"
"v_cvt_pk_f16_f32 v41, v41, v149\n"
"v_cvt_pk_f16_f32 v42, v42, v150\n"
"v_cvt_pk_f16_f32 v43, v43, v151\n"
"v_pk_mul_f32 v[44:45], v[152:153], v[196:197]\n"
"v_pk_mul_f32 v[46:47], v[154:155], v[198:199]\n"
"v_pk_fma_f32 v[44:45], v[156:157], v[204:205], v[44:45]\n"
"v_pk_fma_f32 v[46:47], v[158:159], v[206:207], v[46:47]\n"
"v_pk_mul_f32 v[156:157], v[156:157], v[196:197]\n"
"v_pk_mul_f32 v[158:159], v[158:159], v[198:199]\n"
"v_pk_fma_f32 v[156:157], v[152:153], v[204:205], v[156:157] neg_lo:[1,0,0] neg_hi:[1,0,0]\n"
"v_pk_fma_f32 v[158:159], v[154:155], v[206:207], v[158:159] neg_lo:[1,0,0] neg_hi:[1,0,0]\n"
"v_cvt_pk_f16_f32 v44, v44, v156\n"
"v_cvt_pk_f16_f32 v45, v45, v157\n"
"v_cvt_pk_f16_f32 v46, v46, v158\n"
"v_cvt_pk_f16_f32 v47, v47, v159\n"
"v_mfma_f32_16x16x32_f16 v[144:147], v[48:51], v[40:43], 0\n"
"v_cvt_pk_f16_f32 v80, v128, v132\n"
"v_cvt_pk_f16_f32 v81, v129, v133\n"
"v_mfma_f32_16x16x32_f16 v[148:151], v[52:55], v[40:43], 0\n"
"v_cvt_pk_f16_f32 v82, v130, v134\n"
"v_cvt_pk_f16_f32 v83, v131, v135\n"
"v_mfma_f32_16x16x32_f16 v[144:147], v[56:59], v[44:47], v[144:147]\n"
"v_and_or_b32 v80, v80, v236, s37\n"
"v_and_or_b32 v81, v81, v236, s37\n"
"v_mfma_f32_16x16x32_f16 v[148:151], v[60:63], v[44:47], v[148:151]\n"
"v_and_or_b32 v82, v82, v236, s37\n"
"v_and_or_b32 v83, v83, v236, s37\n"
"v_mfma_f32_16x16x32_f16 v[152:155], v[64:67], v[40:43], 0\n"
"buffer_store_dwordx4 v[80:83], v99, s[72:75], s10 offen offset:0 sc1\n"
"v_cvt_pk_f16_f32 v84, v136, v140\n"
"v_mfma_f32_16x16x32_f16 v[152:155], v[72:75], v[44:47], v[152:155]\n"
"v_cvt_pk_f16_f32 v85, v137, v141\n"
"v_cvt_pk_f16_f32 v86, v138, v142\n"
"v_cvt_pk_f16_f32 v87, v139, v143\n"
"v_and_or_b32 v84, v84, v236, s37\n"
"v_and_or_b32 v85, v85, v236, s37\n"
"v_and_or_b32 v86, v86, v236, s37\n"
"v_and_or_b32 v87, v87, v236, s37\n"
"buffer_store_dwordx4 v[84:87], v99, s[72:75], s10 offen offset:2048 sc1\n"
"v_pk_mul_f32 v[32:33], v[0:1], v[192:193]\n"
"v_pk_mul_f32 v[34:35], v[2:3], v[194:195]\n"
"v_pk_fma_f32 v[32:33], v[4:5], v[200:201], v[32:33]\n"
"v_pk_fma_f32 v[34:35], v[6:7], v[202:203], v[34:35]\n"
"v_pk_mul_f32 v[4:5], v[4:5], v[192:193]\n"
"v_pk_mul_f32 v[6:7], v[6:7], v[194:195]\n"
"v_pk_fma_f32 v[4:5], v[0:1], v[200:201], v[4:5] neg_lo:[1,0,0] neg_hi:[1,0,0]\n"
"v_pk_fma_f32 v[6:7], v[2:3], v[202:203], v[6:7] neg_lo:[1,0,0] neg_hi:[1,0,0]\n"
"v_cvt_pk_f16_f32 v32, v32, v4\n"
"v_cvt_pk_f16_f32 v33, v33, v5\n"
"v_cvt_pk_f16_f32 v34, v34, v6\n"
"v_cvt_pk_f16_f32 v35, v35, v7\n"
"v_pk_mul_f32 v[36:37], v[8:9], v[196:197]\n"
"v_pk_mul_f32 v[38:39], v[10:11], v[198:199]\n"
"v_pk_fma_f32 v[36:37], v[12:13], v[204:205], v[36:37]\n"
"v_pk_fma_f32 v[38:39], v[14:15], v[206:207], v[38:39]\n"
"v_pk_mul_f32 v[12:13], v[12:13], v[196:197]\n"
"v_pk_mul_f32 v[14:15], v[14:15], v[198:199]\n"
"v_pk_fma_f32 v[12:13], v[8:9], v[204:205], v[12:13] neg_lo:[1,0,0] neg_hi:[1,0,0]\n"
"v_pk_fma_f32 v[14:15], v[10:11], v[206:207], v[14:15] neg_lo:[1,0,0] neg_hi:[1,0,0]\n"
"v_cvt_pk_f16_f32 v36, v36, v12\n"
"v_cvt_pk_f16_f32 v37, v37, v13\n"
"v_cvt_pk_f16_f32 v38, v38, v14\n"
"v_cvt_pk_f16_f32 v39, v39, v15\n"
"v_mfma_f32_16x16x32_f16 v[156:159], v[64:67], v[32:35], 0\n"
"v_fmac_f32 v148, v108, v152\n"
"v_cvt_pk_f16_f32 v16, v144, v148\n"
"v_mfma_f32_16x16x32_f16 v[156:159], v[72:75], v[36:39], v[156:159]\n"
"v_cvt_pk_f16_f32 v17, v145, v149\n"
"v_cvt_pk_f16_f32 v18, v146, v150\n"
"v_mfma_f32_16x16x32_f16 v[128:131], v[48:51], v[32:35], 0\n"
"v_cvt_pk_f16_f32 v19, v147, v151\n"
"v_and_or_b32 v16, v16, v236, s37\n"
"v_mfma_f32_16x16x32_f16 v[132:135], v[52:55], v[32:35], 0\n"
"v_and_or_b32 v17, v17, v236, s37\n"
"v_and_or_b32 v18, v18, v236, s37\n"
"v_mfma_f32_16x16x32_f16 v[128:131], v[56:59], v[36:39], v[128:131]\n"
"v_and_or_b32 v19, v19, v236, s37\n"
"buffer_store_dwordx4 v[16:19], v99, s[72:75], s11 offen offset:0 sc1\n"
"v_mfma_f32_16x16x32_f16 v[132:135], v[60:63], v[36:39], v[132:135]\n"
"s_nop 7\n"
"v_fmac_f32 v132, v108, v156\n"
"v_cvt_pk_f16_f32 v20, v128, v132\n"
"v_cvt_pk_f16_f32 v21, v129, v133\n"
"v_cvt_pk_f16_f32 v22, v130, v134\n"
"v_cvt_pk_f16_f32 v23, v131, v135\n"
"v_and_or_b32 v20, v20, v236, s37\n"
"v_and_or_b32 v21, v21, v236, s37\n"
"v_and_or_b32 v22, v22, v236, s37\n"
"v_and_or_b32 v23, v23, v236, s37\n"
"buffer_store_dwordx4 v[20:23], v99, s[72:75], s11 offen offset:2048 sc1\n"
"Lk1done_K:\n"
"s_nop 1\n"
"ds_read_b64_tr_b16 v[32:33], v251 offset:0\n"
"ds_read_b64_tr_b16 v[34:35], v251 offset:512\n"
"ds_read_b64_tr_b16 v[36:37], v251 offset:32\n"
"ds_read_b64_tr_b16 v[38:39], v251 offset:544\n"
"ds_read_b64_tr_b16 v[40:41], v251 offset:64\n"
"ds_read_b64_tr_b16 v[42:43], v251 offset:576\n"
"ds_read_b64_tr_b16 v[44:45], v251 offset:96\n"
"ds_read_b64_tr_b16 v[46:47], v251 offset:608\n"
"ds_read_b64_tr_b16 v[48:49], v251 offset:4096\n"
"ds_read_b64_tr_b16 v[50:51], v251 offset:4608\n"
"ds_read_b64_tr_b16 v[52:53], v251 offset:4128\n"
"ds_read_b64_tr_b16 v[54:55], v251 offset:4640\n"
"ds_read_b64_tr_b16 v[56:57], v251 offset:4160\n"
"ds_read_b64_tr_b16 v[58:59], v251 offset:4672\n"
"ds_read_b64_tr_b16 v[60:61], v251 offset:4192\n"
"s_waitcnt lgkmcnt(11)\n"
"ds_read_b64_tr_b16 v[62:63], v251 offset:4704\n"
"v_mfma_f32_16x16x32_bf16 v[32:35], v[32:35], v[208:211], 0\n"
"v_mfma_f32_16x16x32_bf16 v[36:39], v[36:39], v[208:211], 0\n"
"s_waitcnt lgkmcnt(10)\n"
"v_mfma_f32_16x16x32_bf16 v[40:43], v[40:43], v[208:211], 0\n"
"s_waitcnt lgkmcnt(8)\n"
"v_mfma_f32_16x16x32_bf16 v[44:47], v[44:47], v[208:211], 0\n"
"s_waitcnt lgkmcnt(6)\n"
"v_mfma_f32_16x16x32_bf16 v[32:35], v[48:51], v[212:215], v[32:35]\n"
"s_waitcnt lgkmcnt(4)\n"
"v_mfma_f32_16x16x32_bf16 v[36:39], v[52:55], v[212:215], v[36:39]\n"
"s_waitcnt lgkmcnt(2)\n"
"v_mfma_f32_16x16x32_bf16 v[40:43], v[56:59], v[212:215], v[40:43]\n"
"s_waitcnt lgkmcnt(0)\n"
"v_mfma_f32_16x16x32_bf16 v[44:47], v[60:63], v[212:215], v[44:47]\n"
"s_mov_b32 exec_lo, 0x00010001\n"
"s_mov_b32 exec_hi, 0x00010001\n"
"ds_write_b128 v249, v[32:35] offset:17408\n"
"s_nop 0\n"
"ds_write_b128 v249, v[36:39] offset:17472\n"
"s_nop 0\n"
"ds_write_b128 v249, v[40:43] offset:17536\n"
"s_nop 0\n"
"ds_write_b128 v249, v[44:47] offset:17600\n"
"s_mov_b64 exec, -1\n"
"s_waitcnt lgkmcnt(0)\n"
"s_barrier\n"
"ds_read_b128 v[32:35], v250 offset:17408\n"
"ds_read_b128 v[36:39], v250 offset:17664\n"
"ds_read_b128 v[40:43], v250 offset:17920\n"
"ds_read_b128 v[44:47], v250 offset:18176\n"
"s_waitcnt lgkmcnt(2)\n"
"v_pk_add_f32 v[32:33], v[32:33], v[36:37]\n"
"s_waitcnt lgkmcnt(0)\n"
"v_pk_add_f32 v[40:41], v[40:41], v[44:45]\n"
"v_pk_add_f32 v[34:35], v[34:35], v[38:39]\n"
"v_pk_add_f32 v[42:43], v[42:43], v[46:47]\n"
"v_pk_add_f32 v[32:33], v[32:33], v[40:41]\n"
"v_pk_add_f32 v[34:35], v[34:35], v[42:43]\n"
"v_and_or_b32 v32, v32, v247, s38\n"
"v_and_or_b32 v33, v33, v247, s38\n"
"v_and_or_b32 v34, v34, v247, s38\n"
"v_and_or_b32 v35, v35, v247, s38\n"
"buffer_store_dwordx4 v[32:35], v246, s[72:75], s98 offen sc1\n"
"v_cvt_f32_u32 v232, s17\n"
"v_fmamk_f32 v239, v109, 0x41000000, v232\n"
"v_mul_f32 v239, 0x3c000000, v239\n"
"v_mul_f32 v238, v110, v239\n"
"v_cos_f32 v32, v238\n"
"v_sin_f32 v36, v238\n"
"v_add_f32 v40, v238, v239\n"
"v_cos_f32 v33, v40\n"
"v_sin_f32 v37, v40\n"
"v_add_f32 v40, v40, v239\n"
"v_cos_f32 v34, v40\n"
"v_sin_f32 v38, v40\n"
"v_add_f32 v40, v40, v239\n"
"v_cos_f32 v35, v40\n"
"v_sin_f32 v39, v40\n"
"v_cvt_pk_f16_f32 v0, v32, v36\n"
"v_cvt_pk_f16_f32 v4, -v36, v32\n"
"v_cvt_pk_f16_f32 v1, v33, v37\n"
"v_cvt_pk_f16_f32 v5, -v37, v33\n"
"v_cvt_pk_f16_f32 v2, v34, v38\n"
"v_cvt_pk_f16_f32 v6, -v38, v34\n"
"v_cvt_pk_f16_f32 v3, v35, v39\n"
"v_cvt_pk_f16_f32 v7, -v39, v35\n"
"v_mul_f32 v238, 0x3d800000, v238\n"
"v_mul_f32 v239, 0x3d800000, v239\n"
"v_cos_f32 v216, v238\n"
"v_sin_f32 v220, v238\n"
"v_add_f32 v240, v238, v239\n"
"v_cos_f32 v217, v240\n"
"v_sin_f32 v221, v240\n"
"v_add_f32 v240, v240, v239\n"
"v_cos_f32 v218, v240\n"
"v_sin_f32 v222, v240\n"
"v_add_f32 v240, v240, v239\n"
"v_cos_f32 v219, v240\n"
"v_sin_f32 v223, v240\n"
"v_mul_f32 v239, 0x3d800000, v109\n"
"v_mul_f32 v238, v110, v239\n"
"v_cos_f32 v32, v238\n"
"v_sin_f32 v36, v238\n"
"v_add_f32 v40, v238, v239\n"
"v_cos_f32 v33, v40\n"
"v_sin_f32 v37, v40\n"
"v_add_f32 v40, v40, v239\n"
"v_cos_f32 v34, v40\n"
"v_sin_f32 v38, v40\n"
"v_add_f32 v40, v40, v239\n"
"v_cos_f32 v35, v40\n"
"v_sin_f32 v39, v40\n"
"v_mul_f32 v32, 0x3d800000, v32\n"
"v_mul_f32 v36, 0x3d800000, v36\n"
"v_mul_f32 v33, 0x3d800000, v33\n"
"v_mul_f32 v37, 0x3d800000, v37\n"
"v_mul_f32 v34, 0x3d800000, v34\n"
"v_mul_f32 v38, 0x3d800000, v38\n"
"v_mul_f32 v35, 0x3d800000, v35\n"
"v_mul_f32 v39, 0x3d800000, v39\n"
"v_cvt_pk_f16_f32 v8, v32, v36\n"
"v_cvt_pk_f16_f32 v12, -v36, v32\n"
"v_cvt_pk_f16_f32 v9, v33, v37\n"
"v_cvt_pk_f16_f32 v13, -v37, v33\n"
"v_cvt_pk_f16_f32 v10, v34, v38\n"
"v_cvt_pk_f16_f32 v14, -v38, v34\n"
"v_cvt_pk_f16_f32 v11, v35, v39\n"
"v_cvt_pk_f16_f32 v15, -v39, v35\n"
"buffer_load_dwordx4 v[64:67], v118, s[72:75], s8 offen sc1\n"
"s_add_u32 s36, s8, 32768\n"
"buffer_load_dwordx4 v[68:71], v118, s[72:75], s36 offen sc1\n"
"s_add_u32 s36, s8, 65536\n"
"buffer_load_dwordx4 v[72:75], v118, s[72:75], s36 offen sc1\n"
"s_add_u32 s36, s8, 98304\n"
"buffer_load_dwordx4 v[76:79], v118, s[72:75], s36 offen sc1\n"
"v_xor_b32 v16, 0x80000000, v8\n"
"v_xor_b32 v20, 0x8000, v12\n"
"v_xor_b32 v17, 0x80000000, v9\n"
"v_xor_b32 v21, 0x8000, v13\n"
"v_xor_b32 v18, 0x80000000, v10\n"
"v_xor_b32 v22, 0x8000, v14\n"
"v_xor_b32 v19, 0x80000000, v11\n"
"v_xor_b32 v23, 0x8000, v15\n"
"v_fmamk_f32 v238, v110, 0x41000000, v232\n"
"v_mul_f32 v238, v109, v238\n"
"v_mul_f32 v238, 0x3c000000, v238\n"
"v_mul_f32 v239, 0x3d800000, v109\n"
"v_cos_f32 v32, v238\n"
"v_sin_f32 v36, v238\n"
"v_add_f32 v40, v238, v239\n"
"v_cos_f32 v33, v40\n"
"v_sin_f32 v37, v40\n"
"v_add_f32 v40, v40, v239\n"
"v_cos_f32 v34, v40\n"
"v_sin_f32 v38, v40\n"
"v_add_f32 v40, v40, v239\n"
"v_cos_f32 v35, v40\n"
"v_sin_f32 v39, v40\n"
"v_cvt_pk_f16_f32 v24, v32, -v36\n"
"v_cvt_pk_f16_f32 v28, v36, v32\n"
"v_cvt_pk_f16_f32 v25, v33, -v37\n"
"v_cvt_pk_f16_f32 v29, v37, v33\n"
"v_cvt_pk_f16_f32 v26, v34, -v38\n"
"v_cvt_pk_f16_f32 v30, v38, v34\n"
"v_cvt_pk_f16_f32 v27, v35, -v39\n"
"v_cvt_pk_f16_f32 v31, v39, v35\n"
"v_mul_f32 v238, 0x3d800000, v238\n"
"v_mul_f32 v239, 0x3d800000, v239\n"
"v_cos_f32 v224, v238\n"
"v_sin_f32 v228, v238\n"
"v_add_f32 v240, v238, v239\n"
"v_cos_f32 v225, v240\n"
"v_sin_f32 v229, v240\n"
"v_add_f32 v240, v240, v239\n"
"v_cos_f32 v226, v240\n"
"v_sin_f32 v230, v240\n"
"v_add_f32 v240, v240, v239\n"
"v_cos_f32 v227, v240\n"
"v_sin_f32 v231, v240\n"
"s_and_b32 s39, s14, 0x80000000\n"
"s_or_b32 s39, s39, s37\n"
"s_waitcnt vmcnt(3)\n"
"v_xor_b32 v64, s39, v64\n"
"v_xor_b32 v65, s39, v65\n"
"v_xor_b32 v66, s39, v66\n"
"v_xor_b32 v67, s39, v67\n"
"v_or3_b32 v238, v64, v65, v66\n"
"v_bitop3_b32 v238, v238, v67, v236 bitop3:0x54\n"
"v_cmp_ne_u32 s[28:29], 0, v238\n"
"s_waitcnt vmcnt(2)\n"
"v_xor_b32 v68, s39, v68\n"
"v_xor_b32 v69, s39, v69\n"
"v_xor_b32 v70, s39, v70\n"
"v_xor_b32 v71, s39, v71\n"
"v_or3_b32 v238, v68, v69, v70\n"
"v_bitop3_b32 v238, v238, v71, v236 bitop3:0x54\n"
"v_cmp_ne_u32 s[30:31], 0, v238\n"
"s_waitcnt vmcnt(1)\n"
"v_xor_b32 v72, s39, v72\n"
"v_xor_b32 v73, s39, v73\n"
"v_xor_b32 v74, s39, v74\n"
"v_xor_b32 v75, s39, v75\n"
"v_or3_b32 v238, v72, v73, v74\n"
"v_bitop3_b32 v238, v238, v75, v236 bitop3:0x54\n"
"v_cmp_ne_u32 s[32:33], 0, v238\n"
"s_waitcnt vmcnt(0)\n"
"v_xor_b32 v76, s39, v76\n"
"v_xor_b32 v77, s39, v77\n"
"v_xor_b32 v78, s39, v78\n"
"v_xor_b32 v79, s39, v79\n"
"v_or3_b32 v238, v76, v77, v78\n"
"v_bitop3_b32 v238, v238, v79, v236 bitop3:0x54\n"
"v_cmp_ne_u32 s[34:35], 0, v238\n"
"s_or_b64 vcc, s[28:29], s[30:31]\n"
"s_or_b64 vcc, vcc, s[32:33]\n"
"s_or_b64 vcc, vcc, s[34:35]\n"
"s_cbranch_scc1 Lslow_wx_K\n"
"Lret_wx_K:\n"
"v_cvt_f32_f16 v32, v64\n"
"v_cvt_f32_f16_sdwa v36, v64 dst_sel:DWORD dst_unused:UNUSED_PAD src0_sel:WORD_1\n"
"v_cvt_f32_f16 v33, v68\n"
"v_cvt_f32_f16_sdwa v37, v68 dst_sel:DWORD dst_unused:UNUSED_PAD src0_sel:WORD_1\n"
"v_cvt_f32_f16 v34, v72\n"
"v_cvt_f32_f16_sdwa v38, v72 dst_sel:DWORD dst_unused:UNUSED_PAD src0_sel:WORD_1\n"
"v_cvt_f32_f16 v35, v76\n"
"v_cvt_f32_f16_sdwa v39, v76 dst_sel:DWORD dst_unused:UNUSED_PAD src0_sel:WORD_1\n"
"v_cvt_f32_f16 v40, v65\n"
"v_cvt_f32_f16_sdwa v44, v65 dst_sel:DWORD dst_unused:UNUSED_PAD src0_sel:WORD_1\n"
"v_cvt_f32_f16 v41, v69\n"
"v_cvt_f32_f16_sdwa v45, v69 dst_sel:DWORD dst_unused:UNUSED_PAD src0_sel:WORD_1\n"
"v_cvt_f32_f16 v42, v73\n"
"v_cvt_f32_f16_sdwa v46, v73 dst_sel:DWORD dst_unused:UNUSED_PAD src0_sel:WORD_1\n"
"v_cvt_f32_f16 v43, v77\n"
"v_cvt_f32_f16_sdwa v47, v77 dst_sel:DWORD dst_unused:UNUSED_PAD src0_sel:WORD_1\n"
"v_cvt_f32_f16 v48, v66\n"
"v_cvt_f32_f16_sdwa v52, v66 dst_sel:DWORD dst_unused:UNUSED_PAD src0_sel:WORD_1\n"
"v_cvt_f32_f16 v49, v70\n"
"v_cvt_f32_f16_sdwa v53, v70 dst_sel:DWORD dst_unused:UNUSED_PAD src0_sel:WORD_1\n"
"v_cvt_f32_f16 v50, v74\n"
"v_cvt_f32_f16_sdwa v54, v74 dst_sel:DWORD dst_unused:UNUSED_PAD src0_sel:WORD_1\n"
"v_cvt_f32_f16 v51, v78\n"
"v_cvt_f32_f16_sdwa v55, v78 dst_sel:DWORD dst_unused:UNUSED_PAD src0_sel:WORD_1\n"
"v_cvt_f32_f16 v56, v67\n"
"v_cvt_f32_f16_sdwa v60, v67 dst_sel:DWORD dst_unused:UNUSED_PAD src0_sel:WORD_1\n"
"v_cvt_f32_f16 v57, v71\n"
"v_cvt_f32_f16_sdwa v61, v71 dst_sel:DWORD dst_unused:UNUSED_PAD src0_sel:WORD_1\n"
"v_cvt_f32_f16 v58, v75\n"
"v_cvt_f32_f16_sdwa v62, v75 dst_sel:DWORD dst_unused:UNUSED_PAD src0_sel:WORD_1\n"
"v_cvt_f32_f16 v59, v79\n"
"v_cvt_f32_f16_sdwa v63, v79 dst_sel:DWORD dst_unused:UNUSED_PAD src0_sel:WORD_1\n"
"s_cmp_eq_u32 s18, -1\n"
"s_cbranch_scc0 Lslow_nw_K\n"
"Lret_nw_K:\n"
"buffer_load_dwordx4 v[128:131], v100, s[72:75], s13 offen sc1\n"
"s_add_u32 s36, s13, 264192\n"
"buffer_load_dwordx4 v[132:135], v100, s[72:75], s36 offen sc1\n"
"s_add_u32 s36, s13, 528384\n"
"buffer_load_dwordx4 v[136:139], v100, s[72:75], s36 offen sc1\n"
"s_add_u32 s36, s13, 792576\n"
"buffer_load_dwordx4 v[140:143], v100, s[72:75], s36 offen sc1\n"
"v_bfe_u32 v233, v113, 6, 2\n"
"v_mul_u32_u24 v233, 24576, v233\n"
"v_and_or_b32 v233, v113, 48, v233\n"
"s_add_u32 s36, s101, 204800\n"
"buffer_load_dwordx4 v[208:211], v233, s[72:75], s36 offen sc1\n"
"s_mov_b32 s62, s94\n"
"s_waitcnt vmcnt(4)\n"
"v_xor_b32 v128, s37, v128\n"
"v_xor_b32 v129, s37, v129\n"
"v_xor_b32 v130, s37, v130\n"
"v_xor_b32 v131, s37, v131\n"
"v_or3_b32 v238, v128, v129, v130\n"
"v_bitop3_b32 v238, v238, v131, v236 bitop3:0x54\n"
"v_cmp_ne_u32 s[28:29], 0, v238\n"
"s_waitcnt vmcnt(3)\n"
"v_xor_b32 v132, s37, v132\n"
"v_xor_b32 v133, s37, v133\n"
"v_xor_b32 v134, s37, v134\n"
"v_xor_b32 v135, s37, v135\n"
"v_or3_b32 v238, v132, v133, v134\n"
"v_bitop3_b32 v238, v238, v135, v236 bitop3:0x54\n"
"v_cmp_ne_u32 s[30:31], 0, v238\n"
"s_waitcnt vmcnt(2)\n"
"v_xor_b32 v136, s37, v136\n"
"v_xor_b32 v137, s37, v137\n"
"v_xor_b32 v138, s37, v138\n"
"v_xor_b32 v139, s37, v139\n"
"v_or3_b32 v238, v136, v137, v138\n"
"v_bitop3_b32 v238, v238, v139, v236 bitop3:0x54\n"
"v_cmp_ne_u32 s[32:33], 0, v238\n"
"s_waitcnt vmcnt(1)\n"
"v_xor_b32 v140, s37, v140\n"
"v_xor_b32 v141, s37, v141\n"
"v_xor_b32 v142, s37, v142\n"
"v_xor_b32 v143, s37, v143\n"
"v_or3_b32 v238, v140, v141, v142\n"
"v_bitop3_b32 v238, v238, v143, v236 bitop3:0x54\n"
"v_cmp_ne_u32 s[34:35], 0, v238\n"
"s_or_b64 vcc, s[28:29], s[30:31]\n"
"s_or_b64 vcc, vcc, s[32:33]\n"
"s_or_b64 vcc, vcc, s[34:35]\n"
"s_cbranch_scc1 Lslow_i2_K\n"
"Lret_i2_K:\n"
"ds_write_b128 v241, v[128:131] offset:49152\n"
"ds_write_b128 v241, v[132:135] offset:50176\n"
"ds_write_b128 v241, v[136:139] offset:51200\n"
"ds_write_b128 v241, v[140:143] offset:52224\n"
"s_waitcnt lgkmcnt(0)\n"
"s_barrier\n"
"ds_read_b128 v[128:131], v101 offset:49152\n"
"ds_read_b128 v[132:135], v101 offset:50176\n"
"ds_read_b128 v[136:139], v101 offset:51200\n"
"ds_read_b128 v[140:143], v101 offset:52224\n"
"s_waitcnt vmcnt(0)\n"
"v_xor_b32 v208, s38, v208\n"
"v_xor_b32 v209, s38, v209\n"
"v_xor_b32 v210, s38, v210\n"
"v_xor_b32 v211, s38, v211\n"
"v_or3_b32 v238, v208, v209, v210\n"
"v_bitop3_b32 v238, v238, v211, v247 bitop3:0x54\n"
"v_cmp_ne_u32 vcc, 0, v238\n"
"s_lshr_b32 s28, s101, 12\n"
"s_cmp_eq_u32 s28, 2\n"
"s_cselect_b32 s29, 1.0, 0\n"
"s_cbranch_vccnz Lslow_hop_K\n"
"Lret_hop_K:\n"
"v_add_f32_dpp v208, v208, v208 row_ror:4 row_mask:0xf bank_mask:0xf\n"
"v_add_f32_dpp v209, v209, v209 row_ror:4 row_mask:0xf bank_mask:0xf\n"
"v_add_f32_dpp v210, v210, v210 row_ror:4 row_mask:0xf bank_mask:0xf\n"
"v_add_f32_dpp v211, v211, v211 row_ror:4 row_mask:0xf bank_mask:0xf\n"
"v_add_f32_dpp v208, v208, v208 row_ror:8 row_mask:0xf bank_mask:0xf\n"
"v_add_f32_dpp v209, v209, v209 row_ror:8 row_mask:0xf bank_mask:0xf\n"
"v_add_f32_dpp v210, v210, v210 row_ror:8 row_mask:0xf bank_mask:0xf\n"
"v_add_f32_dpp v211, v211, v211 row_ror:8 row_mask:0xf bank_mask:0xf\n"
"v_cvt_pk_bf16_f32 v208, 0, v208\n"
"v_cvt_pk_bf16_f32 v209, 0, v209\n"
"v_cvt_pk_bf16_f32 v210, 0, v210\n"
"v_cvt_pk_bf16_f32 v211, 0, v211\n"
"v_add_f32 v208, s29, v208\n"
"v_add_f32 v209, s29, v209\n"
"v_add_f32 v210, s29, v210\n"
"v_add_f32 v211, s29, v211\n"
"v_cmp_gt_u32 vcc, 64, v97\n"
"v_mov_b32 v239, 0x8000000\n"
"s_add_u32 s28, s101, 294912\n"
"v_cvt_pk_bf16_f32 v208, 0, v208\n"
"v_cvt_pk_bf16_f32 v209, 0, v209\n"
"v_cvt_pk_bf16_f32 v210, 0, v210\n"
"v_cvt_pk_bf16_f32 v211, 0, v211\n"
"v_cndmask_b32 v239, v239, v97, vcc\n"
"v_and_or_b32 v208, v117, v237, v208\n"
"v_and_or_b32 v209, v117, v237, v209\n"
"v_and_or_b32 v210, v117, v237, v210\n"
"v_and_or_b32 v211, v117, v237, v211\n"
"buffer_store_dwordx4 v[208:211], v239, s[72:75], s28 offen sc1\n"
"s_waitcnt lgkmcnt(0)\n"
"s_cmp_lg_u32 s18, -1\n"
"s_cbranch_scc1 Lk2five_K\n"
"s_add_u32 m0, s44, 65536\n"
"s_nop 0\n"
"buffer_load_dwordx4 v122, s[76:79], s42 offen offset:0 lds\n"
"v_mov_b32 v144, v128\n"
"v_mov_b32 v145, v132\n"
"v_mov_b32 v146, v136\n"
"v_mov_b32 v147, v140\n"
"s_nop 1\n"
"v_mfma_f32_16x16x32_f16 v[64:67], v[144:147], v[0:3], 0\n"
"v_mov_b32 v148, v129\n"
"v_mov_b32 v149, v133\n"
"v_mfma_f32_16x16x32_f16 v[68:71], v[144:147], v[4:7], 0\n"
"v_mov_b32 v150, v137\n"
"v_mov_b32 v151, v141\n"
"s_nop 1\n"
"v_mfma_f32_16x16x32_f16 v[72:75], v[148:151], v[0:3], 0\n"
"v_mov_b32 v152, v130\n"
"v_mov_b32 v153, v134\n"
"v_mfma_f32_16x16x32_f16 v[76:79], v[148:151], v[4:7], 0\n"
"buffer_load_dwordx4 v123, s[76:79], s42 offen offset:1024 lds\n"
"v_mov_b32 v154, v138\n"
"v_mov_b32 v155, v142\n"
"s_nop 1\n"
"v_mfma_f32_16x16x32_f16 v[80:83], v[152:155], v[0:3], 0\n"
"v_mov_b32 v156, v131\n"
"v_mov_b32 v157, v135\n"
"v_mfma_f32_16x16x32_f16 v[84:87], v[152:155], v[4:7], 0\n"
"v_mov_b32 v158, v139\n"
"v_mov_b32 v159, v143\n"
"s_nop 1\n"
"v_mfma_f32_16x16x32_f16 v[88:91], v[156:159], v[0:3], 0\n"
"v_mfma_f32_16x16x32_f16 v[92:95], v[156:159], v[4:7], 0\n"
"v_pk_mul_f32 v[144:145], v[64:65], v[216:217]\n"
"v_pk_mul_f32 v[146:147], v[66:67], v[218:219]\n"
"v_pk_fma_f32 v[144:145], v[68:69], v[220:221], v[144:145]\n"
"v_pk_fma_f32 v[146:147], v[70:71], v[222:223], v[146:147]\n"
"buffer_load_dwordx4 v124, s[76:79], s42 offen offset:2048 lds\n"
"v_pk_mul_f32 v[68:69], v[68:69], v[216:217]\n"
"v_pk_mul_f32 v[70:71], v[70:71], v[218:219]\n"
"v_pk_fma_f32 v[68:69], v[64:65], v[220:221], v[68:69] neg_lo:[1,0,0] neg_hi:[1,0,0]\n"
"v_pk_fma_f32 v[70:71], v[66:67], v[222:223], v[70:71] neg_lo:[1,0,0] neg_hi:[1,0,0]\n"
"v_pk_mul_f32 v[148:149], v[72:73], v[216:217]\n"
"v_pk_mul_f32 v[150:151], v[74:75], v[218:219]\n"
"v_pk_fma_f32 v[148:149], v[76:77], v[220:221], v[148:149]\n"
"v_pk_fma_f32 v[150:151], v[78:79], v[222:223], v[150:151]\n"
"v_pk_mul_f32 v[76:77], v[76:77], v[216:217]\n"
"v_pk_mul_f32 v[78:79], v[78:79], v[218:219]\n"
"v_pk_fma_f32 v[76:77], v[72:73], v[220:221], v[76:77] neg_lo:[1,0,0] neg_hi:[1,0,0]\n"
"v_pk_fma_f32 v[78:79], v[74:75], v[222:223], v[78:79] neg_lo:[1,0,0] neg_hi:[1,0,0]\n"
"v_pk_mul_f32 v[152:153], v[80:81], v[216:217]\n"
"v_pk_mul_f32 v[154:155], v[82:83], v[218:219]\n"
"v_pk_fma_f32 v[152:153], v[84:85], v[220:221], v[152:153]\n"
"v_pk_fma_f32 v[154:155], v[86:87], v[222:223], v[154:155]\n"
"v_pk_mul_f32 v[84:85], v[84:85], v[216:217]\n"
"v_pk_mul_f32 v[86:87], v[86:87], v[218:219]\n"
"v_pk_fma_f32 v[84:85], v[80:81], v[220:221], v[84:85] neg_lo:[1,0,0] neg_hi:[1,0,0]\n"
"v_pk_fma_f32 v[86:87], v[82:83], v[222:223], v[86:87] neg_lo:[1,0,0] neg_hi:[1,0,0]\n"
"buffer_load_dwordx4 v125, s[76:79], s42 offen offset:3072 lds\n"
"s_add_u32 s42, s42, 458752\n"
"v_pk_mul_f32 v[156:157], v[88:89], v[216:217]\n"
"v_pk_mul_f32 v[158:159], v[90:91], v[218:219]\n"
"v_pk_fma_f32 v[156:157], v[92:93], v[220:221], v[156:157]\n"
"v_pk_fma_f32 v[158:159], v[94:95], v[222:223], v[158:159]\n"
"v_pk_mul_f32 v[92:93], v[92:93], v[216:217]\n"
"v_pk_mul_f32 v[94:95], v[94:95], v[218:219]\n"
"v_pk_fma_f32 v[92:93], v[88:89], v[220:221], v[92:93] neg_lo:[1,0,0] neg_hi:[1,0,0]\n"
"v_pk_fma_f32 v[94:95], v[90:91], v[222:223], v[94:95] neg_lo:[1,0,0] neg_hi:[1,0,0]\n"
"v_cvt_pk_f16_f32 v144, v144, v68\n"
"v_cvt_pk_f16_f32 v145, v145, v69\n"
"v_cvt_pk_f16_f32 v146, v146, v70\n"
"v_cvt_pk_f16_f32 v147, v147, v71\n"
"s_nop 1\n"
"v_mfma_f32_16x16x32_f16 v[64:67], v[8:11], v[144:147], 0\n"
"v_cvt_pk_f16_f32 v148, v148, v76\n"
"v_cvt_pk_f16_f32 v149, v149, v77\n"
"v_mfma_f32_16x16x32_f16 v[68:71], v[12:15], v[144:147], 0\n"
"v_cvt_pk_f16_f32 v150, v150, v78\n"
"v_cvt_pk_f16_f32 v151, v151, v79\n"
"s_add_u32 m0, s44, 81920\n"
"s_nop 0\n"
"buffer_load_dwordx4 v122, s[76:79], s42 offen offset:0 lds\n"
"v_mfma_f32_16x16x32_f16 v[72:75], v[8:11], v[148:151], 0\n"
"v_cvt_pk_f16_f32 v152, v152, v84\n"
"v_cvt_pk_f16_f32 v153, v153, v85\n"
"v_mfma_f32_16x16x32_f16 v[76:79], v[12:15], v[148:151], 0\n"
"v_cvt_pk_f16_f32 v154, v154, v86\n"
"v_cvt_pk_f16_f32 v155, v155, v87\n"
"s_nop 1\n"
"v_mfma_f32_16x16x32_f16 v[80:83], v[8:11], v[152:155], 0\n"
"v_cvt_pk_f16_f32 v156, v156, v92\n"
"v_cvt_pk_f16_f32 v157, v157, v93\n"
"v_mfma_f32_16x16x32_f16 v[84:87], v[12:15], v[152:155], 0\n"
"v_cvt_pk_f16_f32 v158, v158, v94\n"
"v_cvt_pk_f16_f32 v159, v159, v95\n"
"s_nop 1\n"
"v_mfma_f32_16x16x32_f16 v[88:91], v[8:11], v[156:159], 0\n"
"buffer_load_dwordx4 v123, s[76:79], s42 offen offset:1024 lds\n"
"v_mfma_f32_16x16x32_f16 v[92:95], v[12:15], v[156:159], 0\n"
"v_pk_mul_f32 v[144:145], v[64:65], v[32:33]\n"
"v_pk_mul_f32 v[146:147], v[66:67], v[34:35]\n"
"v_pk_fma_f32 v[144:145], v[68:69], v[36:37], v[144:145] neg_lo:[1,0,0] neg_hi:[1,0,0]\n"
"v_pk_fma_f32 v[146:147], v[70:71], v[38:39], v[146:147] neg_lo:[1,0,0] neg_hi:[1,0,0]\n"
"v_pk_mul_f32 v[68:69], v[68:69], v[32:33]\n"
"v_pk_mul_f32 v[70:71], v[70:71], v[34:35]\n"
"v_pk_fma_f32 v[68:69], v[64:65], v[36:37], v[68:69]\n"
"v_pk_fma_f32 v[70:71], v[66:67], v[38:39], v[70:71]\n"
"v_pk_mul_f32 v[148:149], v[72:73], v[40:41]\n"
"v_pk_mul_f32 v[150:151], v[74:75], v[42:43]\n"
"v_pk_fma_f32 v[148:149], v[76:77], v[44:45], v[148:149] neg_lo:[1,0,0] neg_hi:[1,0,0]\n"
"v_pk_fma_f32 v[150:151], v[78:79], v[46:47], v[150:151] neg_lo:[1,0,0] neg_hi:[1,0,0]\n"
"v_pk_mul_f32 v[76:77], v[76:77], v[40:41]\n"
"v_pk_mul_f32 v[78:79], v[78:79], v[42:43]\n"
"v_pk_fma_f32 v[76:77], v[72:73], v[44:45], v[76:77]\n"
"v_pk_fma_f32 v[78:79], v[74:75], v[46:47], v[78:79]\n"
"buffer_load_dwordx4 v124, s[76:79], s42 offen offset:2048 lds\n"
"v_pk_mul_f32 v[152:153], v[80:81], v[48:49]\n"
"v_pk_mul_f32 v[154:155], v[82:83], v[50:51]\n"
"v_pk_fma_f32 v[152:153], v[84:85], v[52:53], v[152:153] neg_lo:[1,0,0] neg_hi:[1,0,0]\n"
"v_pk_fma_f32 v[154:155], v[86:87], v[54:55], v[154:155] neg_lo:[1,0,0] neg_hi:[1,0,0]\n"
"v_pk_mul_f32 v[84:85], v[84:85], v[48:49]\n"
"v_pk_mul_f32 v[86:87], v[86:87], v[50:51]\n"
"v_pk_fma_f32 v[84:85], v[80:81], v[52:53], v[84:85]\n"
"v_pk_fma_f32 v[86:87], v[82:83], v[54:55], v[86:87]\n"
"v_pk_mul_f32 v[156:157], v[88:89], v[56:57]\n"
"v_pk_mul_f32 v[158:159], v[90:91], v[58:59]\n"
"v_pk_fma_f32 v[156:157], v[92:93], v[60:61], v[156:157] neg_lo:[1,0,0] neg_hi:[1,0,0]\n"
"v_pk_fma_f32 v[158:159], v[94:95], v[62:63], v[158:159] neg_lo:[1,0,0] neg_hi:[1,0,0]\n"
"v_pk_mul_f32 v[92:93], v[92:93], v[56:57]\n"
"v_pk_mul_f32 v[94:95], v[94:95], v[58:59]\n"
"v_pk_fma_f32 v[92:93], v[88:89], v[60:61], v[92:93]\n"
"v_pk_fma_f32 v[94:95], v[90:91], v[62:63], v[94:95]\n"
"v_cvt_pk_f16_f32 v144, v144, v68\n"
"v_cvt_pk_f16_f32 v145, v145, v69\n"
"v_cvt_pk_f16_f32 v146, v146, v70\n"
"v_cvt_pk_f16_f32 v147, v147, v71\n"
"s_nop 1\n"
"v_mfma_f32_16x16x32_f16 v[64:67], v[144:147], v[16:19], 0\n"
"buffer_load_dwordx4 v125, s[76:79], s42 offen offset:3072 lds\n"
"s_add_u32 s42, s42, 458752\n"
"v_cvt_pk_f16_f32 v148, v148, v76\n"
"v_cvt_pk_f16_f32 v149, v149, v77\n"
"v_mfma_f32_16x16x32_f16 v[68:71], v[144:147], v[20:23], 0\n"
"v_cvt_pk_f16_f32 v150, v150, v78\n"
"v_cvt_pk_f16_f32 v151, v151, v79\n"
"s_nop 1\n"
"v_mfma_f32_16x16x32_f16 v[72:75], v[148:151], v[16:19], 0\n"
"v_cvt_pk_f16_f32 v152, v152, v84\n"
"v_cvt_pk_f16_f32 v153, v153, v85\n"
"v_mfma_f32_16x16x32_f16 v[76:79], v[148:151], v[20:23], 0\n"
"v_cvt_pk_f16_f32 v154, v154, v86\n"
"v_cvt_pk_f16_f32 v155, v155, v87\n"
"s_nop 1\n"
"v_mfma_f32_16x16x32_f16 v[80:83], v[152:155], v[16:19], 0\n"
"s_add_u32 m0, s44, 98304\n"
"s_nop 0\n"
"buffer_load_dwordx4 v122, s[76:79], s42 offen offset:0 lds\n"
"v_cvt_pk_f16_f32 v156, v156, v92\n"
"v_cvt_pk_f16_f32 v157, v157, v93\n"
"v_mfma_f32_16x16x32_f16 v[84:87], v[152:155], v[20:23], 0\n"
"v_cvt_pk_f16_f32 v158, v158, v94\n"
"v_cvt_pk_f16_f32 v159, v159, v95\n"
"s_nop 1\n"
"v_mfma_f32_16x16x32_f16 v[88:91], v[156:159], v[16:19], 0\n"
"v_mfma_f32_16x16x32_f16 v[92:95], v[156:159], v[20:23], 0\n"
"v_pk_mul_f32 v[144:145], v[64:65], v[224:225]\n"
"v_pk_mul_f32 v[146:147], v[66:67], v[226:227]\n"
"v_pk_fma_f32 v[144:145], v[68:69], v[228:229], v[144:145] neg_lo:[1,0,0] neg_hi:[1,0,0]\n"
"v_pk_fma_f32 v[146:147], v[70:71], v[230:231], v[146:147] neg_lo:[1,0,0] neg_hi:[1,0,0]\n"
"v_pk_mul_f32 v[68:69], v[68:69], v[224:225]\n"
"v_pk_mul_f32 v[70:71], v[70:71], v[226:227]\n"
"v_pk_fma_f32 v[68:69], v[64:65], v[228:229], v[68:69]\n"
"v_pk_fma_f32 v[70:71], v[66:67], v[230:231], v[70:71]\n"
"buffer_load_dwordx4 v123, s[76:79], s42 offen offset:1024 lds\n"
"v_pk_mul_f32 v[148:149], v[72:73], v[224:225]\n"
"v_pk_mul_f32 v[150:151], v[74:75], v[226:227]\n"
"v_pk_fma_f32 v[148:149], v[76:77], v[228:229], v[148:149] neg_lo:[1,0,0] neg_hi:[1,0,0]\n"
"v_pk_fma_f32 v[150:151], v[78:79], v[230:231], v[150:151] neg_lo:[1,0,0] neg_hi:[1,0,0]\n"
"v_pk_mul_f32 v[76:77], v[76:77], v[224:225]\n"
"v_pk_mul_f32 v[78:79], v[78:79], v[226:227]\n"
"v_pk_fma_f32 v[76:77], v[72:73], v[228:229], v[76:77]\n"
"v_pk_fma_f32 v[78:79], v[74:75], v[230:231], v[78:79]\n"
"v_pk_mul_f32 v[152:153], v[80:81], v[224:225]\n"
"v_pk_mul_f32 v[154:155], v[82:83], v[226:227]\n"
"v_pk_fma_f32 v[152:153], v[84:85], v[228:229], v[152:153] neg_lo:[1,0,0] neg_hi:[1,0,0]\n"
"v_pk_fma_f32 v[154:155], v[86:87], v[230:231], v[154:155] neg_lo:[1,0,0] neg_hi:[1,0,0]\n"
"v_pk_mul_f32 v[84:85], v[84:85], v[224:225]\n"
"v_pk_mul_f32 v[86:87], v[86:87], v[226:227]\n"
"v_pk_fma_f32 v[84:85], v[80:81], v[228:229], v[84:85]\n"
"v_pk_fma_f32 v[86:87], v[82:83], v[230:231], v[86:87]\n"
"v_pk_mul_f32 v[156:157], v[88:89], v[224:225]\n"
"v_pk_mul_f32 v[158:159], v[90:91], v[226:227]\n"
"v_pk_fma_f32 v[156:157], v[92:93], v[228:229], v[156:157] neg_lo:[1,0,0] neg_hi:[1,0,0]\n"
"v_pk_fma_f32 v[158:159], v[94:95], v[230:231], v[158:159] neg_lo:[1,0,0] neg_hi:[1,0,0]\n"
"v_pk_mul_f32 v[92:93], v[92:93], v[224:225]\n"
"buffer_load_dwordx4 v124, s[76:79], s42 offen offset:2048 lds\n"
"v_pk_mul_f32 v[94:95], v[94:95], v[226:227]\n"
"v_pk_fma_f32 v[92:93], v[88:89], v[228:229], v[92:93]\n"
"v_pk_fma_f32 v[94:95], v[90:91], v[230:231], v[94:95]\n"
"v_cvt_pk_f16_f32 v144, v144, v68\n"
"v_cvt_pk_f16_f32 v145, v145, v69\n"
"v_cvt_pk_f16_f32 v146, v146, v70\n"
"v_cvt_pk_f16_f32 v147, v147, v71\n"
"s_nop 1\n"
"v_mfma_f32_16x16x32_f16 v[64:67], v[24:27], v[144:147], 0\n"
"v_cvt_pk_f16_f32 v148, v148, v76\n"
"v_cvt_pk_f16_f32 v149, v149, v77\n"
"v_mfma_f32_16x16x32_f16 v[68:71], v[28:31], v[144:147], 0\n"
"v_cvt_pk_f16_f32 v150, v150, v78\n"
"v_cvt_pk_f16_f32 v151, v151, v79\n"
"s_nop 1\n"
"v_mfma_f32_16x16x32_f16 v[72:75], v[24:27], v[148:151], 0\n"
"v_cvt_pk_f16_f32 v152, v152, v84\n"
"v_cvt_pk_f16_f32 v153, v153, v85\n"
"buffer_load_dwordx4 v125, s[76:79], s42 offen offset:3072 lds\n"
"s_add_u32 s42, s42, 458752\n"
"v_mfma_f32_16x16x32_f16 v[76:79], v[28:31], v[148:151], 0\n"
"v_cvt_pk_f16_f32 v154, v154, v86\n"
"v_cvt_pk_f16_f32 v155, v155, v87\n"
"s_nop 1\n"
"v_mfma_f32_16x16x32_f16 v[80:83], v[24:27], v[152:155], 0\n"
"v_cvt_pk_f16_f32 v156, v156, v92\n"
"v_cvt_pk_f16_f32 v157, v157, v93\n"
"v_mfma_f32_16x16x32_f16 v[84:87], v[28:31], v[152:155], 0\n"
"v_cvt_pk_f16_f32 v158, v158, v94\n"
"v_cvt_pk_f16_f32 v159, v159, v95\n"
"s_nop 1\n"
"v_mfma_f32_16x16x32_f16 v[88:91], v[24:27], v[156:159], 0\n"
"v_mfma_f32_16x16x32_f16 v[92:95], v[28:31], v[156:159], 0\n"
"v_cvt_pk_f16_f32 v129, v72, v76\n"
"s_nop 0\n"
"v_cvt_pk_f16_f32 v130, v80, v84\n"
"s_nop 4\n"
"v_cvt_pk_f16_f32 v131, v88, v92\n"
"v_cvt_pk_f16_f32 v128, v64, v68\n"
"v_and_or_b32 v128, v128, v236, s37\n"
"v_and_or_b32 v129, v129, v236, s37\n"
"v_and_or_b32 v130, v130, v236, s37\n"
"v_and_or_b32 v131, v131, v236, s37\n"
"ds_write_b128 v101, v[128:131] offset:0\n"
"v_cvt_pk_f16_f32 v133, v73, v77\n"
"v_cvt_pk_f16_f32 v134, v81, v85\n"
"v_cvt_pk_f16_f32 v135, v89, v93\n"
"v_cvt_pk_f16_f32 v132, v65, v69\n"
"v_and_or_b32 v132, v132, v236, s37\n"
"v_and_or_b32 v133, v133, v236, s37\n"
"v_and_or_b32 v134, v134, v236, s37\n"
"v_and_or_b32 v135, v135, v236, s37\n"
"ds_write_b128 v101, v[132:135] offset:1024\n"
"v_cvt_pk_f16_f32 v137, v74, v78\n"
"v_cvt_pk_f16_f32 v138, v82, v86\n"
"v_cvt_pk_f16_f32 v139, v90, v94\n"
"v_cvt_pk_f16_f32 v136, v66, v70\n"
"v_and_or_b32 v136, v136, v236, s37\n"
"v_and_or_b32 v137, v137, v236, s37\n"
"v_and_or_b32 v138, v138, v236, s37\n"
"v_and_or_b32 v139, v139, v236, s37\n"
"ds_write_b128 v101, v[136:139] offset:2048\n"
"v_cvt_pk_f16_f32 v141, v75, v79\n"
"v_cvt_pk_f16_f32 v142, v83, v87\n"
"v_cvt_pk_f16_f32 v143, v91, v95\n"
"v_cvt_pk_f16_f32 v140, v67, v71\n"
"v_and_or_b32 v140, v140, v236, s37\n"
"v_and_or_b32 v141, v141, v236, s37\n"
"v_and_or_b32 v142, v142, v236, s37\n"
"v_and_or_b32 v143, v143, v236, s37\n"
"ds_write_b128 v101, v[140:143] offset:3072\n"
"s_waitcnt lgkmcnt(0)\n"
"Lk2done_K:\n"
"s_barrier\n"
"ds_read_b128 v[144:147], v241 offset:0\n"
"ds_read_b128 v[148:151], v241 offset:1024\n"
"ds_read_b128 v[152:155], v241 offset:2048\n"
"ds_read_b128 v[156:159], v241 offset:3072\n"
"s_waitcnt lgkmcnt(3)\n"
"buffer_store_dwordx4 v[144:147], v102, s[72:75], s15 offen offset:0 sc1\n"
"s_waitcnt lgkmcnt(2)\n"
"buffer_store_dwordx4 v[148:151], v102, s[72:75], s15 offen offset:1024 sc1\n"
"s_waitcnt lgkmcnt(1)\n"
"buffer_store_dwordx4 v[152:155], v102, s[72:75], s15 offen offset:2048 sc1\n"
"s_waitcnt lgkmcnt(0)\n"
"buffer_store_dwordx4 v[156:159], v102, s[72:75], s15 offen offset:3072 sc1\n"
"s_add_u32 m0, s44, 114688\n"
"s_nop 0\n"
"buffer_load_dwordx4 v122, s[76:79], s42 offen offset:0 lds\n"
"v_mul_f32 v239, 0x3d000000, v243\n"
"v_mul_f32 v238, v110, v239\n"
"v_cos_f32 v76, v238\n"
"v_sin_f32 v80, v238\n"
"v_add_f32 v84, v238, v239\n"
"v_cos_f32 v77, v84\n"
"v_sin_f32 v81, v84\n"
"v_add_f32 v84, v84, v239\n"
"v_cos_f32 v78, v84\n"
"buffer_load_dwordx4 v123, s[76:79], s42 offen offset:1024 lds\n"
"v_sin_f32 v82, v84\n"
"v_add_f32 v84, v84, v239\n"
"v_cos_f32 v79, v84\n"
"v_sin_f32 v83, v84\n"
"v_cvt_pk_f16_f32 v176, v76, -v80\n"
"v_cvt_pk_f16_f32 v180, v80, v76\n"
"v_cvt_pk_f16_f32 v177, v77, -v81\n"
"v_cvt_pk_f16_f32 v181, v81, v77\n"
"buffer_load_dwordx4 v124, s[76:79], s42 offen offset:2048 lds\n"
"v_cvt_pk_f16_f32 v178, v78, -v82\n"
"v_cvt_pk_f16_f32 v182, v82, v78\n"
"v_cvt_pk_f16_f32 v179, v79, -v83\n"
"v_cvt_pk_f16_f32 v183, v83, v79\n"
"v_xor_b32 v184, v244, v176\n"
"v_xor_b32 v185, v244, v177\n"
"v_xor_b32 v186, v244, v178\n"
"v_xor_b32 v187, v244, v179\n"
"v_xor_b32 v188, v244, v180\n"
"v_xor_b32 v189, v244, v181\n"
"buffer_load_dwordx4 v125, s[76:79], s42 offen offset:3072 lds\n"
"s_add_u32 s42, s42, 458752\n"
"v_xor_b32 v190, v244, v182\n"
"v_xor_b32 v191, v244, v183\n"
"v_mul_f32 v239, 0x3d000000, v109\n"
"v_mul_f32 v238, v110, v239\n"
"v_cos_f32 v76, v238\n"
"v_sin_f32 v80, v238\n"
"v_add_f32 v84, v238, v239\n"
"v_cos_f32 v77, v84\n"
"v_sin_f32 v81, v84\n"
"s_add_u32 m0, s44, 131072\n"
"s_nop 0\n"
"buffer_load_dwordx4 v122, s[76:79], s42 offen offset:0 lds\n"
"v_add_f32 v84, v84, v239\n"
"v_cos_f32 v78, v84\n"
"v_sin_f32 v82, v84\n"
"v_add_f32 v84, v84, v239\n"
"v_cos_f32 v79, v84\n"
"v_sin_f32 v83, v84\n"
"v_cvt_pk_f16_f32 v0, v76, -v80\n"
"buffer_load_dwordx4 v123, s[76:79], s42 offen offset:1024 lds\n"
"v_cvt_pk_f16_f32 v4, v80, v76\n"
"v_cvt_pk_f16_f32 v1, v77, -v81\n"
"v_cvt_pk_f16_f32 v5, v81, v77\n"
"v_cvt_pk_f16_f32 v2, v78, -v82\n"
"v_cvt_pk_f16_f32 v6, v82, v78\n"
"v_cvt_pk_f16_f32 v3, v79, -v83\n"
"v_cvt_pk_f16_f32 v7, v83, v79\n"
"v_xor_b32 v8, v244, v0\n"
"v_xor_b32 v9, v244, v1\n"
"v_xor_b32 v10, v244, v2\n"
"v_xor_b32 v11, v244, v3\n"
"buffer_load_dwordx4 v124, s[76:79], s42 offen offset:2048 lds\n"
"v_xor_b32 v12, v244, v4\n"
"v_xor_b32 v13, v244, v5\n"
"v_xor_b32 v14, v244, v6\n"
"v_xor_b32 v15, v244, v7\n"
"v_mov_b32 v16, v0\n"
"v_xor_b32 v17, s24, v1\n"
"v_mov_b32 v18, v2\n"
"v_xor_b32 v19, s24, v3\n"
"v_mov_b32 v20, v4\n"
"v_xor_b32 v21, s24, v5\n"
"buffer_load_dwordx4 v125, s[76:79], s42 offen offset:3072 lds\n"
"s_add_u32 s42, s42, 458752\n"
"v_mov_b32 v22, v6\n"
"v_xor_b32 v23, s24, v7\n"
"v_xor_b32 v24, v244, v0\n"
"v_bitop3_b32 v25, v1, v244, s24 bitop3:0x96\n"
"v_xor_b32 v26, v244, v2\n"
"v_bitop3_b32 v27, v3, v244, s24 bitop3:0x96\n"
"v_xor_b32 v28, v244, v4\n"
"v_bitop3_b32 v29, v5, v244, s24 bitop3:0x96\n"
"v_xor_b32 v30, v244, v6\n"
"v_bitop3_b32 v31, v7, v244, s24 bitop3:0x96\n"
"s_mov_b32 s36, 294912\n"
"buffer_load_dwordx4 v[64:67], v97, s[72:75], s36 offen sc1\n"
"s_mov_b32 s36, 299008\n"
"buffer_load_dwordx4 v[68:71], v97, s[72:75], s36 offen sc1\n"
"s_mov_b32 s36, 303104\n"
"buffer_load_dwordx4 v[72:75], v97, s[72:75], s36 offen sc1\n"
"s_lshr_b32 s0, s101, 6\n"
"s_lshr_b32 s1, s0, 6\n"
"s_and_b32 s2, s0, 7\n"
"s_bfe_u32 s3, s0, 0x30003\n"
"s_and_b32 s7, s0, 63\n"
"s_mul_i32 s4, s7, 37\n"
"s_lshr_b32 s4, s4, 8\n"
"s_sub_u32 s5, s1, s4\n"
"s_mul_i32 s5, s5, 7\n"
"s_add_u32 s5, s5, s7\n"
"s_cmp_lt_u32 s7, 63\n"
"s_cselect_b32 s66, 4194304, 0\n"
"s_cselect_b32 s70, 14680064, 0\n"
"s_lshl_b32 s6, s1, 3\n"
"s_add_u32 s6, s6, s3\n"
"s_lshl_b32 s40, s4, 15\n"
"s_add_u32 s40, s40, s44\n"
"s_lshr_b32 s7, s44, 2\n"
"s_mul_i32 s41, s4, 114688\n"
"s_add_u32 s41, s41, s7\n"
"s_lshl_b32 s92, s5, 12\n"
"s_add_u32 s41, s41, s92\n"
"s_mul_i32 s43, s1, 1835008\n"
"s_lshl_b32 s92, s44, 3\n"
"s_add_u32 s43, s43, s92\n"
"s_lshl_b32 s2, s2, 8\n"
"s_add_u32 s43, s43, s2\n"
"s_mul_i32 s45, s7, 3\n"
"s_add_u32 s45, s45, 16384\n"
"s_lshl_b32 s92, s0, 12\n"
"s_add_u32 s92, s92, s7\n"
"s_add_u32 s46, s92, 41943040\n"
"s_add_u32 s55, s92, 262144\n"
"s_and_b32 s55, s55, 0xfffff\n"
"s_add_u32 s55, s55, 41943040\n"
"s_add_u32 s56, s92, 524288\n"
"s_and_b32 s56, s56, 0xfffff\n"
"s_add_u32 s56, s56, 42991616\n"
"s_add_u32 s57, s92, 786432\n"
"s_and_b32 s57, s57, 0xfffff\n"
"s_add_u32 s57, s57, 44040192\n"
"s_lshl_b32 s48, s6, 12\n"
"s_lshl_b32 s47, s6, 15\n"
"s_add_u32 s47, s47, 20971520\n"
"s_cmp_eq_u32 s21, 0\n"
"s_cselect_b32 s49, 64, 0\n"
"s_mul_i32 s92, s1, 28672\n"
"s_add_u32 s92, s92, 25165824\n"
"s_add_u32 s51, s6, 8\n"
"s_and_b32 s51, s51, 31\n"
"s_mul_i32 s51, s51, 114688\n"
"s_add_u32 s51, s51, s92\n"
"s_add_u32 s52, s6, 16\n"
"s_and_b32 s52, s52, 31\n"
"s_mul_i32 s52, s52, 114688\n"
"s_add_u32 s52, s52, s92\n"
"s_add_u32 s53, s6, 24\n"
"s_and_b32 s53, s53, 31\n"
"s_mul_i32 s53, s53, 114688\n"
"s_add_u32 s53, s53, s92\n"
"s_add_u32 s54, s6, 32\n"
"s_and_b32 s54, s54, 31\n"
"s_mul_i32 s54, s54, 114688\n"
"s_add_u32 s54, s54, s92\n"
"s_add_u32 s64, s72, 16777216\n"
"s_addc_u32 s65, s73, 0\n"
"s_mov_b32 s67, s75\n"
"s_add_u32 s68, s72, 25165824\n"
"s_addc_u32 s69, s73, 0\n"
"s_mov_b32 s71, s75\n"
"s_mov_b32 s86, 4194304\n"
"s_mov_b32 s87, s75\n"
"s_lshl_b32 s92, s21, 6\n"
"s_add_u32 s2, s2, s92\n"
"s_lshl_b32 s5, s5, 8\n"
"s_add_u32 s5, s5, s92\n"
"s_lshl_b32 s3, s3, 1\n"
"s_lshl_b32 s7, s1, 10\n"
"s_add_u32 s7, s7, 131072\n"
"s_waitcnt vmcnt(3)\n"
"s_waitcnt lgkmcnt(0)\n"
"s_barrier\n"
"ds_read_b64_tr_b16 a[0:1], v120 offset:0\n"
"v_lshlrev_b32 v32, 16, v160\n"
"ds_read_b64_tr_b16 a[2:3], v120 offset:1024\n"
"v_and_b32 v33, s25, v160\n"
"ds_read_b64_tr_b16 a[4:5], v120 offset:8\n"
"v_lshlrev_b32 v34, 16, v161\n"
"ds_read_b64_tr_b16 a[6:7], v120 offset:1032\n"
"v_and_b32 v35, s25, v161\n"
"ds_read_b64_tr_b16 a[8:9], v120 offset:8192\n"
"v_lshlrev_b32 v36, 16, v162\n"
"ds_read_b64_tr_b16 a[10:11], v120 offset:9216\n"
"v_and_b32 v37, s25, v162\n"
"ds_read_b64_tr_b16 a[12:13], v120 offset:8200\n"
"v_lshlrev_b32 v38, 16, v163\n"
"ds_read_b64_tr_b16 a[14:15], v120 offset:9224\n"
"ds_read_b64_tr_b16 a[16:17], v120 offset:16384\n"
"v_and_b32 v39, s25, v163\n"
"ds_read_b64_tr_b16 a[18:19], v120 offset:17408\n"
"v_lshlrev_b32 v40, 16, v164\n"
"ds_read_b64_tr_b16 a[20:21], v120 offset:16392\n"
"v_and_b32 v41, s25, v164\n"
"ds_read_b64_tr_b16 a[22:23], v120 offset:17416\n"
"v_lshlrev_b32 v42, 16, v165\n"
"ds_read_b64_tr_b16 a[24:25], v120 offset:24576\n"
"v_and_b32 v43, s25, v165\n"
"ds_read_b64_tr_b16 a[26:27], v120 offset:25600\n"
"v_lshlrev_b32 v44, 16, v166\n"
"ds_read_b64_tr_b16 a[28:29], v120 offset:24584\n"
"v_and_b32 v45, s25, v166\n"
"s_waitcnt lgkmcnt(11)\n"
"ds_read_b64_tr_b16 a[30:31], v120 offset:25608\n"
"ds_read_b64_tr_b16 a[32:33], v120 offset:32768\n"
"v_lshlrev_b32 v46, 16, v167\n"
"ds_read_b64_tr_b16 a[34:35], v120 offset:33792\n"
"v_and_b32 v47, s25, v167\n"
"ds_read_b64_tr_b16 a[36:37], v120 offset:32776\n"
"v_lshlrev_b32 v48, 16, v168\n"
"s_waitcnt lgkmcnt(11)\n"
"ds_read_b64_tr_b16 a[38:39], v120 offset:33800\n"
"v_and_b32 v49, s25, v168\n"
"ds_read_b64_tr_b16 a[40:41], v120 offset:40960\n"
"v_lshlrev_b32 v50, 16, v169\n"
"ds_read_b64_tr_b16 a[42:43], v120 offset:41984\n"
"v_and_b32 v51, s25, v169\n"
"ds_read_b64_tr_b16 a[44:45], v120 offset:40968\n"
"v_lshlrev_b32 v52, 16, v170\n"
"s_waitcnt lgkmcnt(11)\n"
"ds_read_b64_tr_b16 a[46:47], v120 offset:41992\n"
"ds_read_b64_tr_b16 a[48:49], v120 offset:49152\n"
"v_and_b32 v53, s25, v170\n"
"ds_read_b64_tr_b16 a[50:51], v120 offset:50176\n"
"v_lshlrev_b32 v54, 16, v171\n"
"ds_read_b64_tr_b16 a[52:53], v120 offset:49160\n"
"v_and_b32 v55, s25, v171\n"
"s_waitcnt lgkmcnt(11)\n"
"ds_read_b64_tr_b16 a[54:55], v120 offset:50184\n"
"v_lshlrev_b32 v56, 16, v172\n"
"ds_read_b64_tr_b16 a[56:57], v120 offset:57344\n"
"v_and_b32 v57, s25, v172\n"
"ds_read_b64_tr_b16 a[58:59], v120 offset:58368\n"
"v_lshlrev_b32 v58, 16, v173\n"
"ds_read_b64_tr_b16 a[60:61], v120 offset:57352\n"
"v_and_b32 v59, s25, v173\n"
"s_waitcnt lgkmcnt(11)\n"
"ds_read_b64_tr_b16 a[62:63], v120 offset:58376\n"
"ds_read_b64_tr_b16 a[64:65], v121 offset:0\n"
"v_lshlrev_b32 v60, 16, v174\n"
"ds_read_b64_tr_b16 a[66:67], v121 offset:1024\n"
"v_and_b32 v61, s25, v174\n"
"ds_read_b64_tr_b16 a[68:69], v121 offset:8\n"
"v_lshlrev_b32 v62, 16, v175\n"
"s_waitcnt lgkmcnt(11)\n"
"ds_read_b64_tr_b16 a[70:71], v121 offset:1032\n"
"v_and_b32 v63, s25, v175\n"
"ds_read_b64_tr_b16 a[72:73], v121 offset:8192\n"
"s_lshl_b32 s28, s21, 12\n"
"ds_read_b64_tr_b16 a[74:75], v121 offset:9216\n"
"s_and_b32 s28, s28, 0x2000\n"
"ds_read_b64_tr_b16 a[76:77], v121 offset:8200\n"
"v_add_u32 v242, s28, v99\n"
"s_waitcnt lgkmcnt(11)\n"
"ds_read_b64_tr_b16 a[78:79], v121 offset:9224\n"
"buffer_load_dwordx4 v[128:131], v103, s[72:75], s16 offen sc1\n"
"s_add_u32 s36, s16, 262144\n"
"buffer_load_dwordx4 v[132:135], v103, s[72:75], s36 offen sc1\n"
"s_add_u32 s36, s16, 524288\n"
"buffer_load_dwordx4 v[136:139], v103, s[72:75], s36 offen sc1\n"
"s_add_u32 s36, s16, 786432\n"
"buffer_load_dwordx4 v[140:143], v103, s[72:75], s36 offen sc1\n"
"s_waitcnt vmcnt(6)\n"
"v_xor_b32 v238, v64, v117\n"
"v_bitop3_b32 v238, v238, v65, v117 bitop3:0xf6\n"
"v_bitop3_b32 v238, v238, v66, v117 bitop3:0xf6\n"
"v_bitop3_b32 v238, v238, v67, v117 bitop3:0xf6\n"
"s_waitcnt vmcnt(5)\n"
"v_bitop3_b32 v238, v238, v68, v117 bitop3:0xf6\n"
"v_bitop3_b32 v238, v238, v69, v117 bitop3:0xf6\n"
"v_bitop3_b32 v238, v238, v70, v117 bitop3:0xf6\n"
"v_bitop3_b32 v238, v238, v71, v117 bitop3:0xf6\n"
"s_waitcnt vmcnt(4)\n"
"v_bitop3_b32 v238, v238, v72, v117 bitop3:0xf6\n"
"v_bitop3_b32 v238, v238, v73, v117 bitop3:0xf6\n"
"v_bitop3_b32 v238, v238, v74, v117 bitop3:0xf6\n"
"v_bitop3_b32 v238, v238, v75, v117 bitop3:0xf6\n"
"v_and_b32 v238, v238, v237\n"
"v_cmp_ne_u32 vcc, 0, v238\n"
"s_nop 1\n"
"s_cbranch_vccnz Lslow_pq_K\n"
"Lret_pq_K:\n"
"v_and_b32 v64, s25, v64\n"
"v_and_b32 v65, s25, v65\n"
"v_and_b32 v66, s25, v66\n"
"v_and_b32 v67, s25, v67\n"
"v_and_b32 v68, s25, v68\n"
"v_and_b32 v69, s25, v69\n"
"v_and_b32 v70, s25, v70\n"
"v_and_b32 v71, s25, v71\n"
"v_and_b32 v72, s25, v72\n"
"v_and_b32 v73, s25, v73\n"
"v_and_b32 v74, s25, v74\n"
"v_and_b32 v75, s25, v75\n"
"v_mul_f32 v208, 0x3c000000, v64\n"
"v_mul_f32 v209, 0x3c000000, v65\n"
"v_mul_f32 v210, 0x3c000000, v66\n"
"v_mul_f32 v211, 0x3c000000, v67\n"
"ds_write_b128 v97, v[68:71] offset:36864\n"
"ds_write_b128 v97, v[72:75] offset:40960\n"
"s_waitcnt vmcnt(3)\n"
"v_xor_b32 v128, s37, v128\n"
"v_xor_b32 v129, s37, v129\n"
"v_xor_b32 v130, s37, v130\n"
"v_xor_b32 v131, s37, v131\n"
"v_or3_b32 v238, v128, v129, v130\n"
"v_bitop3_b32 v238, v238, v131, v236 bitop3:0x54\n"
"v_cmp_ne_u32 s[28:29], 0, v238\n"
"s_waitcnt vmcnt(2)\n"
"v_xor_b32 v132, s37, v132\n"
"v_xor_b32 v133, s37, v133\n"
"v_xor_b32 v134, s37, v134\n"
"v_xor_b32 v135, s37, v135\n"
"v_or3_b32 v238, v132, v133, v134\n"
"v_bitop3_b32 v238, v238, v135, v236 bitop3:0x54\n"
"v_cmp_ne_u32 s[30:31], 0, v238\n"
"s_waitcnt vmcnt(1)\n"
"v_xor_b32 v136, s37, v136\n"
"v_xor_b32 v137, s37, v137\n"
"v_xor_b32 v138, s37, v138\n"
"v_xor_b32 v139, s37, v139\n"
"v_or3_b32 v238, v136, v137, v138\n"
"v_bitop3_b32 v238, v238, v139, v236 bitop3:0x54\n"
"v_cmp_ne_u32 s[32:33], 0, v238\n"
"s_waitcnt vmcnt(0)\n"
"v_xor_b32 v140, s37, v140\n"
"v_xor_b32 v141, s37, v141\n"
"v_xor_b32 v142, s37, v142\n"
"v_xor_b32 v143, s37, v143\n"
"v_or3_b32 v238, v140, v141, v142\n"
"v_bitop3_b32 v238, v238, v143, v236 bitop3:0x54\n"
"v_cmp_ne_u32 s[34:35], 0, v238\n"
"s_or_b64 vcc, s[28:29], s[30:31]\n"
"s_or_b64 vcc, vcc, s[32:33]\n"
"s_or_b64 vcc, vcc, s[34:35]\n"
"s_cbranch_scc1 Lslow_i3_K\n"
"Lret_i3_K:\n"
"ds_write_b128 v241, v[128:131] offset:49152\n"
"s_waitcnt lgkmcnt(11)\n"
"ds_write_b128 v241, v[132:135] offset:50176\n"
"ds_write_b128 v241, v[136:139] offset:51200\n"
"ds_write_b128 v241, v[140:143] offset:52224\n"
"s_waitcnt lgkmcnt(0)\n"
"s_barrier\n"
"ds_read_b128 v[64:67], v242 offset:49152\n"
"ds_read_b128 v[68:71], v242 offset:51200\n"
"ds_read_b128 v[72:75], v242 offset:49408\n"
"ds_read_b128 v[76:79], v242 offset:51456\n"
"ds_read_b128 v[80:83], v242 offset:53248\n"
"ds_read_b128 v[84:87], v242 offset:55296\n"
"ds_read_b128 v[88:91], v242 offset:53504\n"
"ds_read_b128 v[92:95], v242 offset:55552\n"
"s_mov_b32 exec_lo, 0\n"
"buffer_load_dword a255, v104, s[76:79], s59 offen\n"
"s_mov_b64 exec, -1\n"
"s_waitcnt lgkmcnt(7)\n"
"v_mfma_f32_16x16x32_f16 v[128:131], v[64:67], v[176:179], 0\n"
"v_mfma_f32_16x16x32_f16 v[132:135], v[64:67], v[180:183], 0\n"
"s_waitcnt lgkmcnt(5)\n"
"v_mfma_f32_16x16x32_f16 v[136:139], v[72:75], v[176:179], 0\n"
"v_mfma_f32_16x16x32_f16 v[140:143], v[72:75], v[180:183], 0\n"
"v_mfma_f32_16x16x32_f16 v[128:131], v[68:71], v[184:187], v[128:131]\n"
"v_mfma_f32_16x16x32_f16 v[132:135], v[68:71], v[188:191], v[132:135]\n"
"s_waitcnt lgkmcnt(4)\n"
"v_mfma_f32_16x16x32_f16 v[136:139], v[76:79], v[184:187], v[136:139]\n"
"v_mfma_f32_16x16x32_f16 v[140:143], v[76:79], v[188:191], v[140:143]\n"
"s_waitcnt lgkmcnt(0)\n"
"s_cmp_ge_u32 s21, 2\n"
"s_cbranch_scc1 Lk3real_K\n"
"v_mfma_f32_16x16x32_f16 v[144:147], v[80:83], v[176:179], 0\n"
"v_mfma_f32_16x16x32_f16 v[148:151], v[80:83], v[180:183], 0\n"
"v_mfma_f32_16x16x32_f16 v[152:155], v[88:91], v[176:179], 0\n"
"v_mfma_f32_16x16x32_f16 v[156:159], v[88:91], v[180:183], 0\n"
"v_mfma_f32_16x16x32_f16 v[144:147], v[84:87], v[184:187], v[144:147]\n"
"v_mfma_f32_16x16x32_f16 v[148:151], v[84:87], v[188:191], v[148:151]\n"
"v_mfma_f32_16x16x32_f16 v[152:155], v[92:95], v[184:187], v[152:155]\n"
"v_mfma_f32_16x16x32_f16 v[156:159], v[92:95], v[188:191], v[156:159]\n"
"v_pk_mul_f32 v[160:161], v[128:129], v[192:193]\n"
"v_pk_mul_f32 v[162:163], v[130:131], v[194:195]\n"
"v_pk_fma_f32 v[160:161], v[132:133], v[200:201], v[160:161] neg_lo:[1,0,0] neg_hi:[1,0,0]\n"
"v_pk_fma_f32 v[162:163], v[134:135], v[202:203], v[162:163] neg_lo:[1,0,0] neg_hi:[1,0,0]\n"
"v_pk_mul_f32 v[132:133], v[132:133], v[192:193]\n"
"v_pk_mul_f32 v[134:135], v[134:135], v[194:195]\n"
"v_pk_fma_f32 v[132:133], v[128:129], v[200:201], v[132:133]\n"
"v_pk_fma_f32 v[134:135], v[130:131], v[202:203], v[134:135]\n"
"v_cvt_pk_f16_f32 v160, v160, v132\n"
"v_cvt_pk_f16_f32 v161, v161, v133\n"
"v_cvt_pk_f16_f32 v162, v162, v134\n"
"v_cvt_pk_f16_f32 v163, v163, v135\n"
"v_pk_mul_f32 v[164:165], v[136:137], v[196:197]\n"
"v_pk_mul_f32 v[166:167], v[138:139], v[198:199]\n"
"v_pk_fma_f32 v[164:165], v[140:141], v[204:205], v[164:165] neg_lo:[1,0,0] neg_hi:[1,0,0]\n"
"v_pk_fma_f32 v[166:167], v[142:143], v[206:207], v[166:167] neg_lo:[1,0,0] neg_hi:[1,0,0]\n"
"v_pk_mul_f32 v[140:141], v[140:141], v[196:197]\n"
"v_pk_mul_f32 v[142:143], v[142:143], v[198:199]\n"
"v_pk_fma_f32 v[140:141], v[136:137], v[204:205], v[140:141]\n"
"v_pk_fma_f32 v[142:143], v[138:139], v[206:207], v[142:143]\n"
"v_cvt_pk_f16_f32 v164, v164, v140\n"
"v_cvt_pk_f16_f32 v165, v165, v141\n"
"v_cvt_pk_f16_f32 v166, v166, v142\n"
"v_cvt_pk_f16_f32 v167, v167, v143\n"
"v_mfma_f32_16x16x32_f16 v[128:131], v[160:163], v[0:3], 0\n"
"v_mfma_f32_16x16x32_f16 v[132:135], v[160:163], v[4:7], 0\n"
"v_mfma_f32_16x16x32_f16 v[136:139], v[160:163], v[16:19], 0\n"
"v_mfma_f32_16x16x32_f16 v[140:143], v[160:163], v[20:23], 0\n"
"v_mfma_f32_16x16x32_f16 v[128:131], v[164:167], v[8:11], v[128:131]\n"
"v_mfma_f32_16x16x32_f16 v[132:135], v[164:167], v[12:15], v[132:135]\n"
"v_mfma_f32_16x16x32_f16 v[136:139], v[164:167], v[24:27], v[136:139]\n"
"v_mfma_f32_16x16x32_f16 v[140:143], v[164:167], v[28:31], v[140:143]\n"
"v_pk_mul_f32 v[168:169], v[144:145], v[192:193]\n"
"v_pk_mul_f32 v[170:171], v[146:147], v[194:195]\n"
"v_pk_fma_f32 v[168:169], v[148:149], v[200:201], v[168:169] neg_lo:[1,0,0] neg_hi:[1,0,0]\n"
"v_pk_fma_f32 v[170:171], v[150:151], v[202:203], v[170:171] neg_lo:[1,0,0] neg_hi:[1,0,0]\n"
"v_pk_mul_f32 v[148:149], v[148:149], v[192:193]\n"
"v_pk_mul_f32 v[150:151], v[150:151], v[194:195]\n"
"v_pk_fma_f32 v[148:149], v[144:145], v[200:201], v[148:149]\n"
"v_pk_fma_f32 v[150:151], v[146:147], v[202:203], v[150:151]\n"
"v_cvt_pk_f16_f32 v168, v168, v148\n"
"v_cvt_pk_f16_f32 v169, v169, v149\n"
"v_cvt_pk_f16_f32 v170, v170, v150\n"
"v_cvt_pk_f16_f32 v171, v171, v151\n"
"v_pk_mul_f32 v[172:173], v[152:153], v[196:197]\n"
"v_pk_mul_f32 v[174:175], v[154:155], v[198:199]\n"
"v_pk_fma_f32 v[172:173], v[156:157], v[204:205], v[172:173] neg_lo:[1,0,0] neg_hi:[1,0,0]\n"
"v_pk_fma_f32 v[174:175], v[158:159], v[206:207], v[174:175] neg_lo:[1,0,0] neg_hi:[1,0,0]\n"
"v_pk_mul_f32 v[156:157], v[156:157], v[196:197]\n"
"v_pk_mul_f32 v[158:159], v[158:159], v[198:199]\n"
"v_pk_fma_f32 v[156:157], v[152:153], v[204:205], v[156:157]\n"
"v_pk_fma_f32 v[158:159], v[154:155], v[206:207], v[158:159]\n"
"v_cvt_pk_f16_f32 v172, v172, v156\n"
"v_cvt_pk_f16_f32 v173, v173, v157\n"
"v_cvt_pk_f16_f32 v174, v174, v158\n"
"v_cvt_pk_f16_f32 v175, v175, v159\n"
"v_mfma_f32_16x16x32_f16 v[144:147], v[168:171], v[0:3], 0\n"
"ds_write_b128 v105, v[128:131] offset:9216\n"
"v_mfma_f32_16x16x32_f16 v[148:151], v[168:171], v[4:7], 0\n"
"ds_write_b128 v105, v[132:135] offset:13824\n"
"v_mfma_f32_16x16x32_f16 v[152:155], v[168:171], v[16:19], 0\n"
"ds_write_b128 v105, v[136:139] offset:11520\n"
"v_mfma_f32_16x16x32_f16 v[156:159], v[168:171], v[20:23], 0\n"
"ds_write_b128 v105, v[140:143] offset:16128\n"
"v_mfma_f32_16x16x32_f16 v[144:147], v[172:175], v[8:11], v[144:147]\n"
"v_mfma_f32_16x16x32_f16 v[148:151], v[172:175], v[12:15], v[148:151]\n"
"v_mfma_f32_16x16x32_f16 v[152:155], v[172:175], v[24:27], v[152:155]\n"
"v_mfma_f32_16x16x32_f16 v[156:159], v[172:175], v[28:31], v[156:159]\n"
"s_nop 4\n"
"ds_write_b128 v105, v[144:147] offset:18432\n"
"ds_write_b128 v105, v[148:151] offset:23040\n"
"ds_write_b128 v105, v[152:155] offset:20736\n"
"ds_write_b128 v105, v[156:159] offset:25344\n"
"s_waitcnt lgkmcnt(0)\n"
"s_branch Lk3done_K\n"
"Lk3real_K:\n"
"v_readfirstlane_b32 s28, v80\n"
"v_readfirstlane_b32 s29, v84\n"
"v_mfma_f32_16x16x32_f16 v[144:147], v[80:83], v[176:179], 0\n"
"v_mfma_f32_16x16x32_f16 v[148:151], v[80:83], v[180:183], 0\n"
"v_mfma_f32_16x16x32_f16 v[152:155], v[88:91], v[176:179], 0\n"
"v_mfma_f32_16x16x32_f16 v[156:159], v[88:91], v[180:183], 0\n"
"v_mfma_f32_16x16x32_f16 v[64:67], v[84:87], v[176:179], 0\n"
"v_mfma_f32_16x16x32_f16 v[68:71], v[84:87], v[180:183], 0\n"
"v_mfma_f32_16x16x32_f16 v[72:75], v[92:95], v[176:179], 0\n"
"v_mfma_f32_16x16x32_f16 v[76:79], v[92:95], v[180:183], 0\n"
"v_pk_mul_f32 v[160:161], v[128:129], v[192:193]\n"
"v_pk_mul_f32 v[162:163], v[130:131], v[194:195]\n"
"v_pk_fma_f32 v[160:161], v[132:133], v[200:201], v[160:161] neg_lo:[1,0,0] neg_hi:[1,0,0]\n"
"v_pk_fma_f32 v[162:163], v[134:135], v[202:203], v[162:163] neg_lo:[1,0,0] neg_hi:[1,0,0]\n"
"v_pk_mul_f32 v[132:133], v[132:133], v[192:193]\n"
"v_pk_mul_f32 v[134:135], v[134:135], v[194:195]\n"
"v_pk_fma_f32 v[132:133], v[128:129], v[200:201], v[132:133]\n"
"v_pk_fma_f32 v[134:135], v[130:131], v[202:203], v[134:135]\n"
"v_cvt_pk_f16_f32 v160, v160, v132\n"
"v_cvt_pk_f16_f32 v161, v161, v133\n"
"v_cvt_pk_f16_f32 v162, v162, v134\n"
"v_cvt_pk_f16_f32 v163, v163, v135\n"
"v_pk_mul_f32 v[164:165], v[136:137], v[196:197]\n"
"v_pk_mul_f32 v[166:167], v[138:139], v[198:199]\n"
"v_pk_fma_f32 v[164:165], v[140:141], v[204:205], v[164:165] neg_lo:[1,0,0] neg_hi:[1,0,0]\n"
"v_pk_fma_f32 v[166:167], v[142:143], v[206:207], v[166:167] neg_lo:[1,0,0] neg_hi:[1,0,0]\n"
"v_pk_mul_f32 v[140:141], v[140:141], v[196:197]\n"
"v_pk_mul_f32 v[142:143], v[142:143], v[198:199]\n"
"v_pk_fma_f32 v[140:141], v[136:137], v[204:205], v[140:141]\n"
"v_pk_fma_f32 v[142:143], v[138:139], v[206:207], v[142:143]\n"
"v_cvt_pk_f16_f32 v164, v164, v140\n"
"v_cvt_pk_f16_f32 v165, v165, v141\n"
"v_cvt_pk_f16_f32 v166, v166, v142\n"
"v_cvt_pk_f16_f32 v167, v167, v143\n"
"v_mfma_f32_16x16x32_f16 v[128:131], v[160:163], v[0:3], 0\n"
"v_mfma_f32_16x16x32_f16 v[132:135], v[160:163], v[4:7], 0\n"
"v_mfma_f32_16x16x32_f16 v[136:139], v[160:163], v[16:19], 0\n"
"v_mfma_f32_16x16x32_f16 v[140:143], v[160:163], v[20:23], 0\n"
"v_mfma_f32_16x16x32_f16 v[128:131], v[164:167], v[8:11], v[128:131]\n"
"v_mfma_f32_16x16x32_f16 v[132:135], v[164:167], v[12:15], v[132:135]\n"
"v_mfma_f32_16x16x32_f16 v[136:139], v[164:167], v[24:27], v[136:139]\n"
"v_mfma_f32_16x16x32_f16 v[140:143], v[164:167], v[28:31], v[140:143]\n"
"v_fma_mix_f32 v144, s28, v111, v144 op_sel:[1,0,0] op_sel_hi:[1,0,0]\n"
"v_fma_mix_f32 v64, s29, v111, v64 op_sel:[1,0,0] op_sel_hi:[1,0,0]\n"
"v_pk_mul_f32 v[168:169], v[144:145], v[192:193]\n"
"v_pk_mul_f32 v[170:171], v[146:147], v[194:195]\n"
"v_pk_fma_f32 v[168:169], v[148:149], v[200:201], v[168:169] neg_lo:[1,0,0] neg_hi:[1,0,0]\n"
"v_pk_fma_f32 v[170:171], v[150:151], v[202:203], v[170:171] neg_lo:[1,0,0] neg_hi:[1,0,0]\n"
"v_pk_mul_f32 v[148:149], v[148:149], v[192:193]\n"
"v_pk_mul_f32 v[150:151], v[150:151], v[194:195]\n"
"v_pk_fma_f32 v[148:149], v[144:145], v[200:201], v[148:149]\n"
"v_pk_fma_f32 v[150:151], v[146:147], v[202:203], v[150:151]\n"
"v_cvt_pk_f16_f32 v168, v168, v148\n"
"v_cvt_pk_f16_f32 v169, v169, v149\n"
"v_cvt_pk_f16_f32 v170, v170, v150\n"
"v_cvt_pk_f16_f32 v171, v171, v151\n"
"v_pk_mul_f32 v[172:173], v[152:153], v[196:197]\n"
"v_pk_mul_f32 v[174:175], v[154:155], v[198:199]\n"
"v_pk_fma_f32 v[172:173], v[156:157], v[204:205], v[172:173] neg_lo:[1,0,0] neg_hi:[1,0,0]\n"
"v_pk_fma_f32 v[174:175], v[158:159], v[206:207], v[174:175] neg_lo:[1,0,0] neg_hi:[1,0,0]\n"
"v_pk_mul_f32 v[156:157], v[156:157], v[196:197]\n"
"v_pk_mul_f32 v[158:159], v[158:159], v[198:199]\n"
"v_pk_fma_f32 v[156:157], v[152:153], v[204:205], v[156:157]\n"
"v_pk_fma_f32 v[158:159], v[154:155], v[206:207], v[158:159]\n"
"v_cvt_pk_f16_f32 v172, v172, v156\n"
"v_cvt_pk_f16_f32 v173, v173, v157\n"
"v_cvt_pk_f16_f32 v174, v174, v158\n"
"v_cvt_pk_f16_f32 v175, v175, v159\n"
"v_mfma_f32_16x16x32_f16 v[144:147], v[168:171], v[0:3], 0\n"
"ds_write_b128 v105, v[128:131] offset:27648\n"
"v_mfma_f32_16x16x32_f16 v[148:151], v[168:171], v[16:19], 0\n"
"ds_write_b128 v105, v[132:135] offset:32256\n"
"v_mfma_f32_16x16x32_f16 v[144:147], v[172:175], v[8:11], v[144:147]\n"
"ds_write_b128 v105, v[136:139] offset:29952\n"
"v_mfma_f32_16x16x32_f16 v[148:151], v[172:175], v[24:27], v[148:151]\n"
"ds_write_b128 v105, v[140:143] offset:34560\n"
"v_pk_mul_f32 v[160:161], v[64:65], v[192:193]\n"
"v_pk_mul_f32 v[162:163], v[66:67], v[194:195]\n"
"v_pk_fma_f32 v[160:161], v[68:69], v[200:201], v[160:161] neg_lo:[1,0,0] neg_hi:[1,0,0]\n"
"v_pk_fma_f32 v[162:163], v[70:71], v[202:203], v[162:163] neg_lo:[1,0,0] neg_hi:[1,0,0]\n"
"v_pk_mul_f32 v[68:69], v[68:69], v[192:193]\n"
"v_pk_mul_f32 v[70:71], v[70:71], v[194:195]\n"
"v_pk_fma_f32 v[68:69], v[64:65], v[200:201], v[68:69]\n"
"v_pk_fma_f32 v[70:71], v[66:67], v[202:203], v[70:71]\n"
"v_cvt_pk_f16_f32 v160, v160, v68\n"
"v_cvt_pk_f16_f32 v161, v161, v69\n"
"v_cvt_pk_f16_f32 v162, v162, v70\n"
"v_cvt_pk_f16_f32 v163, v163, v71\n"
"v_pk_mul_f32 v[164:165], v[72:73], v[196:197]\n"
"v_pk_mul_f32 v[166:167], v[74:75], v[198:199]\n"
"v_pk_fma_f32 v[164:165], v[76:77], v[204:205], v[164:165] neg_lo:[1,0,0] neg_hi:[1,0,0]\n"
"v_pk_fma_f32 v[166:167], v[78:79], v[206:207], v[166:167] neg_lo:[1,0,0] neg_hi:[1,0,0]\n"
"v_pk_mul_f32 v[76:77], v[76:77], v[196:197]\n"
"v_pk_mul_f32 v[78:79], v[78:79], v[198:199]\n"
"v_pk_fma_f32 v[76:77], v[72:73], v[204:205], v[76:77]\n"
"v_pk_fma_f32 v[78:79], v[74:75], v[206:207], v[78:79]\n"
"v_cvt_pk_f16_f32 v164, v164, v76\n"
"v_cvt_pk_f16_f32 v165, v165, v77\n"
"v_cvt_pk_f16_f32 v166, v166, v78\n"
"v_cvt_pk_f16_f32 v167, v167, v79\n"
"v_mfma_f32_16x16x32_f16 v[152:155], v[160:163], v[0:3], 0\n"
"ds_write_b128 v105, v[144:147] offset:0\n"
"v_mfma_f32_16x16x32_f16 v[156:159], v[160:163], v[16:19], 0\n"
"ds_write_b128 v105, v[148:151] offset:2304\n"
"v_mfma_f32_16x16x32_f16 v[152:155], v[164:167], v[8:11], v[152:155]\n"
"v_mfma_f32_16x16x32_f16 v[156:159], v[164:167], v[24:27], v[156:159]\n"
"s_nop 6\n"
"ds_write_b128 v105, v[152:155] offset:4608\n"
"ds_write_b128 v105, v[156:159] offset:6912\n"
"s_waitcnt lgkmcnt(0)\n"
"Lk3done_K:\n"
"s_barrier\n"
"ds_read_b128 v[64:67], v106 offset:0\n"
"ds_read_b128 v[68:71], v106 offset:4608\n"
"ds_read_b128 v[80:83], v106 offset:18432\n"
"ds_read_b128 v[84:87], v106 offset:23040\n"
"ds_read_b128 v[72:75], v106 offset:9216\n"
"ds_read_b128 v[88:91], v106 offset:27648\n"
"ds_read_b128 v[76:79], v106 offset:13824\n"
"ds_read_b128 v[92:95], v106 offset:32256\n"
"v_lshlrev_b32 v238, 1, v113\n"
"s_waitcnt lgkmcnt(6)\n"
"v_pk_add_f32 v[144:145], v[64:65], v[68:69]\n"
"v_pk_add_f32 v[148:149], v[64:65], v[68:69] neg_lo:[0,1] neg_hi:[0,1]\n"
"v_pk_add_f32 v[146:147], v[66:67], v[70:71]\n"
"v_pk_add_f32 v[150:151], v[66:67], v[70:71] neg_lo:[0,1] neg_hi:[0,1]\n"
"s_waitcnt lgkmcnt(5)\n"
"v_pk_add_f32 v[128:129], v[144:145], v[80:81]\n"
"v_pk_add_f32 v[136:137], v[144:145], v[80:81] neg_lo:[0,1] neg_hi:[0,1]\n"
"s_waitcnt lgkmcnt(4)\n"
"v_pk_add_f32 v[132:133], v[148:149], v[84:85] neg_lo:[0,1] neg_hi:[0,1]\n"
"v_pk_add_f32 v[140:141], v[148:149], v[84:85]\n"
"v_pk_add_f32 v[130:131], v[146:147], v[82:83]\n"
"v_pk_add_f32 v[138:139], v[146:147], v[82:83] neg_lo:[0,1] neg_hi:[0,1]\n"
"v_pk_add_f32 v[134:135], v[150:151], v[86:87] neg_lo:[0,1] neg_hi:[0,1]\n"
"v_pk_add_f32 v[142:143], v[150:151], v[86:87]\n"
"s_waitcnt lgkmcnt(2)\n"
"v_pk_add_f32 v[144:145], v[72:73], v[88:89]\n"
"v_pk_add_f32 v[64:65], v[72:73], v[88:89] neg_lo:[0,1] neg_hi:[0,1]\n"
"s_waitcnt lgkmcnt(0)\n"
"v_pk_add_f32 v[152:153], v[92:93], v[76:77] neg_lo:[0,1] neg_hi:[0,1]\n"
"v_pk_add_f32 v[68:69], v[76:77], v[92:93]\n"
"v_pk_add_f32 v[146:147], v[74:75], v[90:91]\n"
"v_pk_add_f32 v[66:67], v[74:75], v[90:91] neg_lo:[0,1] neg_hi:[0,1]\n"
"v_pk_add_f32 v[154:155], v[94:95], v[78:79] neg_lo:[0,1] neg_hi:[0,1]\n"
"v_pk_add_f32 v[70:71], v[78:79], v[94:95]\n"
"v_pk_add_f32 v[148:149], v[64:65], v[68:69] neg_lo:[0,1] neg_hi:[0,1]\n"
"v_pk_add_f32 v[156:157], v[64:65], v[68:69]\n"
"v_pk_add_f32 v[150:151], v[66:67], v[70:71] neg_lo:[0,1] neg_hi:[0,1]\n"
"v_pk_add_f32 v[158:159], v[66:67], v[70:71]\n"
"ds_read_b128 v[16:19], v238 offset:36864\n"
"v_pk_add_f32 v[64:65], v[128:129], v[144:145]\n"
"v_pk_add_f32 v[66:67], v[130:131], v[146:147]\n"
"v_pk_fma_f32 v[64:65], v[64:65], v[208:209], v[32:33]\n"
"v_pk_fma_f32 v[66:67], v[66:67], v[210:211], v[34:35]\n"
"v_cvt_pk_bf16_f32 v64, v64, v65\n"
"ds_read_b128 v[20:23], v238 offset:36880\n"
"v_cvt_pk_bf16_f32 v65, v66, v67\n"
"ds_write_b64 v96, v[64:65] offset:49152\n"
"ds_read_b128 v[24:27], v238 offset:38912\n"
"v_pk_fma_f32 v[68:69], v[148:149], s[26:27], v[132:133]\n"
"v_pk_fma_f32 v[70:71], v[150:151], s[26:27], v[134:135]\n"
"v_pk_fma_f32 v[68:69], v[68:69], v[208:209], v[36:37]\n"
"v_pk_fma_f32 v[70:71], v[70:71], v[210:211], v[38:39]\n"
"v_cvt_pk_bf16_f32 v68, v68, v69\n"
"ds_read_b128 v[28:31], v238 offset:38928\n"
"v_cvt_pk_bf16_f32 v69, v70, v71\n"
"ds_write_b64 v96, v[68:69] offset:51200\n"
"ds_read_b128 v[0:3], v238 offset:40960\n"
"v_pk_add_f32 v[72:73], v[136:137], v[152:153]\n"
"v_pk_add_f32 v[74:75], v[138:139], v[154:155]\n"
"v_pk_fma_f32 v[72:73], v[72:73], v[208:209], v[40:41]\n"
"v_pk_fma_f32 v[74:75], v[74:75], v[210:211], v[42:43]\n"
"v_cvt_pk_bf16_f32 v72, v72, v73\n"
"ds_read_b128 v[4:7], v238 offset:40976\n"
"v_cvt_pk_bf16_f32 v73, v74, v75\n"
"ds_write_b64 v96, v[72:73] offset:53248\n"
"ds_read_b128 v[8:11], v238 offset:43008\n"
"v_pk_fma_f32 v[76:77], v[156:157], s[26:27], v[140:141] neg_lo:[1,0,0] neg_hi:[1,0,0]\n"
"v_pk_fma_f32 v[78:79], v[158:159], s[26:27], v[142:143] neg_lo:[1,0,0] neg_hi:[1,0,0]\n"
"v_pk_fma_f32 v[76:77], v[76:77], v[208:209], v[44:45]\n"
"v_pk_fma_f32 v[78:79], v[78:79], v[210:211], v[46:47]\n"
"v_cvt_pk_bf16_f32 v76, v76, v77\n"
"ds_read_b128 v[12:15], v238 offset:43024\n"
"v_cvt_pk_bf16_f32 v77, v78, v79\n"
"ds_write_b64 v96, v[76:77] offset:55296\n"
"s_add_u32 m0, s44, 81920\n"
"s_nop 0\n"
"buffer_load_dwordx4 v122, s[76:79], s42 offen offset:0 lds\n"
"v_pk_add_f32 v[80:81], v[128:129], v[144:145] neg_lo:[0,1] neg_hi:[0,1]\n"
"v_pk_add_f32 v[82:83], v[130:131], v[146:147] neg_lo:[0,1] neg_hi:[0,1]\n"
"v_pk_fma_f32 v[80:81], v[80:81], v[208:209], v[48:49]\n"
"v_pk_fma_f32 v[82:83], v[82:83], v[210:211], v[50:51]\n"
"v_cvt_pk_bf16_f32 v80, v80, v81\n"
"buffer_load_dwordx4 v123, s[76:79], s42 offen offset:1024 lds\n"
"v_cvt_pk_bf16_f32 v81, v82, v83\n"
"ds_write_b64 v96, v[80:81] offset:57344\n"
"buffer_load_dwordx4 v124, s[76:79], s42 offen offset:2048 lds\n"
"v_pk_fma_f32 v[84:85], v[148:149], s[26:27], v[132:133] neg_lo:[1,0,0] neg_hi:[1,0,0]\n"
"v_pk_fma_f32 v[86:87], v[150:151], s[26:27], v[134:135] neg_lo:[1,0,0] neg_hi:[1,0,0]\n"
"v_pk_fma_f32 v[84:85], v[84:85], v[208:209], v[52:53]\n"
"v_pk_fma_f32 v[86:87], v[86:87], v[210:211], v[54:55]\n"
"v_cvt_pk_bf16_f32 v84, v84, v85\n"
"buffer_load_dwordx4 v125, s[76:79], s42 offen offset:3072 lds\n"
"s_add_u32 s42, s42, 458752\n"
"v_cvt_pk_bf16_f32 v85, v86, v87\n"
"ds_write_b64 v96, v[84:85] offset:59392\n"
"s_add_u32 m0, s44, 98304\n"
"s_nop 0\n"
"buffer_load_dwordx4 v122, s[76:79], s42 offen offset:0 lds\n"
"v_pk_add_f32 v[88:89], v[136:137], v[152:153] neg_lo:[0,1] neg_hi:[0,1]\n"
"v_pk_add_f32 v[90:91], v[138:139], v[154:155] neg_lo:[0,1] neg_hi:[0,1]\n"
"v_pk_fma_f32 v[88:89], v[88:89], v[208:209], v[56:57]\n"
"v_pk_fma_f32 v[90:91], v[90:91], v[210:211], v[58:59]\n"
"v_cvt_pk_bf16_f32 v88, v88, v89\n"
"buffer_load_dwordx4 v123, s[76:79], s42 offen offset:1024 lds\n"
"v_cvt_pk_bf16_f32 v89, v90, v91\n"
"ds_write_b64 v96, v[88:89] offset:61440\n"
"buffer_load_dwordx4 v124, s[76:79], s42 offen offset:2048 lds\n"
"v_pk_fma_f32 v[92:93], v[156:157], s[26:27], v[140:141]\n"
"v_pk_fma_f32 v[94:95], v[158:159], s[26:27], v[142:143]\n"
"v_pk_fma_f32 v[92:93], v[92:93], v[208:209], v[60:61]\n"
"v_pk_fma_f32 v[94:95], v[94:95], v[210:211], v[62:63]\n"
"v_cvt_pk_bf16_f32 v92, v92, v93\n"
"buffer_load_dwordx4 v125, s[76:79], s42 offen offset:3072 lds\n"
"s_add_u32 s42, s42, 458752\n"
"v_cvt_pk_bf16_f32 v93, v94, v95\n"
"s_waitcnt lgkmcnt(11)\n"
"ds_write_b64 v96, v[92:93] offset:63488\n"
"s_waitcnt lgkmcnt(0)\n"
"s_barrier\n"
"ds_read_b128 v[64:67], v241 offset:49152\n"
"ds_read_b128 v[68:71], v241 offset:50176\n"
"ds_read_b128 v[72:75], v241 offset:51200\n"
"ds_read_b128 v[76:79], v241 offset:52224\n"
"v_mov_b32 v239, 0x3f803f80\n"
"v_mov_b32 v32, 0\n"
"v_mov_b32 v33, 0\n"
"v_mov_b32 v34, 0\n"
"v_mov_b32 v35, 0\n"
"s_add_u32 m0, s44, 114688\n"
"s_nop 0\n"
"buffer_load_dwordx4 v122, s[76:79], s42 offen offset:0 lds\n"
"s_waitcnt lgkmcnt(3)\n"
"v_dot2c_f32_bf16 v32, v64, v239\n"
"v_dot2c_f32_bf16 v34, v64, v64\n"
"s_waitcnt lgkmcnt(1)\n"
"v_dot2c_f32_bf16 v33, v72, v239\n"
"v_dot2c_f32_bf16 v35, v72, v72\n"
"v_dot2c_f32_bf16 v32, v65, v239\n"
"v_dot2c_f32_bf16 v34, v65, v65\n"
"v_dot2c_f32_bf16 v33, v73, v239\n"
"v_dot2c_f32_bf16 v35, v73, v73\n"
"v_dot2c_f32_bf16 v32, v66, v239\n"
"v_dot2c_f32_bf16 v34, v66, v66\n"
"v_dot2c_f32_bf16 v33, v74, v239\n"
"buffer_load_dwordx4 v123, s[76:79], s42 offen offset:1024 lds\n"
"v_dot2c_f32_bf16 v35, v74, v74\n"
"v_dot2c_f32_bf16 v32, v67, v239\n"
"v_dot2c_f32_bf16 v34, v67, v67\n"
"v_dot2c_f32_bf16 v33, v75, v239\n"
"v_dot2c_f32_bf16 v35, v75, v75\n"
"v_dot2c_f32_bf16 v32, v68, v239\n"
"v_dot2c_f32_bf16 v34, v68, v68\n"
"s_waitcnt lgkmcnt(0)\n"
"v_dot2c_f32_bf16 v33, v76, v239\n"
"v_dot2c_f32_bf16 v35, v76, v76\n"
"v_dot2c_f32_bf16 v32, v69, v239\n"
"v_dot2c_f32_bf16 v34, v69, v69\n"
"buffer_load_dwordx4 v124, s[76:79], s42 offen offset:2048 lds\n"
"v_dot2c_f32_bf16 v33, v77, v239\n"
"v_dot2c_f32_bf16 v35, v77, v77\n"
"v_dot2c_f32_bf16 v32, v70, v239\n"
"v_dot2c_f32_bf16 v34, v70, v70\n"
"v_dot2c_f32_bf16 v33, v78, v239\n"
"v_dot2c_f32_bf16 v35, v78, v78\n"
"v_dot2c_f32_bf16 v32, v71, v239\n"
"v_dot2c_f32_bf16 v34, v71, v71\n"
"v_dot2c_f32_bf16 v33, v79, v239\n"
"v_dot2c_f32_bf16 v35, v79, v79\n"
"v_lshlrev_b32 v128, 16, v64\n"
"buffer_load_dwordx4 v125, s[76:79], s42 offen offset:3072 lds\n"
"s_add_u32 s42, s42, 458752\n"
"v_and_b32 v129, s25, v64\n"
"v_lshlrev_b32 v130, 16, v65\n"
"v_permlane32_swap_b32 v32, v33\n"
"v_permlane32_swap_b32 v34, v35\n"
"v_add_f32 v32, v32, v33\n"
"v_add_f32 v34, v34, v35\n"
"v_and_b32 v131, s25, v65\n"
"v_lshlrev_b32 v132, 16, v66\n"
"v_permlane16_swap_b32 v32, v34\n"
"v_add_f32 v32, v32, v34\n"
"v_and_b32 v133, s25, v66\n"
"s_add_u32 m0, s44, 131072\n"
"s_nop 0\n"
"buffer_load_dwordx4 v122, s[76:79], s42 offen offset:0 lds\n"
"v_lshlrev_b32 v134, 16, v67\n"
"v_add_f32_dpp v32, v32, v32 quad_perm:[1,0,3,2] row_mask:0xf bank_mask:0xf\n"
"v_and_b32 v135, s25, v67\n"
"v_lshlrev_b32 v136, 16, v68\n"
"v_add_f32_dpp v32, v32, v32 quad_perm:[2,3,0,1] row_mask:0xf bank_mask:0xf\n"
"v_and_b32 v137, s25, v68\n"
"v_lshlrev_b32 v138, 16, v69\n"
"v_add_f32_dpp v32, v32, v32 row_half_mirror row_mask:0xf bank_mask:0xf\n"
"v_and_b32 v139, s25, v69\n"
"v_lshlrev_b32 v140, 16, v70\n"
"v_add_f32_dpp v32, v32, v32 row_mirror row_mask:0xf bank_mask:0xf\n"
"buffer_load_dwordx4 v123, s[76:79], s42 offen offset:1024 lds\n"
"v_and_b32 v141, s25, v70\n"
"v_lshlrev_b32 v142, 16, v71\n"
"v_mul_f32 v32, 0x3a800000, v32\n"
"v_mov_b32 v33, v32\n"
"v_and_b32 v143, s25, v71\n"
"v_lshlrev_b32 v144, 16, v72\n"
"v_permlane16_swap_b32 v32, v33\n"
"v_fma_f32 v33, -v32, v32, v33\n"
"v_max_f32 v33, 0, v33\n"
"v_add_f32 v33, 0x3727c5ac, v33\n"
"v_rsq_f32 v33, v33\n"
"buffer_load_dwordx4 v124, s[76:79], s42 offen offset:2048 lds\n"
"v_and_b32 v145, s25, v72\n"
"v_mul_f32_e64 v32, -v32, v33\n"
"v_lshlrev_b32 v146, 16, v73\n"
"v_readlane_b32 s28, v33, 0\n"
"v_readlane_b32 s29, v32, 0\n"
"v_readlane_b32 s30, v33, 32\n"
"v_readlane_b32 s31, v32, 32\n"
"v_and_b32 v147, s25, v73\n"
"v_lshlrev_b32 v148, 16, v74\n"
"v_and_b32 v149, s25, v74\n"
"v_lshlrev_b32 v150, 16, v75\n"
"buffer_load_dwordx4 v125, s[76:79], s42 offen offset:3072 lds\n"
"s_add_u32 s42, s42, 458752\n"
"v_and_b32 v151, s25, v75\n"
"v_lshlrev_b32 v152, 16, v76\n"
"v_and_b32 v153, s25, v76\n"
"v_lshlrev_b32 v154, 16, v77\n"
"v_and_b32 v155, s25, v77\n"
"v_lshlrev_b32 v156, 16, v78\n"
"v_and_b32 v157, s25, v78\n"
"v_lshlrev_b32 v158, 16, v79\n"
"v_and_b32 v159, s25, v79\n"
"v_pk_fma_f32 v[128:129], v[128:129], s[28:29], s[28:29] op_sel:[0,0,1] op_sel_hi:[1,0,1]\n"
"s_add_u32 m0, s44, 147456\n"
"s_nop 0\n"
"buffer_load_dwordx4 v122, s[76:79], s42 offen offset:0 lds\n"
"v_pk_fma_f32 v[144:145], v[144:145], s[30:31], s[30:31] op_sel:[0,0,1] op_sel_hi:[1,0,1]\n"
"v_pk_fma_f32 v[128:129], v[128:129], v[0:1], v[16:17]\n"
"v_pk_fma_f32 v[144:145], v[144:145], v[0:1], v[16:17]\n"
"v_pk_mul_f32 v[36:37], v[128:129], v[128:129]\n"
"v_pk_mul_f32 v[38:39], v[144:145], v[144:145]\n"
"v_pk_fma_f32 v[130:131], v[130:131], s[28:29], s[28:29] op_sel:[0,0,1] op_sel_hi:[1,0,1]\n"
"v_pk_fma_f32 v[146:147], v[146:147], s[30:31], s[30:31] op_sel:[0,0,1] op_sel_hi:[1,0,1]\n"
"v_pk_fma_f32 v[130:131], v[130:131], v[2:3], v[18:19]\n"
"v_pk_fma_f32 v[146:147], v[146:147], v[2:3], v[18:19]\n"
"v_pk_fma_f32 v[36:37], v[130:131], v[130:131], v[36:37]\n"
"v_pk_add_f32 v[32:33], v[128:129], v[130:131]\n"
"buffer_load_dwordx4 v123, s[76:79], s42 offen offset:1024 lds\n"
"v_pk_fma_f32 v[38:39], v[146:147], v[146:147], v[38:39]\n"
"v_pk_add_f32 v[34:35], v[144:145], v[146:147]\n"
"v_pk_fma_f32 v[132:133], v[132:133], s[28:29], s[28:29] op_sel:[0,0,1] op_sel_hi:[1,0,1]\n"
"v_pk_fma_f32 v[148:149], v[148:149], s[30:31], s[30:31] op_sel:[0,0,1] op_sel_hi:[1,0,1]\n"
"v_pk_fma_f32 v[132:133], v[132:133], v[4:5], v[20:21]\n"
"v_pk_fma_f32 v[148:149], v[148:149], v[4:5], v[20:21]\n"
"v_pk_fma_f32 v[36:37], v[132:133], v[132:133], v[36:37]\n"
"v_pk_add_f32 v[32:33], v[32:33], v[132:133]\n"
"v_pk_fma_f32 v[38:39], v[148:149], v[148:149], v[38:39]\n"
"v_pk_add_f32 v[34:35], v[34:35], v[148:149]\n"
"v_pk_fma_f32 v[134:135], v[134:135], s[28:29], s[28:29] op_sel:[0,0,1] op_sel_hi:[1,0,1]\n"
"buffer_load_dwordx4 v124, s[76:79], s42 offen offset:2048 lds\n"
"v_pk_fma_f32 v[150:151], v[150:151], s[30:31], s[30:31] op_sel:[0,0,1] op_sel_hi:[1,0,1]\n"
"v_pk_fma_f32 v[134:135], v[134:135], v[6:7], v[22:23]\n"
"v_pk_fma_f32 v[150:151], v[150:151], v[6:7], v[22:23]\n"
"v_pk_fma_f32 v[36:37], v[134:135], v[134:135], v[36:37]\n"
"v_pk_add_f32 v[32:33], v[32:33], v[134:135]\n"
"v_pk_fma_f32 v[38:39], v[150:151], v[150:151], v[38:39]\n"
"v_pk_add_f32 v[34:35], v[34:35], v[150:151]\n"
"v_pk_fma_f32 v[136:137], v[136:137], s[28:29], s[28:29] op_sel:[0,0,1] op_sel_hi:[1,0,1]\n"
"v_pk_fma_f32 v[152:153], v[152:153], s[30:31], s[30:31] op_sel:[0,0,1] op_sel_hi:[1,0,1]\n"
"v_pk_fma_f32 v[136:137], v[136:137], v[8:9], v[24:25]\n"
"v_pk_fma_f32 v[152:153], v[152:153], v[8:9], v[24:25]\n"
"buffer_load_dwordx4 v125, s[76:79], s42 offen offset:3072 lds\n"
"s_add_u32 s42, s42, 458752\n"
"v_pk_fma_f32 v[36:37], v[136:137], v[136:137], v[36:37]\n"
"v_pk_add_f32 v[32:33], v[32:33], v[136:137]\n"
"v_pk_fma_f32 v[38:39], v[152:153], v[152:153], v[38:39]\n"
"v_pk_add_f32 v[34:35], v[34:35], v[152:153]\n"
"v_pk_fma_f32 v[138:139], v[138:139], s[28:29], s[28:29] op_sel:[0,0,1] op_sel_hi:[1,0,1]\n"
"v_pk_fma_f32 v[154:155], v[154:155], s[30:31], s[30:31] op_sel:[0,0,1] op_sel_hi:[1,0,1]\n"
"v_pk_fma_f32 v[138:139], v[138:139], v[10:11], v[26:27]\n"
"v_pk_fma_f32 v[154:155], v[154:155], v[10:11], v[26:27]\n"
"v_pk_fma_f32 v[36:37], v[138:139], v[138:139], v[36:37]\n"
"v_pk_add_f32 v[32:33], v[32:33], v[138:139]\n"
"v_pk_fma_f32 v[38:39], v[154:155], v[154:155], v[38:39]\n"
"s_add_u32 m0, s44, 65536\n"
"s_nop 0\n"
"buffer_load_dwordx4 v122, s[76:79], s42 offen offset:0 lds\n"
"v_pk_add_f32 v[34:35], v[34:35], v[154:155]\n"
"v_pk_fma_f32 v[140:141], v[140:141], s[28:29], s[28:29] op_sel:[0,0,1] op_sel_hi:[1,0,1]\n"
"v_pk_fma_f32 v[156:157], v[156:157], s[30:31], s[30:31] op_sel:[0,0,1] op_sel_hi:[1,0,1]\n"
"v_pk_fma_f32 v[140:141], v[140:141], v[12:13], v[28:29]\n"
"v_pk_fma_f32 v[156:157], v[156:157], v[12:13], v[28:29]\n"
"v_pk_fma_f32 v[36:37], v[140:141], v[140:141], v[36:37]\n"
"v_pk_add_f32 v[32:33], v[32:33], v[140:141]\n"
"v_pk_fma_f32 v[38:39], v[156:157], v[156:157], v[38:39]\n"
"v_pk_add_f32 v[34:35], v[34:35], v[156:157]\n"
"v_pk_fma_f32 v[142:143], v[142:143], s[28:29], s[28:29] op_sel:[0,0,1] op_sel_hi:[1,0,1]\n"
"v_pk_fma_f32 v[158:159], v[158:159], s[30:31], s[30:31] op_sel:[0,0,1] op_sel_hi:[1,0,1]\n"
"buffer_load_dwordx4 v123, s[76:79], s42 offen offset:1024 lds\n"
"v_pk_fma_f32 v[142:143], v[142:143], v[14:15], v[30:31]\n"
"v_pk_fma_f32 v[158:159], v[158:159], v[14:15], v[30:31]\n"
"v_pk_fma_f32 v[36:37], v[142:143], v[142:143], v[36:37]\n"
"v_pk_add_f32 v[32:33], v[32:33], v[142:143]\n"
"v_pk_fma_f32 v[38:39], v[158:159], v[158:159], v[38:39]\n"
"v_pk_add_f32 v[34:35], v[34:35], v[158:159]\n"
"v_add_f32 v32, v32, v33\n"
"v_add_f32 v33, v34, v35\n"
"v_add_f32 v34, v36, v37\n"
"v_add_f32 v35, v38, v39\n"
"v_permlane32_swap_b32 v32, v33\n"
"buffer_load_dwordx4 v124, s[76:79], s42 offen offset:2048 lds\n"
"v_permlane32_swap_b32 v34, v35\n"
"v_add_f32 v32, v32, v33\n"
"v_add_f32 v34, v34, v35\n"
"s_nop 1\n"
"v_permlane16_swap_b32 v32, v34\n"
"v_add_f32 v32, v32, v34\n"
"s_nop 1\n"
"v_add_f32_dpp v32, v32, v32 quad_perm:[1,0,3,2] row_mask:0xf bank_mask:0xf\n"
"s_nop 1\n"
"v_add_f32_dpp v32, v32, v32 quad_perm:[2,3,0,1] row_mask:0xf bank_mask:0xf\n"
"s_nop 1\n"
"v_add_f32_dpp v32, v32, v32 row_half_mirror row_mask:0xf bank_mask:0xf\n"
"s_nop 1\n"
"v_add_f32_dpp v32, v32, v32 row_mirror row_mask:0xf bank_mask:0xf\n"
"v_mul_f32 v32, 0x3a800000, v32\n"
"v_mov_b32 v33, v32\n"
"buffer_load_dwordx4 v125, s[76:79], s42 offen offset:3072 lds\n"
"s_add_u32 s42, s42, 458752\n"
"v_permlane16_swap_b32 v32, v33\n"
"v_fma_f32 v33, -v32, v32, v33\n"
"v_max_f32 v33, 0, v33\n"
"v_add_f32 v33, 0x3727c5ac, v33\n"
"v_rsq_f32 v33, v33\n"
"s_nop 0\n"
"v_mul_f32_e64 v32, -v32, v33\n"
"v_readlane_b32 s28, v33, 0\n"
"v_readlane_b32 s29, v32, 0\n"
"v_readlane_b32 s30, v33, 32\n"
"v_readlane_b32 s31, v32, 32\n"
"s_add_u32 s32, s20, 16384\n"
"v_pk_fma_f32 v[128:129], v[128:129], s[28:29], s[28:29] op_sel:[0,0,1] op_sel_hi:[1,0,1]\n"
"v_pk_fma_f32 v[130:131], v[130:131], s[28:29], s[28:29] op_sel:[0,0,1] op_sel_hi:[1,0,1]\n"
"v_pk_fma_f32 v[132:133], v[132:133], s[28:29], s[28:29] op_sel:[0,0,1] op_sel_hi:[1,0,1]\n"
"v_pk_fma_f32 v[134:135], v[134:135], s[28:29], s[28:29] op_sel:[0,0,1] op_sel_hi:[1,0,1]\n"
"v_cvt_pk_bf16_f32 v0, v128, v129\n"
"v_cvt_pk_bf16_f32 v1, v130, v131\n"
"v_cvt_pk_bf16_f32 v2, v132, v133\n"
"v_cvt_pk_bf16_f32 v3, v134, v135\n"
"buffer_store_dwordx4 v[0:3], v107, s[72:75], s20 offen offset:0 sc1\n"
"v_pk_fma_f32 v[136:137], v[136:137], s[28:29], s[28:29] op_sel:[0,0,1] op_sel_hi:[1,0,1]\n"
"v_pk_fma_f32 v[138:139], v[138:139], s[28:29], s[28:29] op_sel:[0,0,1] op_sel_hi:[1,0,1]\n"
"v_pk_fma_f32 v[140:141], v[140:141], s[28:29], s[28:29] op_sel:[0,0,1] op_sel_hi:[1,0,1]\n"
"v_pk_fma_f32 v[142:143], v[142:143], s[28:29], s[28:29] op_sel:[0,0,1] op_sel_hi:[1,0,1]\n"
"v_cvt_pk_bf16_f32 v4, v136, v137\n"
"v_cvt_pk_bf16_f32 v5, v138, v139\n"
"v_cvt_pk_bf16_f32 v6, v140, v141\n"
"v_cvt_pk_bf16_f32 v7, v142, v143\n"
"buffer_store_dwordx4 v[4:7], v107, s[72:75], s32 offen offset:0 sc1\n"
"v_pk_fma_f32 v[144:145], v[144:145], s[30:31], s[30:31] op_sel:[0,0,1] op_sel_hi:[1,0,1]\n"
"v_pk_fma_f32 v[146:147], v[146:147], s[30:31], s[30:31] op_sel:[0,0,1] op_sel_hi:[1,0,1]\n"
"v_pk_fma_f32 v[148:149], v[148:149], s[30:31], s[30:31] op_sel:[0,0,1] op_sel_hi:[1,0,1]\n"
"v_pk_fma_f32 v[150:151], v[150:151], s[30:31], s[30:31] op_sel:[0,0,1] op_sel_hi:[1,0,1]\n"
"v_cvt_pk_bf16_f32 v8, v144, v145\n"
"v_cvt_pk_bf16_f32 v9, v146, v147\n"
"v_cvt_pk_bf16_f32 v10, v148, v149\n"
"v_cvt_pk_bf16_f32 v11, v150, v151\n"
"buffer_store_dwordx4 v[8:11], v107, s[72:75], s20 offen offset:128 sc1\n"
"v_pk_fma_f32 v[152:153], v[152:153], s[30:31], s[30:31] op_sel:[0,0,1] op_sel_hi:[1,0,1]\n"
"v_pk_fma_f32 v[154:155], v[154:155], s[30:31], s[30:31] op_sel:[0,0,1] op_sel_hi:[1,0,1]\n"
"v_pk_fma_f32 v[156:157], v[156:157], s[30:31], s[30:31] op_sel:[0,0,1] op_sel_hi:[1,0,1]\n"
"v_pk_fma_f32 v[158:159], v[158:159], s[30:31], s[30:31] op_sel:[0,0,1] op_sel_hi:[1,0,1]\n"
"v_cvt_pk_bf16_f32 v12, v152, v153\n"
"v_cvt_pk_bf16_f32 v13, v154, v155\n"
"v_cvt_pk_bf16_f32 v14, v156, v157\n"
"v_cvt_pk_bf16_f32 v15, v158, v159\n"
"buffer_store_dwordx4 v[12:15], v107, s[72:75], s32 offen offset:128 sc1\n"
"buffer_store_dwordx4 v[64:67], v113, s[72:75], s19 offen offset:0 sc1\n"
"buffer_store_dwordx4 v[68:71], v113, s[72:75], s19 offen offset:1024 sc1\n"
"buffer_store_dwordx4 v[72:75], v113, s[72:75], s19 offen offset:2048 sc1\n"
"buffer_store_dwordx4 v[76:79], v113, s[72:75], s19 offen offset:3072 sc1\n"
"s_waitcnt vmcnt(8)\n"
"s_barrier\n"
"v_add_u32 v2, s44, v113\n"
"ds_read_b64_tr_b16 a[80:81], v120 offset:16384\n"
"v_readfirstlane_b32 s50, v117\n"
"v_mov_b32 v24, v117\n"
"ds_read_b64_tr_b16 a[82:83], v120 offset:17408\n"
"v_mov_b32 v25, v117\n"
"ds_read_b64_tr_b16 a[84:85], v120 offset:16392\n"
"v_mov_b32 v26, v117\n"
"ds_read_b64_tr_b16 a[86:87], v120 offset:17416\n"
"v_mov_b32 v27, v117\n"
"ds_read_b64_tr_b16 a[88:89], v120 offset:24576\n"
"v_bfe_u32 v76, v113, 4, 4\n"
"ds_read_b64_tr_b16 a[90:91], v120 offset:25600\n"
"v_lshrrev_b32 v77, 8, v113\n"
"ds_read_b64_tr_b16 a[92:93], v120 offset:24584\n"
"v_mov_b32 v80, 0x8000000\n"
"v_lshl_add_u32 v78, v77, 4, s5\n"
"ds_read_b64_tr_b16 a[94:95], v120 offset:25608\n"
"global_load_dwordx4 v[28:31], v78, s[88:89]\n"
"ds_read_b64_tr_b16 a[96:97], v120 offset:32768\n"
"v_cmp_gt_u32 vcc, 64, v97\n"
"ds_read_b64_tr_b16 a[98:99], v120 offset:33792\n"
"v_mov_b32 v1, 1\n"
"ds_read_b64_tr_b16 a[100:101], v120 offset:32776\n"
"v_mov_b32 v0, s7\n"
"ds_read_b64_tr_b16 a[102:103], v120 offset:33800\n"
"v_cndmask_b32 v22, v80, v97, vcc\n"
"ds_read_b64_tr_b16 a[104:105], v120 offset:40960\n"
"v_cmp_eq_u32 vcc, 0, v97\n"
"v_lshrrev_b32 v78, 1, v76\n"
"ds_read_b64_tr_b16 a[106:107], v120 offset:41984\n"
"v_xor_b32 v78, v78, v77\n"
"ds_read_b64_tr_b16 a[108:109], v120 offset:40968\n"
"v_cndmask_b32 v0, v80, v0, vcc\n"
"s_waitcnt lgkmcnt(11)\n"
"ds_read_b64_tr_b16 a[110:111], v120 offset:41992\n"
"v_lshlrev_b32 v78, 4, v78\n"
"ds_read_b64_tr_b16 a[112:113], v120 offset:49152\n"
"v_lshl_add_u32 v114, v76, 7, v78\n"
"ds_read_b64_tr_b16 a[114:115], v120 offset:50176\n"
"v_add_u32 v115, 0x10000, v114\n"
"ds_read_b64_tr_b16 a[116:117], v120 offset:49160\n"
"v_xor_b32 v116, 64, v114\n"
"v_xor_b32 v117, 64, v115\n"
"s_waitcnt lgkmcnt(11)\n"
"ds_read_b64_tr_b16 a[118:119], v120 offset:50184\n"
"v_add_u32 v118, 0x10000, v113\n"
"ds_read_b64_tr_b16 a[120:121], v120 offset:57344\n"
"v_lshl_add_u32 v96, v77, 4, s2\n"
"ds_read_b64_tr_b16 a[122:123], v120 offset:58368\n"
"v_lshl_add_u32 v126, v76, 11, v96\n"
"ds_read_b64_tr_b16 a[124:125], v120 offset:57352\n"
"v_and_b32 v78, 7, v76\n"
"s_waitcnt lgkmcnt(11)\n"
"ds_read_b64_tr_b16 a[126:127], v120 offset:58376\n"
"v_lshrrev_b32 v79, 3, v76\n"
"ds_read_b64_tr_b16 a[128:129], v121 offset:0\n"
"v_lshl_add_u32 v78, v78, 8, v79\n"
"v_lshl_add_u32 v127, v78, 11, v96\n"
"ds_read_b64_tr_b16 a[130:131], v121 offset:1024\n"
"v_lshlrev_b32 v104, 1, v96\n"
"ds_read_b64_tr_b16 a[132:133], v121 offset:8\n"
"v_add_u32 v104, 307200, v104\n"
"s_waitcnt lgkmcnt(11)\n"
"ds_read_b64_tr_b16 a[134:135], v121 offset:1032\n"
"v_lshlrev_b32 v78, 2, v77\n"
"ds_read_b64_tr_b16 a[136:137], v121 offset:8192\n"
"v_xor_b32 v78, v78, v76\n"
"ds_read_b64_tr_b16 a[138:139], v121 offset:9216\n"
"v_lshlrev_b32 v78, 4, v78\n"
"ds_read_b64_tr_b16 a[140:141], v121 offset:8200\n"
"v_lshl_add_u32 v81, v77, 11, v78\n"
"v_lshrrev_b32 v78, 5, v113\n"
"s_waitcnt lgkmcnt(11)\n"
"ds_read_b64_tr_b16 a[142:143], v121 offset:9224\n"
"v_mad_u32_u24 v20, v78, 9, s4\n"
"ds_read_b64_tr_b16 a[144:145], v121 offset:16384\n"
"v_and_b32 v78, 3, v78\n"
"ds_read_b64_tr_b16 a[146:147], v121 offset:17408\n"
"v_lshl_add_u32 v78, v78, 5, s6\n"
"ds_read_b64_tr_b16 a[148:149], v121 offset:16392\n"
"v_cmp_gt_u32 vcc, 0x80, v20\n"
"s_waitcnt lgkmcnt(11)\n"
"ds_read_b64_tr_b16 a[150:151], v121 offset:17416\n"
"v_and_b32 v79, 16, v113\n"
"ds_read_b64_tr_b16 a[152:153], v121 offset:24576\n"
"v_lshlrev_b32 v79, 2, v79\n"
"v_cndmask_b32 v20, v78, v20, vcc\n"
"ds_read_b64_tr_b16 a[154:155], v121 offset:25600\n"
"v_lshl_add_u32 v20, v20, 7, v79\n"
"ds_read_b64_tr_b16 a[156:157], v121 offset:24584\n"
"v_add_u32 v20, 73728, v20\n"
"s_waitcnt lgkmcnt(11)\n"
"ds_read_b64_tr_b16 a[158:159], v121 offset:25608\n"
"v_bfe_u32 v78, v113, 6, 1\n"
"ds_read_b64_tr_b16 a[160:161], v120 offset:0\n"
"v_lshl_add_u32 v78, v78, 6, s3\n"
"ds_read_b64_tr_b16 a[162:163], v120 offset:1024\n"
"v_bfe_u32 v3, v113, 5, 1\n"
"ds_read_b64_tr_b16 a[164:165], v120 offset:8\n"
"v_add_u32 v78, v78, v3\n"
"v_lshlrev_b32 v79, 1, v79\n"
"s_waitcnt lgkmcnt(11)\n"
"ds_read_b64_tr_b16 a[166:167], v120 offset:1032\n"
"v_cmp_gt_u32 vcc, 0x80, v113\n"
"ds_read_b64_tr_b16 a[168:169], v120 offset:8192\n"
"v_lshl_add_u32 v78, v78, 11, v79\n"
"ds_read_b64_tr_b16 a[170:171], v120 offset:9216\n"
"s_nop 0\n"
"ds_read_b64_tr_b16 a[172:173], v120 offset:8200\n"
"v_cndmask_b32 v3, v80, v78, vcc\n"
"s_waitcnt lgkmcnt(11)\n"
"ds_read_b64_tr_b16 a[174:175], v120 offset:9224\n"
"s_add_u32 s95, s101, 73728\n"
"s_waitcnt vmcnt(5)\n"
"s_waitcnt lgkmcnt(0)\n"
"s_barrier\n"
"buffer_store_dwordx4 v[24:27], v22, s[72:75], s95 offen sc1\n"
"s_add_u32 m0, s44, 81920\n"
"s_nop 0\n"
"buffer_load_dwordx4 v122, s[76:79], s42 offen offset:0 lds\n"
"buffer_load_dwordx4 v123, s[76:79], s42 offen offset:1024 lds\n"
"buffer_load_dwordx4 v124, s[76:79], s42 offen offset:2048 lds\n"
"buffer_load_dwordx4 v125, s[76:79], s42 offen offset:3072 lds\n"
"s_add_u32 s42, s42, 458752\n"
"s_add_u32 m0, s44, 98304\n"
"s_nop 0\n"
"buffer_load_dwordx4 v122, s[76:79], s42 offen offset:0 lds\n"
"buffer_load_dwordx4 v123, s[76:79], s42 offen offset:1024 lds\n"
"buffer_load_dwordx4 v124, s[76:79], s42 offen offset:2048 lds\n"
"buffer_load_dwordx4 v125, s[76:79], s42 offen offset:3072 lds\n"
"s_add_u32 s42, s42, 458752\n"
"s_add_u32 m0, s44, 114688\n"
"s_nop 0\n"
"buffer_load_dwordx4 v122, s[76:79], s42 offen offset:0 lds\n"
"buffer_load_dwordx4 v123, s[76:79], s42 offen offset:1024 lds\n"
"buffer_load_dwordx4 v124, s[76:79], s42 offen offset:2048 lds\n"
"buffer_load_dwordx4 v125, s[76:79], s42 offen offset:3072 lds\n"
"s_add_u32 s42, s42, 458752\n"
"s_add_u32 m0, s44, 131072\n"
"s_nop 0\n"
"buffer_load_dwordx4 v122, s[76:79], s42 offen offset:0 lds\n"
"buffer_load_dwordx4 v123, s[76:79], s42 offen offset:1024 lds\n"
"buffer_load_dwordx4 v124, s[76:79], s42 offen offset:2048 lds\n"
"buffer_load_dwordx4 v125, s[76:79], s42 offen offset:3072 lds\n"
"s_add_u32 s42, s42, 458752\n"
"s_add_u32 m0, s44, 147456\n"
"s_nop 0\n"
"buffer_load_dwordx4 v122, s[76:79], s42 offen offset:0 lds\n"
"buffer_load_dwordx4 v123, s[76:79], s42 offen offset:1024 lds\n"
"buffer_load_dwordx4 v124, s[76:79], s42 offen offset:2048 lds\n"
"buffer_load_dwordx4 v125, s[76:79], s42 offen offset:3072 lds\n"
"s_add_u32 s42, s42, 458752\n"
"s_waitcnt vmcnt(21)\n"
"v_lshlrev_b32 v88, 16, v28\n"
"v_and_b32 v89, 0xffff0000, v28\n"
"v_lshlrev_b32 v90, 16, v29\n"
"v_and_b32 v91, 0xffff0000, v29\n"
"v_lshlrev_b32 v92, 16, v30\n"
"v_and_b32 v93, 0xffff0000, v30\n"
"v_lshlrev_b32 v94, 16, v31\n"
"v_and_b32 v95, 0xffff0000, v31\n"
"s_cmp_lg_u32 s49, 0\n"
"s_cbranch_scc1 Lslow_a1_K\n"
"Lret_a1_K:\n"
"s_barrier\n"
"s_add_u32 m0, s44, 0\n"
"s_nop 0\n"
"buffer_load_dwordx4 v113, s[64:67], s40 offen offset:0 sc1 lds\n"
"buffer_load_dwordx4 v113, s[64:67], s40 offen offset:1024 sc1 lds\n"
"buffer_load_dwordx4 v113, s[64:67], s40 offen offset:2048 sc1 lds\n"
"buffer_load_dwordx4 v113, s[64:67], s40 offen offset:3072 sc1 lds\n"
"s_add_u32 s40, s40, 294912\n"
"s_add_u32 m0, s44, 16384\n"
"s_nop 0\n"
"buffer_load_dwordx4 v113, s[64:67], s40 offen offset:0 sc1 lds\n"
"buffer_load_dwordx4 v113, s[64:67], s40 offen offset:1024 sc1 lds\n"
"buffer_load_dwordx4 v113, s[64:67], s40 offen offset:2048 sc1 lds\n"
"buffer_load_dwordx4 v113, s[64:67], s40 offen offset:3072 sc1 lds\n"
"s_add_u32 s40, s40, -278528\n"
"s_add_u32 m0, s44, 32768\n"
"s_nop 0\n"
"buffer_load_dwordx4 v113, s[64:67], s40 offen offset:0 sc1 lds\n"
"buffer_load_dwordx4 v113, s[64:67], s40 offen offset:1024 sc1 lds\n"
"buffer_load_dwordx4 v113, s[64:67], s40 offen offset:2048 sc1 lds\n"
"buffer_load_dwordx4 v113, s[64:67], s40 offen offset:3072 sc1 lds\n"
"s_add_u32 s40, s40, 294912\n"
"v_add_u32 v122, 0, v81\n"
"v_add_u32 v123, 7168, v81\n"
"v_add_u32 v124, 14336, v81\n"
"v_add_u32 v125, 21504, v81\n"
"v_and_b32 v119, 0xf0, v113\n"
"s_lshr_b32 s92, s44, 1\n"
"v_lshl_add_u32 v119, v119, 3, s92\n"
"s_waitcnt vmcnt(8)\n"
"s_barrier\n"
"v_mov_b32 v112, 0xc0135761\n"
"ds_read_b128 v[28:31], v114 offset:0\n"
"ds_read_b128 v[32:35], v116 offset:0\n"
"ds_read_b128 v[36:39], v114 offset:2048\n"
"ds_read_b128 v[40:43], v116 offset:2048\n"
"ds_read_b128 v[44:47], v114 offset:4096\n"
"ds_read_b128 v[48:51], v116 offset:4096\n"
"ds_read_b128 v[52:55], v114 offset:6144\n"
"s_waitcnt lgkmcnt(5)\n"
"v_mfma_f32_16x16x32_bf16 v[60:63], a[0:3], v[28:31], v[88:91]\n"
"s_add_u32 m0, s44, 49152\n"
"ds_read_b128 v[56:59], v116 offset:6144\n"
"ds_read_b64_tr_b16 a[176:177], v120 offset:16384\n"
"v_mfma_f32_16x16x32_bf16 v[64:67], a[4:7], v[28:31], v[92:95]\n"
"buffer_load_dwordx4 v113, s[64:67], s40 offen offset:0 sc1 lds\n"
"v_mfma_f32_16x16x32_bf16 v[60:63], a[8:11], v[32:35], v[60:63]\n"
"ds_read_b128 v[28:31], v114 offset:8192\n"
"ds_read_b64_tr_b16 a[178:179], v120 offset:17408\n"
"v_mfma_f32_16x16x32_bf16 v[64:67], a[12:15], v[32:35], v[64:67]\n"
"s_waitcnt lgkmcnt(7)\n"
"v_mfma_f32_16x16x32_bf16 v[60:63], a[16:19], v[36:39], v[60:63]\n"
"ds_read_b128 v[32:35], v116 offset:8192\n"
"ds_read_b64_tr_b16 a[180:181], v120 offset:16392\n"
"v_mfma_f32_16x16x32_bf16 v[64:67], a[20:23], v[36:39], v[64:67]\n"
"buffer_load_dwordx4 v113, s[64:67], s40 offen offset:1024 sc1 lds\n"
"v_mfma_f32_16x16x32_bf16 v[60:63], a[24:27], v[40:43], v[60:63]\n"
"ds_read_b128 v[36:39], v114 offset:10240\n"
"ds_read_b64_tr_b16 a[182:183], v120 offset:17416\n"
"v_mfma_f32_16x16x32_bf16 v[64:67], a[28:31], v[40:43], v[64:67]\n"
"s_waitcnt lgkmcnt(9)\n"
"v_mfma_f32_16x16x32_bf16 v[60:63], a[32:35], v[44:47], v[60:63]\n"
"ds_read_b128 v[40:43], v116 offset:10240\n"
"ds_read_b64_tr_b16 a[184:185], v120 offset:24576\n"
"v_mfma_f32_16x16x32_bf16 v[64:67], a[36:39], v[44:47], v[64:67]\n"
"buffer_load_dwordx4 v113, s[64:67], s40 offen offset:2048 sc1 lds\n"
"v_mfma_f32_16x16x32_bf16 v[60:63], a[40:43], v[48:51], v[60:63]\n"
"ds_read_b128 v[44:47], v114 offset:12288\n"
"ds_read_b64_tr_b16 a[186:187], v120 offset:25600\n"
"v_mfma_f32_16x16x32_bf16 v[64:67], a[44:47], v[48:51], v[64:67]\n"
"s_waitcnt lgkmcnt(11)\n"
"v_mfma_f32_16x16x32_bf16 v[60:63], a[48:51], v[52:55], v[60:63]\n"
"ds_read_b128 v[48:51], v116 offset:12288\n"
"ds_read_b64_tr_b16 a[188:189], v120 offset:24584\n"
"v_mfma_f32_16x16x32_bf16 v[64:67], a[52:55], v[52:55], v[64:67]\n"
"buffer_load_dwordx4 v113, s[64:67], s40 offen offset:3072 sc1 lds\n"
"v_mfma_f32_16x16x32_bf16 v[60:63], a[56:59], v[56:59], v[60:63]\n"
"s_add_u32 s40, s40, 278528\n"
"ds_read_b128 v[52:55], v114 offset:14336\n"
"ds_read_b64_tr_b16 a[190:191], v120 offset:25608\n"
"v_mfma_f32_16x16x32_bf16 v[64:67], a[60:63], v[56:59], v[64:67]\n"
"s_waitcnt lgkmcnt(11)\n"
"v_mfma_f32_16x16x32_bf16 v[60:63], a[64:67], v[28:31], v[60:63]\n"
"s_waitcnt vmcnt(8)\n"
"ds_read_b128 v[56:59], v116 offset:14336\n"
"ds_read_b64_tr_b16 a[192:193], v120 offset:32768\n"
"v_mfma_f32_16x16x32_bf16 v[64:67], a[68:71], v[28:31], v[64:67]\n"
"s_barrier\n"
"v_mfma_f32_16x16x32_bf16 v[60:63], a[72:75], v[32:35], v[60:63]\n"
"s_add_u32 m0, s44, 65536\n"
"ds_read_b128 v[28:31], v114 offset:16384\n"
"ds_read_b64_tr_b16 a[194:195], v120 offset:33792\n"
"v_mfma_f32_16x16x32_bf16 v[64:67], a[76:79], v[32:35], v[64:67]\n"
"s_waitcnt lgkmcnt(11)\n"
"v_mfma_f32_16x16x32_bf16 v[60:63], a[80:83], v[36:39], v[60:63]\n"
"ds_read_b128 v[32:35], v116 offset:16384\n"
"buffer_load_dwordx4 v113, s[64:67], s40 offen offset:0 sc1 lds\n"
"v_mfma_f32_16x16x32_bf16 v[64:67], a[84:87], v[36:39], v[64:67]\n"
"ds_read_b64_tr_b16 a[196:197], v120 offset:32776\n"
"v_mfma_f32_16x16x32_bf16 v[60:63], a[88:91], v[40:43], v[60:63]\n"
"ds_read_b128 v[36:39], v114 offset:18432\n"
"v_mfma_f32_16x16x32_bf16 v[64:67], a[92:95], v[40:43], v[64:67]\n"
"s_waitcnt lgkmcnt(10)\n"
"ds_read_b64_tr_b16 a[198:199], v120 offset:33800\n"
"v_mfma_f32_16x16x32_bf16 v[60:63], a[96:99], v[44:47], v[60:63]\n"
"ds_read_b128 v[40:43], v116 offset:18432\n"
"v_mfma_f32_16x16x32_bf16 v[64:67], a[100:103], v[44:47], v[64:67]\n"
"ds_read_b64_tr_b16 a[200:201], v120 offset:40960\n"
"v_mfma_f32_16x16x32_bf16 v[60:63], a[104:107], v[48:51], v[60:63]\n"
"ds_read_b128 v[44:47], v114 offset:20480\n"
"buffer_load_dwordx4 v113, s[64:67], s40 offen offset:1024 sc1 lds\n"
"v_mfma_f32_16x16x32_bf16 v[64:67], a[108:111], v[48:51], v[64:67]\n"
"s_waitcnt lgkmcnt(10)\n"
"ds_read_b64_tr_b16 a[202:203], v120 offset:41984\n"
"v_mfma_f32_16x16x32_bf16 v[60:63], a[112:115], v[52:55], v[60:63]\n"
"ds_read_b128 v[48:51], v116 offset:20480\n"
"v_mfma_f32_16x16x32_bf16 v[64:67], a[116:119], v[52:55], v[64:67]\n"
"ds_read_b64_tr_b16 a[204:205], v120 offset:40968\n"
"v_mfma_f32_16x16x32_bf16 v[60:63], a[120:123], v[56:59], v[60:63]\n"
"ds_read_b128 v[52:55], v114 offset:22528\n"
"v_mfma_f32_16x16x32_bf16 v[64:67], a[124:127], v[56:59], v[64:67]\n"
"s_waitcnt lgkmcnt(10)\n"
"ds_read_b64_tr_b16 a[206:207], v120 offset:41992\n"
"v_mfma_f32_16x16x32_bf16 v[68:71], a[0:3], v[28:31], v[88:91]\n"
"ds_read_b128 v[56:59], v116 offset:22528\n"
"buffer_load_dwordx4 v113, s[64:67], s40 offen offset:2048 sc1 lds\n"
"v_mfma_f32_16x16x32_bf16 v[72:75], a[4:7], v[28:31], v[92:95]\n"
"ds_read_b64_tr_b16 a[208:209], v120 offset:49152\n"
"v_mfma_f32_16x16x32_bf16 v[68:71], a[8:11], v[32:35], v[68:71]\n"
"ds_read_b128 v[28:31], v114 offset:24576\n"
"v_mfma_f32_16x16x32_bf16 v[72:75], a[12:15], v[32:35], v[72:75]\n"
"s_waitcnt lgkmcnt(10)\n"
"ds_read_b64_tr_b16 a[210:211], v120 offset:50176\n"
"v_mfma_f32_16x16x32_bf16 v[68:71], a[16:19], v[36:39], v[68:71]\n"
"ds_read_b128 v[32:35], v116 offset:24576\n"
"v_mfma_f32_16x16x32_bf16 v[72:75], a[20:23], v[36:39], v[72:75]\n"
"ds_read_b64_tr_b16 a[212:213], v120 offset:49160\n"
"v_mfma_f32_16x16x32_bf16 v[68:71], a[24:27], v[40:43], v[68:71]\n"
"ds_read_b128 v[36:39], v114 offset:26624\n"
"buffer_load_dwordx4 v113, s[64:67], s40 offen offset:3072 sc1 lds\n"
"v_mfma_f32_16x16x32_bf16 v[72:75], a[28:31], v[40:43], v[72:75]\n"
"s_waitcnt lgkmcnt(10)\n"
"ds_read_b64_tr_b16 a[214:215], v120 offset:50184\n"
"v_mfma_f32_16x16x32_bf16 v[68:71], a[32:35], v[44:47], v[68:71]\n"
"s_add_u32 s40, s40, 16384\n"
"ds_read_b128 v[40:43], v116 offset:26624\n"
"v_mfma_f32_16x16x32_bf16 v[72:75], a[36:39], v[44:47], v[72:75]\n"
"ds_read_b64_tr_b16 a[216:217], v120 offset:57344\n"
"v_mfma_f32_16x16x32_bf16 v[68:71], a[40:43], v[48:51], v[68:71]\n"
"ds_read_b128 v[44:47], v114 offset:28672\n"
"v_mfma_f32_16x16x32_bf16 v[72:75], a[44:47], v[48:51], v[72:75]\n"
"s_waitcnt lgkmcnt(10)\n"
"ds_read_b64_tr_b16 a[218:219], v120 offset:58368\n"
"v_mfma_f32_16x16x32_bf16 v[68:71], a[48:51], v[52:55], v[68:71]\n"
"ds_read_b128 v[48:51], v116 offset:28672\n"
"v_mfma_f32_16x16x32_bf16 v[72:75], a[52:55], v[52:55], v[72:75]\n"
"ds_read_b64_tr_b16 a[220:221], v120 offset:57352\n"
"v_mfma_f32_16x16x32_bf16 v[68:71], a[56:59], v[56:59], v[68:71]\n"
"ds_read_b128 v[52:55], v114 offset:30720\n"
"v_mfma_f32_16x16x32_bf16 v[72:75], a[60:63], v[56:59], v[72:75]\n"
"s_waitcnt lgkmcnt(10)\n"
"ds_read_b64_tr_b16 a[222:223], v120 offset:58376\n"
"v_mfma_f32_16x16x32_bf16 v[68:71], a[64:67], v[28:31], v[68:71]\n"
"s_waitcnt vmcnt(8)\n"
"ds_read_b128 v[56:59], v116 offset:30720\n"
"v_mfma_f32_16x16x32_bf16 v[72:75], a[68:71], v[28:31], v[72:75]\n"
"s_barrier\n"
"ds_read_b64_tr_b16 a[224:225], v121 offset:0\n"
"v_mfma_f32_16x16x32_bf16 v[68:71], a[72:75], v[32:35], v[68:71]\n"
"s_add_u32 m0, s44, 0\n"
"ds_read_b128 v[28:31], v114 offset:32768\n"
"v_mfma_f32_16x16x32_bf16 v[72:75], a[76:79], v[32:35], v[72:75]\n"
"s_waitcnt lgkmcnt(10)\n"
"ds_read_b64_tr_b16 a[226:227], v121 offset:1024\n"
"v_mfma_f32_16x16x32_bf16 v[68:71], a[80:83], v[36:39], v[68:71]\n"
"s_add_u32 s61, s43, 0\n"
"ds_read_b128 v[32:35], v116 offset:32768\n"
"buffer_load_dwordx4 v113, s[64:67], s40 offen offset:0 sc1 lds\n"
"v_mfma_f32_16x16x32_bf16 v[72:75], a[84:87], v[36:39], v[72:75]\n"
"buffer_load_dword v23, v3, s[80:83], s61 offen\n"
"ds_read_b64_tr_b16 a[228:229], v121 offset:8\n"
"v_mfma_f32_16x16x32_bf16 v[68:71], a[88:91], v[40:43], v[68:71]\n"
"ds_read_b128 v[36:39], v114 offset:34816\n"
"v_mfma_f32_16x16x32_bf16 v[72:75], a[92:95], v[40:43], v[72:75]\n"
"s_waitcnt lgkmcnt(10)\n"
"ds_read_b64_tr_b16 a[230:231], v121 offset:1032\n"
"v_mfma_f32_16x16x32_bf16 v[68:71], a[96:99], v[44:47], v[68:71]\n"
"s_add_u32 s61, s43, 262144\n"
"ds_read_b128 v[40:43], v116 offset:34816\n"
"v_mfma_f32_16x16x32_bf16 v[72:75], a[100:103], v[44:47], v[72:75]\n"
"buffer_load_dword v23, v3, s[80:83], s61 offen\n"
"ds_read_b64_tr_b16 a[232:233], v121 offset:8192\n"
"v_mfma_f32_16x16x32_bf16 v[68:71], a[104:107], v[48:51], v[68:71]\n"
"ds_read_b128 v[44:47], v114 offset:36864\n"
"buffer_load_dwordx4 v113, s[64:67], s40 offen offset:1024 sc1 lds\n"
"v_mfma_f32_16x16x32_bf16 v[72:75], a[108:111], v[48:51], v[72:75]\n"
"s_waitcnt lgkmcnt(10)\n"
"ds_read_b64_tr_b16 a[234:235], v121 offset:9216\n"
"v_mfma_f32_16x16x32_bf16 v[68:71], a[112:115], v[52:55], v[68:71]\n"
"s_add_u32 s61, s43, 524288\n"
"ds_read_b128 v[48:51], v116 offset:36864\n"
"v_mfma_f32_16x16x32_bf16 v[72:75], a[116:119], v[52:55], v[72:75]\n"
"buffer_load_dword v23, v3, s[80:83], s61 offen\n"
"ds_read_b64_tr_b16 a[236:237], v121 offset:8200\n"
"v_mfma_f32_16x16x32_bf16 v[68:71], a[120:123], v[56:59], v[68:71]\n"
"ds_read_b128 v[52:55], v114 offset:38912\n"
"v_mfma_f32_16x16x32_bf16 v[72:75], a[124:127], v[56:59], v[72:75]\n"
"s_waitcnt lgkmcnt(10)\n"
"ds_read_b64_tr_b16 a[238:239], v121 offset:9224\n"
"v_mfma_f32_16x16x32_bf16 v[60:63], a[128:131], v[28:31], v[60:63]\n"
"s_add_u32 s61, s43, 786432\n"
"ds_read_b128 v[56:59], v116 offset:38912\n"
"buffer_load_dwordx4 v113, s[64:67], s40 offen offset:2048 sc1 lds\n"
"v_mfma_f32_16x16x32_bf16 v[64:67], a[132:135], v[28:31], v[64:67]\n"
"buffer_load_dword v23, v3, s[80:83], s61 offen\n"
"ds_read_b64_tr_b16 a[240:241], v121 offset:16384\n"
"v_mfma_f32_16x16x32_bf16 v[60:63], a[136:139], v[32:35], v[60:63]\n"
"ds_read_b128 v[28:31], v114 offset:40960\n"
"v_mfma_f32_16x16x32_bf16 v[64:67], a[140:143], v[32:35], v[64:67]\n"
"s_waitcnt lgkmcnt(10)\n"
"ds_read_b64_tr_b16 a[242:243], v121 offset:17408\n"
"v_mfma_f32_16x16x32_bf16 v[60:63], a[144:147], v[36:39], v[60:63]\n"
"s_add_u32 s61, s43, 1048576\n"
"ds_read_b128 v[32:35], v116 offset:40960\n"
"v_mfma_f32_16x16x32_bf16 v[64:67], a[148:151], v[36:39], v[64:67]\n"
"buffer_load_dword v23, v3, s[80:83], s61 offen\n"
"ds_read_b64_tr_b16 a[244:245], v121 offset:16392\n"
"v_mfma_f32_16x16x32_bf16 v[60:63], a[152:155], v[40:43], v[60:63]\n"
"ds_read_b128 v[36:39], v114 offset:43008\n"
"buffer_load_dwordx4 v113, s[64:67], s40 offen offset:3072 sc1 lds\n"
"v_mfma_f32_16x16x32_bf16 v[64:67], a[156:159], v[40:43], v[64:67]\n"
"s_waitcnt lgkmcnt(10)\n"
"ds_read_b64_tr_b16 a[246:247], v121 offset:17416\n"
"v_mfma_f32_16x16x32_bf16 v[60:63], a[160:163], v[44:47], v[60:63]\n"
"s_add_u32 s40, s40, 278528\n"
"ds_read_b128 v[40:43], v116 offset:43008\n"
"v_mfma_f32_16x16x32_bf16 v[64:67], a[164:167], v[44:47], v[64:67]\n"
"s_add_u32 s61, s43, 1310720\n"
"ds_read_b64_tr_b16 a[248:249], v121 offset:24576\n"
"v_mfma_f32_16x16x32_bf16 v[60:63], a[168:171], v[48:51], v[60:63]\n"
"ds_read_b128 v[44:47], v114 offset:45056\n"
"buffer_load_dword v23, v3, s[80:83], s61 offen\n"
"v_mfma_f32_16x16x32_bf16 v[64:67], a[172:175], v[48:51], v[64:67]\n"
"s_waitcnt lgkmcnt(10)\n"
"ds_read_b64_tr_b16 a[250:251], v121 offset:25600\n"
"v_mfma_f32_16x16x32_bf16 v[60:63], a[176:179], v[52:55], v[60:63]\n"
"s_add_u32 s61, s43, 1572864\n"
"ds_read_b128 v[48:51], v116 offset:45056\n"
"v_mfma_f32_16x16x32_bf16 v[64:67], a[180:183], v[52:55], v[64:67]\n"
"buffer_load_dword v23, v3, s[80:83], s61 offen\n"
"ds_read_b64_tr_b16 a[252:253], v121 offset:24584\n"
"v_mfma_f32_16x16x32_bf16 v[60:63], a[184:187], v[56:59], v[60:63]\n"
"ds_read_b128 v[52:55], v114 offset:47104\n"
"v_mfma_f32_16x16x32_bf16 v[64:67], a[188:191], v[56:59], v[64:67]\n"
"s_waitcnt lgkmcnt(10)\n"
"ds_read_b64_tr_b16 a[254:255], v121 offset:25608\n"
"v_mfma_f32_16x16x32_bf16 v[60:63], a[192:195], v[28:31], v[60:63]\n"
"s_waitcnt vmcnt(15)\n"
"ds_read_b128 v[56:59], v116 offset:47104\n"
"v_mfma_f32_16x16x32_bf16 v[64:67], a[196:199], v[28:31], v[64:67]\n"
"s_barrier\n"
"v_mfma_f32_16x16x32_bf16 v[60:63], a[200:203], v[32:35], v[60:63]\n"
"s_add_u32 m0, s44, 16384\n"
"ds_read_b128 v[28:31], v114 offset:49152\n"
"v_mfma_f32_16x16x32_bf16 v[64:67], a[204:207], v[32:35], v[64:67]\n"
"s_waitcnt lgkmcnt(9)\n"
"v_mfma_f32_16x16x32_bf16 v[60:63], a[208:211], v[36:39], v[60:63]\n"
"ds_read_b128 v[32:35], v116 offset:49152\n"
"buffer_load_dwordx4 v113, s[64:67], s40 offen offset:0 sc1 lds\n"
"v_mfma_f32_16x16x32_bf16 v[64:67], a[212:215], v[36:39], v[64:67]\n"
"v_mfma_f32_16x16x32_bf16 v[60:63], a[216:219], v[40:43], v[60:63]\n"
"ds_read_b128 v[36:39], v114 offset:51200\n"
"v_mfma_f32_16x16x32_bf16 v[64:67], a[220:223], v[40:43], v[64:67]\n"
"s_waitcnt lgkmcnt(7)\n"
"v_mfma_f32_16x16x32_bf16 v[60:63], a[224:227], v[44:47], v[60:63]\n"
"ds_read_b128 v[40:43], v116 offset:51200\n"
"v_mfma_f32_16x16x32_bf16 v[64:67], a[228:231], v[44:47], v[64:67]\n"
"v_mfma_f32_16x16x32_bf16 v[60:63], a[232:235], v[48:51], v[60:63]\n"
"ds_read_b128 v[44:47], v114 offset:53248\n"
"buffer_load_dwordx4 v113, s[64:67], s40 offen offset:1024 sc1 lds\n"
"v_mfma_f32_16x16x32_bf16 v[64:67], a[236:239], v[48:51], v[64:67]\n"
"s_waitcnt lgkmcnt(5)\n"
"v_mfma_f32_16x16x32_bf16 v[60:63], a[240:243], v[52:55], v[60:63]\n"
"ds_read_b128 v[48:51], v116 offset:53248\n"
"v_mfma_f32_16x16x32_bf16 v[64:67], a[244:247], v[52:55], v[64:67]\n"
"v_mfma_f32_16x16x32_bf16 v[60:63], a[248:251], v[56:59], v[60:63]\n"
"ds_read_b128 v[52:55], v114 offset:55296\n"
"v_mfma_f32_16x16x32_bf16 v[64:67], a[252:255], v[56:59], v[64:67]\n"
"s_waitcnt lgkmcnt(5)\n"
"v_mfma_f32_16x16x32_bf16 v[68:71], a[128:131], v[28:31], v[68:71]\n"
"ds_read_b128 v[56:59], v116 offset:55296\n"
"buffer_load_dwordx4 v113, s[64:67], s40 offen offset:2048 sc1 lds\n"
"v_mfma_f32_16x16x32_bf16 v[72:75], a[132:135], v[28:31], v[72:75]\n"
"v_mfma_f32_16x16x32_bf16 v[68:71], a[136:139], v[32:35], v[68:71]\n"
"ds_read_b128 v[28:31], v114 offset:57344\n"
"v_mfma_f32_16x16x32_bf16 v[72:75], a[140:143], v[32:35], v[72:75]\n"
"s_waitcnt lgkmcnt(5)\n"
"v_mfma_f32_16x16x32_bf16 v[68:71], a[144:147], v[36:39], v[68:71]\n"
"ds_read_b128 v[32:35], v116 offset:57344\n"
"v_mul_f32 v76, v60, v60\n"
"v_mul_f32 v77, v61, v61\n"
"v_mul_f32 v78, v62, v62\n"
"v_mfma_f32_16x16x32_bf16 v[72:75], a[148:151], v[36:39], v[72:75]\n"
"v_fmamk_f32 v76, v76, 0xbdd2d3e2, v112\n"
"v_fmamk_f32 v77, v77, 0xbdd2d3e2, v112\n"
"v_fmamk_f32 v78, v78, 0xbdd2d3e2, v112\n"
"v_mul_f32 v79, v63, v63\n"
"v_mfma_f32_16x16x32_bf16 v[68:71], a[152:155], v[40:43], v[68:71]\n"
"ds_read_b128 v[36:39], v114 offset:59392\n"
"buffer_load_dwordx4 v113, s[64:67], s40 offen offset:3072 sc1 lds\n"
"v_mul_f32 v76, v76, v60\n"
"v_mul_f32 v77, v77, v61\n"
"v_mfma_f32_16x16x32_bf16 v[72:75], a[156:159], v[40:43], v[72:75]\n"
"s_waitcnt lgkmcnt(5)\n"
"v_exp_f32 v76, v76\n"
"v_exp_f32 v77, v77\n"
"v_mfma_f32_16x16x32_bf16 v[68:71], a[160:163], v[44:47], v[68:71]\n"
"s_add_u32 s40, s40, 16384\n"
"ds_read_b128 v[40:43], v116 offset:59392\n"
"v_add_f32 v76, 1.0, v76\n"
"v_add_f32 v77, 1.0, v77\n"
"v_mul_f32 v78, v78, v62\n"
"v_mfma_f32_16x16x32_bf16 v[72:75], a[164:167], v[44:47], v[72:75]\n"
"v_rcp_f32 v76, v76\n"
"v_rcp_f32 v77, v77\n"
"v_mfma_f32_16x16x32_bf16 v[68:71], a[168:171], v[48:51], v[68:71]\n"
"ds_read_b128 v[44:47], v114 offset:61440\n"
"v_exp_f32 v78, v78\n"
"v_fmamk_f32 v79, v79, 0xbdd2d3e2, v112\n"
"v_mfma_f32_16x16x32_bf16 v[72:75], a[172:175], v[48:51], v[72:75]\n"
"s_waitcnt lgkmcnt(5)\n"
"v_add_f32 v78, 1.0, v78\n"
"v_mul_f32 v79, v79, v63\n"
"v_mul_f32 v80, v64, v64\n"
"v_mul_f32 v81, v65, v65\n"
"v_mfma_f32_16x16x32_bf16 v[68:71], a[176:179], v[52:55], v[68:71]\n"
"ds_read_b128 v[48:51], v116 offset:61440\n"
"v_rcp_f32 v78, v78\n"
"v_fmamk_f32 v80, v80, 0xbdd2d3e2, v112\n"
"v_mfma_f32_16x16x32_bf16 v[72:75], a[180:183], v[52:55], v[72:75]\n"
"v_exp_f32 v79, v79\n"
"v_mul_f32 v80, v80, v64\n"
"v_fmamk_f32 v81, v81, 0xbdd2d3e2, v112\n"
"v_mfma_f32_16x16x32_bf16 v[68:71], a[184:187], v[56:59], v[68:71]\n"
"ds_read_b128 v[52:55], v114 offset:63488\n"
"v_exp_f32 v80, v80\n"
"v_add_f32 v79, 1.0, v79\n"
"v_mfma_f32_16x16x32_bf16 v[72:75], a[188:191], v[56:59], v[72:75]\n"
"s_waitcnt lgkmcnt(5)\n"
"v_rcp_f32 v79, v79\n"
"v_add_f32 v80, 1.0, v80\n"
"v_mul_f32 v81, v81, v65\n"
"v_mfma_f32_16x16x32_bf16 v[68:71], a[192:195], v[28:31], v[68:71]\n"
"s_waitcnt vmcnt(15)\n"
"ds_read_b128 v[56:59], v116 offset:63488\n"
"v_rcp_f32 v80, v80\n"
"v_mul_f32 v82, v66, v66\n"
"v_mfma_f32_16x16x32_bf16 v[72:75], a[196:199], v[28:31], v[72:75]\n"
"s_barrier\n"
"v_exp_f32 v81, v81\n"
"v_fmamk_f32 v82, v82, 0xbdd2d3e2, v112\n"
"v_mul_f32 v83, v67, v67\n"
"v_mfma_f32_16x16x32_bf16 v[68:71], a[200:203], v[32:35], v[68:71]\n"
"s_add_u32 m0, s44, 32768\n"
"ds_read_b128 v[28:31], v115 offset:0\n"
"v_add_f32 v81, 1.0, v81\n"
"v_mul_f32 v82, v82, v66\n"
"v_mfma_f32_16x16x32_bf16 v[72:75], a[204:207], v[32:35], v[72:75]\n"
"s_waitcnt lgkmcnt(5)\n"
"v_rcp_f32 v81, v81\n"
"v_fmamk_f32 v83, v83, 0xbdd2d3e2, v112\n"
"v_mfma_f32_16x16x32_bf16 v[68:71], a[208:211], v[36:39], v[68:71]\n"
"ds_read_b128 v[32:35], v117 offset:0\n"
"buffer_load_dwordx4 v113, s[64:67], s40 offen offset:0 sc1 lds\n"
"v_mul_f32 v83, v83, v67\n"
"v_mfma_f32_16x16x32_bf16 v[72:75], a[212:215], v[36:39], v[72:75]\n"
"v_exp_f32 v82, v82\n"
"v_mul_f32 v76, v60, v76\n"
"v_mfma_f32_16x16x32_bf16 v[68:71], a[216:219], v[40:43], v[68:71]\n"
"ds_read_b128 v[36:39], v115 offset:2048\n"
"buffer_load_dwordx4 v113, s[64:67], s40 offen offset:1024 sc1 lds\n"
"v_add_f32 v82, 1.0, v82\n"
"v_mfma_f32_16x16x32_bf16 v[72:75], a[220:223], v[40:43], v[72:75]\n"
"s_waitcnt lgkmcnt(5)\n"
"v_rcp_f32 v82, v82\n"
"v_mul_f32 v77, v61, v77\n"
"v_mfma_f32_16x16x32_bf16 v[68:71], a[224:227], v[44:47], v[68:71]\n"
"ds_read_b128 v[40:43], v117 offset:2048\n"
"buffer_load_dwordx4 v113, s[64:67], s40 offen offset:2048 sc1 lds\n"
"v_mul_f32 v78, v62, v78\n"
"v_mfma_f32_16x16x32_bf16 v[72:75], a[228:231], v[44:47], v[72:75]\n"
"v_exp_f32 v83, v83\n"
"v_mul_f32 v79, v63, v79\n"
"v_mfma_f32_16x16x32_bf16 v[68:71], a[232:235], v[48:51], v[68:71]\n"
"ds_read_b128 v[44:47], v115 offset:4096\n"
"buffer_load_dwordx4 v113, s[64:67], s40 offen offset:3072 sc1 lds\n"
"v_add_f32 v83, 1.0, v83\n"
"v_mfma_f32_16x16x32_bf16 v[72:75], a[236:239], v[48:51], v[72:75]\n"
"s_waitcnt lgkmcnt(5)\n"
"v_rcp_f32 v83, v83\n"
"v_mul_f32 v80, v64, v80\n"
"v_mfma_f32_16x16x32_bf16 v[68:71], a[240:243], v[52:55], v[68:71]\n"
"s_add_u32 s40, s40, 278528\n"
"ds_read_b128 v[48:51], v117 offset:4096\n"
"v_mul_f32 v81, v65, v81\n"
"v_mul_f32 v82, v66, v82\n"
"v_mfma_f32_16x16x32_bf16 v[72:75], a[244:247], v[52:55], v[72:75]\n"
"s_add_u32 s95, s43, 0\n"
"v_mul_f32 v83, v67, v83\n"
"v_cvt_pk_bf16_f32 v84, v76, v77\n"
"v_cvt_pk_bf16_f32 v85, v78, v79\n"
"v_mfma_f32_16x16x32_bf16 v[68:71], a[248:251], v[56:59], v[68:71]\n"
"s_add_u32 m0, s44, 81920\n"
"ds_read_b128 v[52:55], v115 offset:6144\n"
"v_cvt_pk_bf16_f32 v86, v80, v81\n"
"v_cvt_pk_bf16_f32 v87, v82, v83\n"
"v_mfma_f32_16x16x32_bf16 v[72:75], a[252:255], v[56:59], v[72:75]\n"
"s_waitcnt lgkmcnt(5)\n"
"v_mfma_f32_16x16x32_bf16 v[60:63], a[0:3], v[28:31], v[88:91]\n"
"ds_read_b128 v[56:59], v117 offset:6144\n"
"buffer_load_dwordx4 v122, s[80:83], s95 offen offset:0 lds\n"
"v_mfma_f32_16x16x32_bf16 v[64:67], a[4:7], v[28:31], v[92:95]\n"
"buffer_store_dwordx4 v[84:87], v113, s[68:71], s41 offen sc1\n"
"v_mfma_f32_16x16x32_bf16 v[60:63], a[8:11], v[32:35], v[60:63]\n"
"s_add_u32 s41, s41, 1032192\n"
"ds_read_b128 v[28:31], v115 offset:8192\n"
"buffer_load_dwordx4 v123, s[80:83], s95 offen offset:1024 lds\n"
"v_mfma_f32_16x16x32_bf16 v[64:67], a[12:15], v[32:35], v[64:67]\n"
"s_waitcnt lgkmcnt(5)\n"
"v_mul_f32 v76, v68, v68\n"
"v_mul_f32 v77, v69, v69\n"
"v_mfma_f32_16x16x32_bf16 v[60:63], a[16:19], v[36:39], v[60:63]\n"
"ds_read_b128 v[32:35], v117 offset:8192\n"
"buffer_load_dwordx4 v124, s[80:83], s95 offen offset:2048 lds\n"
"v_mfma_f32_16x16x32_bf16 v[64:67], a[20:23], v[36:39], v[64:67]\n"
"v_fmamk_f32 v76, v76, 0xbdd2d3e2, v112\n"
"v_fmamk_f32 v77, v77, 0xbdd2d3e2, v112\n"
"v_mfma_f32_16x16x32_bf16 v[60:63], a[24:27], v[40:43], v[60:63]\n"
"ds_read_b128 v[36:39], v115 offset:10240\n"
"buffer_load_dwordx4 v125, s[80:83], s95 offen offset:3072 lds\n"
"v_mfma_f32_16x16x32_bf16 v[64:67], a[28:31], v[40:43], v[64:67]\n"
"s_waitcnt lgkmcnt(5)\n"
"v_mul_f32 v76, v76, v68\n"
"v_mul_f32 v77, v77, v69\n"
"v_mfma_f32_16x16x32_bf16 v[60:63], a[32:35], v[44:47], v[60:63]\n"
"ds_read_b128 v[40:43], v117 offset:10240\n"
"v_mul_f32 v78, v70, v70\n"
"v_mfma_f32_16x16x32_bf16 v[64:67], a[36:39], v[44:47], v[64:67]\n"
"v_exp_f32 v76, v76\n"
"v_mfma_f32_16x16x32_bf16 v[60:63], a[40:43], v[48:51], v[60:63]\n"
"ds_read_b128 v[44:47], v115 offset:12288\n"
"v_add_f32 v76, 1.0, v76\n"
"v_mfma_f32_16x16x32_bf16 v[64:67], a[44:47], v[48:51], v[64:67]\n"
"s_waitcnt lgkmcnt(5)\n"
"v_rcp_f32 v76, v76\n"
"v_mfma_f32_16x16x32_bf16 v[60:63], a[48:51], v[52:55], v[60:63]\n"
"ds_read_b128 v[48:51], v117 offset:12288\n"
"v_fmamk_f32 v78, v78, 0xbdd2d3e2, v112\n"
"v_mfma_f32_16x16x32_bf16 v[64:67], a[52:55], v[52:55], v[64:67]\n"
"v_exp_f32 v77, v77\n"
"v_mfma_f32_16x16x32_bf16 v[60:63], a[56:59], v[56:59], v[60:63]\n"
"ds_read_b128 v[52:55], v115 offset:14336\n"
"v_add_f32 v77, 1.0, v77\n"
"v_mfma_f32_16x16x32_bf16 v[64:67], a[60:63], v[56:59], v[64:67]\n"
"s_waitcnt lgkmcnt(5)\n"
"v_rcp_f32 v77, v77\n"
"v_mfma_f32_16x16x32_bf16 v[60:63], a[64:67], v[28:31], v[60:63]\n"
"s_waitcnt vmcnt(15)\n"
"ds_read_b128 v[56:59], v117 offset:14336\n"
"v_mul_f32 v78, v78, v70\n"
"v_mfma_f32_16x16x32_bf16 v[64:67], a[68:71], v[28:31], v[64:67]\n"
"s_barrier\n"
"v_exp_f32 v78, v78\n"
"v_mfma_f32_16x16x32_bf16 v[60:63], a[72:75], v[32:35], v[60:63]\n"
"s_add_u32 m0, s44, 49152\n"
"ds_read_b128 v[28:31], v114 offset:0\n"
"v_add_f32 v78, 1.0, v78\n"
"v_mfma_f32_16x16x32_bf16 v[64:67], a[76:79], v[32:35], v[64:67]\n"
"s_waitcnt lgkmcnt(5)\n"
"v_rcp_f32 v78, v78\n"
"v_mfma_f32_16x16x32_bf16 v[60:63], a[80:83], v[36:39], v[60:63]\n"
"ds_read_b128 v[32:35], v116 offset:0\n"
"buffer_load_dwordx4 v113, s[64:67], s40 offen offset:0 sc1 lds\n"
"v_mfma_f32_16x16x32_bf16 v[64:67], a[84:87], v[36:39], v[64:67]\n"
"v_mul_f32 v79, v71, v71\n"
"v_mul_f32 v80, v72, v72\n"
"v_mfma_f32_16x16x32_bf16 v[60:63], a[88:91], v[40:43], v[60:63]\n"
"ds_read_b128 v[36:39], v114 offset:2048\n"
"v_fmamk_f32 v79, v79, 0xbdd2d3e2, v112\n"
"v_mfma_f32_16x16x32_bf16 v[64:67], a[92:95], v[40:43], v[64:67]\n"
"s_waitcnt lgkmcnt(5)\n"
"v_mul_f32 v79, v79, v71\n"
"v_fmamk_f32 v80, v80, 0xbdd2d3e2, v112\n"
"v_mfma_f32_16x16x32_bf16 v[60:63], a[96:99], v[44:47], v[60:63]\n"
"ds_read_b128 v[40:43], v116 offset:2048\n"
"v_mul_f32 v80, v80, v72\n"
"v_mfma_f32_16x16x32_bf16 v[64:67], a[100:103], v[44:47], v[64:67]\n"
"v_exp_f32 v79, v79\n"
"v_mfma_f32_16x16x32_bf16 v[60:63], a[104:107], v[48:51], v[60:63]\n"
"ds_read_b128 v[44:47], v114 offset:4096\n"
"buffer_load_dwordx4 v113, s[64:67], s40 offen offset:1024 sc1 lds\n"
"v_mfma_f32_16x16x32_bf16 v[64:67], a[108:111], v[48:51], v[64:67]\n"
"s_waitcnt lgkmcnt(5)\n"
"v_exp_f32 v80, v80\n"
"v_mfma_f32_16x16x32_bf16 v[60:63], a[112:115], v[52:55], v[60:63]\n"
"ds_read_b128 v[48:51], v116 offset:4096\n"
"v_add_f32 v79, 1.0, v79\n"
"v_mfma_f32_16x16x32_bf16 v[64:67], a[116:119], v[52:55], v[64:67]\n"
"v_rcp_f32 v79, v79\n"
"v_mfma_f32_16x16x32_bf16 v[60:63], a[120:123], v[56:59], v[60:63]\n"
"ds_read_b128 v[52:55], v114 offset:6144\n"
"v_add_f32 v80, 1.0, v80\n"
"v_mfma_f32_16x16x32_bf16 v[64:67], a[124:127], v[56:59], v[64:67]\n"
"s_waitcnt lgkmcnt(5)\n"
"v_rcp_f32 v80, v80\n"
"v_mfma_f32_16x16x32_bf16 v[60:63], a[128:131], v[28:31], v[60:63]\n"
"ds_read_b128 v[56:59], v116 offset:6144\n"
"buffer_load_dwordx4 v113, s[64:67], s40 offen offset:2048 sc1 lds\n"
"v_mfma_f32_16x16x32_bf16 v[64:67], a[132:135], v[28:31], v[64:67]\n"
"v_mul_f32 v81, v73, v73\n"
"v_mul_f32 v82, v74, v74\n"
"v_mfma_f32_16x16x32_bf16 v[60:63], a[136:139], v[32:35], v[60:63]\n"
"ds_read_b128 v[28:31], v114 offset:8192\n"
"v_fmamk_f32 v81, v81, 0xbdd2d3e2, v112\n"
"v_mfma_f32_16x16x32_bf16 v[64:67], a[140:143], v[32:35], v[64:67]\n"
"s_waitcnt lgkmcnt(5)\n"
"v_mul_f32 v81, v81, v73\n"
"v_fmamk_f32 v82, v82, 0xbdd2d3e2, v112\n"
"v_mfma_f32_16x16x32_bf16 v[60:63], a[144:147], v[36:39], v[60:63]\n"
"ds_read_b128 v[32:35], v116 offset:8192\n"
"v_mul_f32 v82, v82, v74\n"
"v_mfma_f32_16x16x32_bf16 v[64:67], a[148:151], v[36:39], v[64:67]\n"
"v_exp_f32 v81, v81\n"
"v_mfma_f32_16x16x32_bf16 v[60:63], a[152:155], v[40:43], v[60:63]\n"
"ds_read_b128 v[36:39], v114 offset:10240\n"
"buffer_load_dwordx4 v113, s[64:67], s40 offen offset:3072 sc1 lds\n"
"v_mfma_f32_16x16x32_bf16 v[64:67], a[156:159], v[40:43], v[64:67]\n"
"s_waitcnt lgkmcnt(5)\n"
"v_exp_f32 v82, v82\n"
"v_mfma_f32_16x16x32_bf16 v[60:63], a[160:163], v[44:47], v[60:63]\n"
"s_add_u32 s40, s40, 16384\n"
"ds_read_b128 v[40:43], v116 offset:10240\n"
"v_add_f32 v81, 1.0, v81\n"
"v_mfma_f32_16x16x32_bf16 v[64:67], a[164:167], v[44:47], v[64:67]\n"
"v_rcp_f32 v81, v81\n"
"v_mfma_f32_16x16x32_bf16 v[60:63], a[168:171], v[48:51], v[60:63]\n"
"ds_read_b128 v[44:47], v114 offset:12288\n"
"v_add_f32 v82, 1.0, v82\n"
"v_mfma_f32_16x16x32_bf16 v[64:67], a[172:175], v[48:51], v[64:67]\n"
"s_waitcnt lgkmcnt(5)\n"
"v_rcp_f32 v82, v82\n"
"v_mfma_f32_16x16x32_bf16 v[60:63], a[176:179], v[52:55], v[60:63]\n"
"ds_read_b128 v[48:51], v116 offset:12288\n"
"v_mul_f32 v83, v75, v75\n"
"v_mfma_f32_16x16x32_bf16 v[64:67], a[180:183], v[52:55], v[64:67]\n"
"v_fmamk_f32 v83, v83, 0xbdd2d3e2, v112\n"
"v_mul_f32 v76, v68, v76\n"
"v_mfma_f32_16x16x32_bf16 v[60:63], a[184:187], v[56:59], v[60:63]\n"
"ds_read_b128 v[52:55], v114 offset:14336\n"
"v_mul_f32 v83, v83, v75\n"
"v_mfma_f32_16x16x32_bf16 v[64:67], a[188:191], v[56:59], v[64:67]\n"
"s_waitcnt lgkmcnt(5)\n"
"v_exp_f32 v83, v83\n"
"v_mfma_f32_16x16x32_bf16 v[60:63], a[192:195], v[28:31], v[60:63]\n"
"s_waitcnt vmcnt(13)\n"
"ds_read_b128 v[56:59], v116 offset:14336\n"
"v_add_f32 v83, 1.0, v83\n"
"v_mfma_f32_16x16x32_bf16 v[64:67], a[196:199], v[28:31], v[64:67]\n"
"s_barrier\n"
"v_rcp_f32 v83, v83\n"
"v_mfma_f32_16x16x32_bf16 v[60:63], a[200:203], v[32:35], v[60:63]\n"
"s_add_u32 m0, s44, 65536\n"
"ds_read_b128 v[28:31], v114 offset:16384\n"
"v_mul_f32 v77, v69, v77\n"
"v_mfma_f32_16x16x32_bf16 v[64:67], a[204:207], v[32:35], v[64:67]\n"
"s_waitcnt lgkmcnt(5)\n"
"v_mul_f32 v78, v70, v78\n"
"v_mul_f32 v79, v71, v79\n"
"v_mfma_f32_16x16x32_bf16 v[60:63], a[208:211], v[36:39], v[60:63]\n"
"ds_read_b128 v[32:35], v116 offset:16384\n"
"buffer_load_dwordx4 v113, s[64:67], s40 offen offset:0 sc1 lds\n"
"v_mfma_f32_16x16x32_bf16 v[64:67], a[212:215], v[36:39], v[64:67]\n"
"v_mul_f32 v80, v72, v80\n"
"v_mul_f32 v81, v73, v81\n"
"v_mfma_f32_16x16x32_bf16 v[60:63], a[216:219], v[40:43], v[60:63]\n"
"ds_read_b128 v[36:39], v114 offset:18432\n"
"buffer_load_dwordx4 v113, s[64:67], s40 offen offset:1024 sc1 lds\n"
"v_mfma_f32_16x16x32_bf16 v[64:67], a[220:223], v[40:43], v[64:67]\n"
"s_waitcnt lgkmcnt(5)\n"
"v_mul_f32 v82, v74, v82\n"
"v_mul_f32 v83, v75, v83\n"
"v_mfma_f32_16x16x32_bf16 v[60:63], a[224:227], v[44:47], v[60:63]\n"
"ds_read_b128 v[40:43], v116 offset:18432\n"
"buffer_load_dwordx4 v113, s[64:67], s40 offen offset:2048 sc1 lds\n"
"v_mfma_f32_16x16x32_bf16 v[64:67], a[228:231], v[44:47], v[64:67]\n"
"v_cvt_pk_bf16_f32 v84, v76, v77\n"
"v_cvt_pk_bf16_f32 v85, v78, v79\n"
"v_mfma_f32_16x16x32_bf16 v[60:63], a[232:235], v[48:51], v[60:63]\n"
"ds_read_b128 v[44:47], v114 offset:20480\n"
"buffer_load_dwordx4 v113, s[64:67], s40 offen offset:3072 sc1 lds\n"
"v_mfma_f32_16x16x32_bf16 v[64:67], a[236:239], v[48:51], v[64:67]\n"
"s_waitcnt lgkmcnt(5)\n"
"v_cvt_pk_bf16_f32 v86, v80, v81\n"
"v_cvt_pk_bf16_f32 v87, v82, v83\n"
"v_mfma_f32_16x16x32_bf16 v[60:63], a[240:243], v[52:55], v[60:63]\n"
"s_add_u32 s40, s40, 278528\n"
"ds_read_b128 v[48:51], v116 offset:20480\n"
"v_mfma_f32_16x16x32_bf16 v[64:67], a[244:247], v[52:55], v[64:67]\n"
"s_add_u32 s95, s43, 131072\n"
"v_mfma_f32_16x16x32_bf16 v[60:63], a[248:251], v[56:59], v[60:63]\n"
"s_add_u32 m0, s44, 98304\n"
"ds_read_b128 v[52:55], v114 offset:22528\n"
"v_mfma_f32_16x16x32_bf16 v[64:67], a[252:255], v[56:59], v[64:67]\n"
"s_waitcnt lgkmcnt(5)\n"
"v_mfma_f32_16x16x32_bf16 v[68:71], a[0:3], v[28:31], v[88:91]\n"
"ds_read_b128 v[56:59], v116 offset:22528\n"
"buffer_load_dwordx4 v122, s[80:83], s95 offen offset:0 lds\n"
"v_mfma_f32_16x16x32_bf16 v[72:75], a[4:7], v[28:31], v[92:95]\n"
"buffer_store_dwordx4 v[84:87], v113, s[68:71], s41 offen sc1\n"
"v_mfma_f32_16x16x32_bf16 v[68:71], a[8:11], v[32:35], v[68:71]\n"
"s_add_u32 s41, s41, 1032192\n"
"ds_read_b128 v[28:31], v114 offset:24576\n"
"buffer_load_dwordx4 v123, s[80:83], s95 offen offset:1024 lds\n"
"v_mfma_f32_16x16x32_bf16 v[72:75], a[12:15], v[32:35], v[72:75]\n"
"s_waitcnt lgkmcnt(5)\n"
"v_mul_f32 v76, v60, v60\n"
"v_mul_f32 v77, v61, v61\n"
"v_mul_f32 v78, v62, v62\n"
"v_mfma_f32_16x16x32_bf16 v[68:71], a[16:19], v[36:39], v[68:71]\n"
"ds_read_b128 v[32:35], v116 offset:24576\n"
"buffer_load_dwordx4 v124, s[80:83], s95 offen offset:2048 lds\n"
"v_fmamk_f32 v76, v76, 0xbdd2d3e2, v112\n"
"v_mfma_f32_16x16x32_bf16 v[72:75], a[20:23], v[36:39], v[72:75]\n"
"v_mul_f32 v76, v76, v60\n"
"v_fmamk_f32 v77, v77, 0xbdd2d3e2, v112\n"
"v_fmamk_f32 v78, v78, 0xbdd2d3e2, v112\n"
"v_mfma_f32_16x16x32_bf16 v[68:71], a[24:27], v[40:43], v[68:71]\n"
"ds_read_b128 v[36:39], v114 offset:26624\n"
"buffer_load_dwordx4 v125, s[80:83], s95 offen offset:3072 lds\n"
"v_mfma_f32_16x16x32_bf16 v[72:75], a[28:31], v[40:43], v[72:75]\n"
"s_waitcnt lgkmcnt(5)\n"
"v_exp_f32 v76, v76\n"
"v_mfma_f32_16x16x32_bf16 v[68:71], a[32:35], v[44:47], v[68:71]\n"
"ds_read_b128 v[40:43], v116 offset:26624\n"
"v_add_f32 v76, 1.0, v76\n"
"v_mfma_f32_16x16x32_bf16 v[72:75], a[36:39], v[44:47], v[72:75]\n"
"v_rcp_f32 v76, v76\n"
"v_mfma_f32_16x16x32_bf16 v[68:71], a[40:43], v[48:51], v[68:71]\n"
"ds_read_b128 v[44:47], v114 offset:28672\n"
"v_mul_f32 v77, v77, v61\n"
"v_mfma_f32_16x16x32_bf16 v[72:75], a[44:47], v[48:51], v[72:75]\n"
"s_waitcnt lgkmcnt(5)\n"
"v_exp_f32 v77, v77\n"
"v_mfma_f32_16x16x32_bf16 v[68:71], a[48:51], v[52:55], v[68:71]\n"
"ds_read_b128 v[48:51], v116 offset:28672\n"
"v_add_f32 v77, 1.0, v77\n"
"v_mfma_f32_16x16x32_bf16 v[72:75], a[52:55], v[52:55], v[72:75]\n"
"v_rcp_f32 v77, v77\n"
"v_mfma_f32_16x16x32_bf16 v[68:71], a[56:59], v[56:59], v[68:71]\n"
"ds_read_b128 v[52:55], v114 offset:30720\n"
"v_mul_f32 v78, v78, v62\n"
"v_mfma_f32_16x16x32_bf16 v[72:75], a[60:63], v[56:59], v[72:75]\n"
"s_waitcnt lgkmcnt(5)\n"
"v_exp_f32 v78, v78\n"
"v_mfma_f32_16x16x32_bf16 v[68:71], a[64:67], v[28:31], v[68:71]\n"
"s_waitcnt vmcnt(18)\n"
"ds_read_b128 v[56:59], v116 offset:30720\n"
"v_add_f32 v78, 1.0, v78\n"
"v_mfma_f32_16x16x32_bf16 v[72:75], a[68:71], v[28:31], v[72:75]\n"
"s_barrier\n"
"v_rcp_f32 v78, v78\n"
"v_mfma_f32_16x16x32_bf16 v[68:71], a[72:75], v[32:35], v[68:71]\n"
"s_add_u32 m0, s44, 0\n"
"ds_read_b128 v[28:31], v114 offset:32768\n"
"v_mul_f32 v79, v63, v63\n"
"v_mfma_f32_16x16x32_bf16 v[72:75], a[76:79], v[32:35], v[72:75]\n"
"s_waitcnt lgkmcnt(5)\n"
"v_fmamk_f32 v79, v79, 0xbdd2d3e2, v112\n"
"v_mul_f32 v80, v64, v64\n"
"v_mfma_f32_16x16x32_bf16 v[68:71], a[80:83], v[36:39], v[68:71]\n"
"ds_read_b128 v[32:35], v116 offset:32768\n"
"buffer_load_dwordx4 v113, s[64:67], s40 offen offset:0 sc1 lds\n"
"v_mfma_f32_16x16x32_bf16 v[72:75], a[84:87], v[36:39], v[72:75]\n"
"v_mul_f32 v79, v79, v63\n"
"v_fmamk_f32 v80, v80, 0xbdd2d3e2, v112\n"
"v_mfma_f32_16x16x32_bf16 v[68:71], a[88:91], v[40:43], v[68:71]\n"
"ds_read_b128 v[36:39], v114 offset:34816\n"
"v_mul_f32 v80, v80, v64\n"
"v_mfma_f32_16x16x32_bf16 v[72:75], a[92:95], v[40:43], v[72:75]\n"
"s_waitcnt lgkmcnt(5)\n"
"v_exp_f32 v79, v79\n"
"v_mfma_f32_16x16x32_bf16 v[68:71], a[96:99], v[44:47], v[68:71]\n"
"ds_read_b128 v[40:43], v116 offset:34816\n"
"v_add_f32 v79, 1.0, v79\n"
"v_mfma_f32_16x16x32_bf16 v[72:75], a[100:103], v[44:47], v[72:75]\n"
"v_rcp_f32 v79, v79\n"
"v_mfma_f32_16x16x32_bf16 v[68:71], a[104:107], v[48:51], v[68:71]\n"
"ds_read_b128 v[44:47], v114 offset:36864\n"
"buffer_load_dwordx4 v113, s[64:67], s40 offen offset:1024 sc1 lds\n"
"v_mfma_f32_16x16x32_bf16 v[72:75], a[108:111], v[48:51], v[72:75]\n"
"s_waitcnt lgkmcnt(5)\n"
"v_exp_f32 v80, v80\n"
"v_mfma_f32_16x16x32_bf16 v[68:71], a[112:115], v[52:55], v[68:71]\n"
"ds_read_b128 v[48:51], v116 offset:36864\n"
"v_add_f32 v80, 1.0, v80\n"
"v_mfma_f32_16x16x32_bf16 v[72:75], a[116:119], v[52:55], v[72:75]\n"
"v_rcp_f32 v80, v80\n"
"v_mfma_f32_16x16x32_bf16 v[68:71], a[120:123], v[56:59], v[68:71]\n"
"s_getpc_b64 s[62:63]\n"
"ds_read_b128 v[52:55], v114 offset:38912\n"
"v_mul_f32 v81, v65, v65\n"
"v_mfma_f32_16x16x32_bf16 v[72:75], a[124:127], v[56:59], v[72:75]\n"
"s_waitcnt lgkmcnt(5)\n"
"global_load_dword v21, v119, s[62:63] offset:3072\n"
"v_fmamk_f32 v81, v81, 0xbdd2d3e2, v112\n"
"v_mfma_f32_16x16x32_bf16 v[68:71], a[128:131], v[28:31], v[68:71]\n"
"ds_read_b128 v[56:59], v116 offset:38912\n"
"buffer_load_dwordx4 v113, s[64:67], s40 offen offset:2048 sc1 lds\n"
"v_mfma_f32_16x16x32_bf16 v[72:75], a[132:135], v[28:31], v[72:75]\n"
"v_mul_f32 v81, v81, v65\n"
"v_mul_f32 v82, v66, v66\n"
"v_mfma_f32_16x16x32_bf16 v[68:71], a[136:139], v[32:35], v[68:71]\n"
"ds_read_b128 v[28:31], v114 offset:40960\n"
"v_fmamk_f32 v82, v82, 0xbdd2d3e2, v112\n"
"v_mfma_f32_16x16x32_bf16 v[72:75], a[140:143], v[32:35], v[72:75]\n"
"s_waitcnt lgkmcnt(5)\n"
"v_exp_f32 v81, v81\n"
"v_mfma_f32_16x16x32_bf16 v[68:71], a[144:147], v[36:39], v[68:71]\n"
"ds_read_b128 v[32:35], v116 offset:40960\n"
"v_add_f32 v81, 1.0, v81\n"
"v_mfma_f32_16x16x32_bf16 v[72:75], a[148:151], v[36:39], v[72:75]\n"
"v_rcp_f32 v81, v81\n"
"v_mfma_f32_16x16x32_bf16 v[68:71], a[152:155], v[40:43], v[68:71]\n"
"ds_read_b128 v[36:39], v114 offset:43008\n"
"buffer_load_dwordx4 v113, s[64:67], s40 offen offset:3072 sc1 lds\n"
"v_mfma_f32_16x16x32_bf16 v[72:75], a[156:159], v[40:43], v[72:75]\n"
"s_waitcnt lgkmcnt(5)\n"
"v_mul_f32 v82, v82, v66\n"
"v_mul_f32 v83, v67, v67\n"
"v_mfma_f32_16x16x32_bf16 v[68:71], a[160:163], v[44:47], v[68:71]\n"
"s_add_u32 s40, s40, 16384\n"
"ds_read_b128 v[40:43], v116 offset:43008\n"
"v_fmamk_f32 v83, v83, 0xbdd2d3e2, v112\n"
"v_mfma_f32_16x16x32_bf16 v[72:75], a[164:167], v[44:47], v[72:75]\n"
"v_exp_f32 v82, v82\n"
"v_mfma_f32_16x16x32_bf16 v[68:71], a[168:171], v[48:51], v[68:71]\n"
"ds_read_b128 v[44:47], v114 offset:45056\n"
"v_add_f32 v82, 1.0, v82\n"
"v_mfma_f32_16x16x32_bf16 v[72:75], a[172:175], v[48:51], v[72:75]\n"
"s_waitcnt lgkmcnt(5)\n"
"v_rcp_f32 v82, v82\n"
"v_mfma_f32_16x16x32_bf16 v[68:71], a[176:179], v[52:55], v[68:71]\n"
"ds_read_b128 v[48:51], v116 offset:45056\n"
"v_mul_f32 v83, v83, v67\n"
"v_mfma_f32_16x16x32_bf16 v[72:75], a[180:183], v[52:55], v[72:75]\n"
"v_exp_f32 v83, v83\n"
"v_mfma_f32_16x16x32_bf16 v[68:71], a[184:187], v[56:59], v[68:71]\n"
"ds_read_b128 v[52:55], v114 offset:47104\n"
"v_add_f32 v83, 1.0, v83\n"
"v_mfma_f32_16x16x32_bf16 v[72:75], a[188:191], v[56:59], v[72:75]\n"
"s_waitcnt lgkmcnt(5)\n"
"v_rcp_f32 v83, v83\n"
"v_mfma_f32_16x16x32_bf16 v[68:71], a[192:195], v[28:31], v[68:71]\n"
"s_waitcnt vmcnt(14)\n"
"ds_read_b128 v[56:59], v116 offset:47104\n"
"v_mul_f32 v76, v60, v76\n"
"v_mfma_f32_16x16x32_bf16 v[72:75], a[196:199], v[28:31], v[72:75]\n"
"s_barrier\n"
"v_mul_f32 v77, v61, v77\n"
"v_mul_f32 v78, v62, v78\n"
"v_mfma_f32_16x16x32_bf16 v[68:71], a[200:203], v[32:35], v[68:71]\n"
"s_add_u32 m0, s44, 16384\n"
"ds_read_b128 v[28:31], v114 offset:49152\n"
"ds_read_b64_tr_b16 v[128:129], v120 offset:16384\n"
"v_mfma_f32_16x16x32_bf16 v[72:75], a[204:207], v[32:35], v[72:75]\n"
"s_waitcnt lgkmcnt(6)\n"
"v_mul_f32 v79, v63, v79\n"
"v_mul_f32 v80, v64, v80\n"
"v_mfma_f32_16x16x32_bf16 v[68:71], a[208:211], v[36:39], v[68:71]\n"
"ds_read_b128 v[32:35], v116 offset:49152\n"
"buffer_load_dwordx4 v113, s[64:67], s40 offen offset:0 sc1 lds\n"
"v_mfma_f32_16x16x32_bf16 v[72:75], a[212:215], v[36:39], v[72:75]\n"
"ds_read_b64_tr_b16 v[130:131], v120 offset:17408\n"
"v_mul_f32 v81, v65, v81\n"
"v_mfma_f32_16x16x32_bf16 v[68:71], a[216:219], v[40:43], v[68:71]\n"
"ds_read_b128 v[36:39], v114 offset:51200\n"
"buffer_load_dwordx4 v113, s[64:67], s40 offen offset:1024 sc1 lds\n"
"v_mfma_f32_16x16x32_bf16 v[72:75], a[220:223], v[40:43], v[72:75]\n"
"s_waitcnt lgkmcnt(7)\n"
"ds_read_b64_tr_b16 v[132:133], v120 offset:16392\n"
"v_mul_f32 v82, v66, v82\n"
"v_mfma_f32_16x16x32_bf16 v[68:71], a[224:227], v[44:47], v[68:71]\n"
"ds_read_b128 v[40:43], v116 offset:51200\n"
"buffer_load_dwordx4 v113, s[64:67], s40 offen offset:2048 sc1 lds\n"
"v_mfma_f32_16x16x32_bf16 v[72:75], a[228:231], v[44:47], v[72:75]\n"
"ds_read_b64_tr_b16 v[134:135], v120 offset:17416\n"
"v_mul_f32 v83, v67, v83\n"
"v_mfma_f32_16x16x32_bf16 v[68:71], a[232:235], v[48:51], v[68:71]\n"
"ds_read_b128 v[44:47], v114 offset:53248\n"
"buffer_load_dwordx4 v113, s[64:67], s40 offen offset:3072 sc1 lds\n"
"v_mfma_f32_16x16x32_bf16 v[72:75], a[236:239], v[48:51], v[72:75]\n"
"s_waitcnt lgkmcnt(9)\n"
"v_cvt_pk_bf16_f32 v84, v76, v77\n"
"v_cvt_pk_bf16_f32 v85, v78, v79\n"
"v_mfma_f32_16x16x32_bf16 v[68:71], a[240:243], v[52:55], v[68:71]\n"
"s_add_u32 s40, s40, 278528\n"
"ds_read_b128 v[48:51], v116 offset:53248\n"
"ds_read_b64_tr_b16 v[136:137], v120 offset:24576\n"
"v_mfma_f32_16x16x32_bf16 v[72:75], a[244:247], v[52:55], v[72:75]\n"
"s_add_u32 s95, s43, 262144\n"
"v_cvt_pk_bf16_f32 v86, v80, v81\n"
"v_cvt_pk_bf16_f32 v87, v82, v83\n"
"v_mfma_f32_16x16x32_bf16 v[68:71], a[248:251], v[56:59], v[68:71]\n"
"s_add_u32 m0, s44, 114688\n"
"ds_read_b128 v[52:55], v114 offset:55296\n"
"ds_read_b64_tr_b16 v[138:139], v120 offset:25600\n"
"v_mfma_f32_16x16x32_bf16 v[72:75], a[252:255], v[56:59], v[72:75]\n"
"s_waitcnt lgkmcnt(10)\n"
"v_mfma_f32_16x16x32_bf16 v[60:63], a[0:3], v[28:31], v[88:91]\n"
"ds_read_b128 v[56:59], v116 offset:55296\n"
"buffer_load_dwordx4 v122, s[80:83], s95 offen offset:0 lds\n"
"v_mfma_f32_16x16x32_bf16 v[64:67], a[4:7], v[28:31], v[92:95]\n"
"buffer_store_dwordx4 v[84:87], v113, s[68:71], s41 offen sc1\n"
"ds_read_b64_tr_b16 v[140:141], v120 offset:24584\n"
"v_mfma_f32_16x16x32_bf16 v[60:63], a[8:11], v[32:35], v[60:63]\n"
"s_add_u32 s41, s41, 1032192\n"
"ds_read_b128 v[28:31], v114 offset:57344\n"
"buffer_load_dwordx4 v123, s[80:83], s95 offen offset:1024 lds\n"
"v_mfma_f32_16x16x32_bf16 v[64:67], a[12:15], v[32:35], v[64:67]\n"
"s_waitcnt lgkmcnt(9)\n"
"ds_read_b64_tr_b16 v[142:143], v120 offset:25608\n"
"v_mul_f32 v76, v68, v68\n"
"v_mul_f32 v77, v69, v69\n"
"v_mfma_f32_16x16x32_bf16 v[60:63], a[16:19], v[36:39], v[60:63]\n"
"ds_read_b128 v[32:35], v116 offset:57344\n"
"buffer_load_dwordx4 v124, s[80:83], s95 offen offset:2048 lds\n"
"v_fmamk_f32 v76, v76, 0xbdd2d3e2, v112\n"
"v_mfma_f32_16x16x32_bf16 v[64:67], a[20:23], v[36:39], v[64:67]\n"
"v_mul_f32 v76, v76, v68\n"
"v_fmamk_f32 v77, v77, 0xbdd2d3e2, v112\n"
"v_mul_f32 v78, v70, v70\n"
"v_mfma_f32_16x16x32_bf16 v[60:63], a[24:27], v[40:43], v[60:63]\n"
"ds_read_b128 v[36:39], v114 offset:59392\n"
"buffer_load_dwordx4 v125, s[80:83], s95 offen offset:3072 lds\n"
"v_mul_f32 v77, v77, v69\n"
"v_mfma_f32_16x16x32_bf16 v[64:67], a[28:31], v[40:43], v[64:67]\n"
"s_waitcnt lgkmcnt(9)\n"
"v_exp_f32 v76, v76\n"
"v_mfma_f32_16x16x32_bf16 v[60:63], a[32:35], v[44:47], v[60:63]\n"
"ds_read_b128 v[40:43], v116 offset:59392\n"
"v_add_f32 v76, 1.0, v76\n"
"v_mfma_f32_16x16x32_bf16 v[64:67], a[36:39], v[44:47], v[64:67]\n"
"v_rcp_f32 v76, v76\n"
"v_mfma_f32_16x16x32_bf16 v[60:63], a[40:43], v[48:51], v[60:63]\n"
"ds_read_b128 v[44:47], v114 offset:61440\n"
"v_fmamk_f32 v78, v78, 0xbdd2d3e2, v112\n"
"v_mfma_f32_16x16x32_bf16 v[64:67], a[44:47], v[48:51], v[64:67]\n"
"s_waitcnt lgkmcnt(7)\n"
"v_exp_f32 v77, v77\n"
"v_mfma_f32_16x16x32_bf16 v[60:63], a[48:51], v[52:55], v[60:63]\n"
"ds_read_b128 v[48:51], v116 offset:61440\n"
"v_add_f32 v77, 1.0, v77\n"
"v_mfma_f32_16x16x32_bf16 v[64:67], a[52:55], v[52:55], v[64:67]\n"
"v_rcp_f32 v77, v77\n"
"v_mfma_f32_16x16x32_bf16 v[60:63], a[56:59], v[56:59], v[60:63]\n"
"ds_read_b128 v[52:55], v114 offset:63488\n"
"v_mul_f32 v78, v78, v70\n"
"v_mfma_f32_16x16x32_bf16 v[64:67], a[60:63], v[56:59], v[64:67]\n"
"s_waitcnt lgkmcnt(5)\n"
"v_exp_f32 v78, v78\n"
"v_mfma_f32_16x16x32_bf16 v[60:63], a[64:67], v[28:31], v[60:63]\n"
"s_waitcnt vmcnt(19)\n"
"ds_read_b128 v[56:59], v116 offset:63488\n"
"v_add_f32 v78, 1.0, v78\n"
"v_mfma_f32_16x16x32_bf16 v[64:67], a[68:71], v[28:31], v[64:67]\n"
"s_barrier\n"
"v_rcp_f32 v78, v78\n"
"v_mfma_f32_16x16x32_bf16 v[60:63], a[72:75], v[32:35], v[60:63]\n"
"s_add_u32 m0, s44, 32768\n"
"ds_read_b128 v[28:31], v115 offset:0\n"
"v_mul_f32 v79, v71, v71\n"
"v_mfma_f32_16x16x32_bf16 v[64:67], a[76:79], v[32:35], v[64:67]\n"
"s_waitcnt lgkmcnt(5)\n"
"v_fmamk_f32 v79, v79, 0xbdd2d3e2, v112\n"
"v_mul_f32 v80, v72, v72\n"
"v_mfma_f32_16x16x32_bf16 v[60:63], a[80:83], v[36:39], v[60:63]\n"
"ds_read_b128 v[32:35], v117 offset:0\n"
"buffer_load_dwordx4 v113, s[64:67], s40 offen offset:0 sc1 lds\n"
"v_mfma_f32_16x16x32_bf16 v[64:67], a[84:87], v[36:39], v[64:67]\n"
"v_mul_f32 v79, v79, v71\n"
"v_fmamk_f32 v80, v80, 0xbdd2d3e2, v112\n"
"v_mfma_f32_16x16x32_bf16 v[60:63], a[88:91], v[40:43], v[60:63]\n"
"ds_read_b128 v[36:39], v115 offset:2048\n"
"buffer_load_dwordx4 v113, s[64:67], s40 offen offset:1024 sc1 lds\n"
"v_mfma_f32_16x16x32_bf16 v[64:67], a[92:95], v[40:43], v[64:67]\n"
"s_waitcnt lgkmcnt(5)\n"
"v_exp_f32 v79, v79\n"
"v_mfma_f32_16x16x32_bf16 v[60:63], a[96:99], v[44:47], v[60:63]\n"
"ds_read_b128 v[40:43], v117 offset:2048\n"
"buffer_load_dwordx4 v113, s[64:67], s40 offen offset:2048 sc1 lds\n"
"v_mfma_f32_16x16x32_bf16 v[64:67], a[100:103], v[44:47], v[64:67]\n"
"v_add_f32 v79, 1.0, v79\n"
"v_mul_f32 v80, v80, v72\n"
"v_mfma_f32_16x16x32_bf16 v[60:63], a[104:107], v[48:51], v[60:63]\n"
"ds_read_b128 v[44:47], v115 offset:4096\n"
"buffer_load_dwordx4 v113, s[64:67], s40 offen offset:3072 sc1 lds\n"
"v_mfma_f32_16x16x32_bf16 v[64:67], a[108:111], v[48:51], v[64:67]\n"
"s_waitcnt lgkmcnt(5)\n"
"v_rcp_f32 v79, v79\n"
"v_mfma_f32_16x16x32_bf16 v[60:63], a[112:115], v[52:55], v[60:63]\n"
"s_add_u32 s40, s40, 16384\n"
"ds_read_b128 v[48:51], v117 offset:4096\n"
"v_mul_f32 v81, v73, v73\n"
"v_mfma_f32_16x16x32_bf16 v[64:67], a[116:119], v[52:55], v[64:67]\n"
"s_add_u32 s95, s43, 393216\n"
"v_exp_f32 v80, v80\n"
"v_mfma_f32_16x16x32_bf16 v[60:63], a[120:123], v[56:59], v[60:63]\n"
"s_add_u32 m0, s44, 131072\n"
"ds_read_b128 v[52:55], v115 offset:6144\n"
"v_add_f32 v80, 1.0, v80\n"
"v_mfma_f32_16x16x32_bf16 v[64:67], a[124:127], v[56:59], v[64:67]\n"
"s_waitcnt lgkmcnt(5)\n"
"v_rcp_f32 v80, v80\n"
"v_mfma_f32_16x16x32_bf16 v[60:63], a[128:131], v[28:31], v[60:63]\n"
"ds_read_b128 v[56:59], v117 offset:6144\n"
"buffer_load_dwordx4 v122, s[80:83], s95 offen offset:0 lds\n"
"v_mfma_f32_16x16x32_bf16 v[64:67], a[132:135], v[28:31], v[64:67]\n"
"v_fmamk_f32 v81, v81, 0xbdd2d3e2, v112\n"
"v_mul_f32 v82, v74, v74\n"
"v_mfma_f32_16x16x32_bf16 v[60:63], a[136:139], v[32:35], v[60:63]\n"
"ds_read_b128 v[28:31], v115 offset:8192\n"
"buffer_load_dwordx4 v123, s[80:83], s95 offen offset:1024 lds\n"
"v_mfma_f32_16x16x32_bf16 v[64:67], a[140:143], v[32:35], v[64:67]\n"
"s_waitcnt lgkmcnt(5)\n"
"v_mul_f32 v81, v81, v73\n"
"v_fmamk_f32 v82, v82, 0xbdd2d3e2, v112\n"
"v_mfma_f32_16x16x32_bf16 v[60:63], a[144:147], v[36:39], v[60:63]\n"
"ds_read_b128 v[32:35], v117 offset:8192\n"
"buffer_load_dwordx4 v124, s[80:83], s95 offen offset:2048 lds\n"
"v_mfma_f32_16x16x32_bf16 v[64:67], a[148:151], v[36:39], v[64:67]\n"
"v_exp_f32 v81, v81\n"
"v_mfma_f32_16x16x32_bf16 v[60:63], a[152:155], v[40:43], v[60:63]\n"
"ds_read_b128 v[36:39], v115 offset:10240\n"
"buffer_load_dwordx4 v125, s[80:83], s95 offen offset:3072 lds\n"
"v_mfma_f32_16x16x32_bf16 v[64:67], a[156:159], v[40:43], v[64:67]\n"
"s_waitcnt lgkmcnt(5)\n"
"v_add_f32 v81, 1.0, v81\n"
"v_mul_f32 v82, v82, v74\n"
"v_mfma_f32_16x16x32_bf16 v[60:63], a[160:163], v[44:47], v[60:63]\n"
"ds_read_b128 v[40:43], v117 offset:10240\n"
"v_mul_f32 v83, v75, v75\n"
"v_mfma_f32_16x16x32_bf16 v[64:67], a[164:167], v[44:47], v[64:67]\n"
"v_rcp_f32 v81, v81\n"
"v_mfma_f32_16x16x32_bf16 v[60:63], a[168:171], v[48:51], v[60:63]\n"
"ds_read_b128 v[44:47], v115 offset:12288\n"
"v_fmamk_f32 v83, v83, 0xbdd2d3e2, v112\n"
"v_mfma_f32_16x16x32_bf16 v[64:67], a[172:175], v[48:51], v[64:67]\n"
"s_waitcnt lgkmcnt(5)\n"
"v_exp_f32 v82, v82\n"
"v_mfma_f32_16x16x32_bf16 v[60:63], a[176:179], v[52:55], v[60:63]\n"
"ds_read_b128 v[48:51], v117 offset:12288\n"
"v_add_f32 v82, 1.0, v82\n"
"v_mfma_f32_16x16x32_bf16 v[64:67], a[180:183], v[52:55], v[64:67]\n"
"v_rcp_f32 v82, v82\n"
"v_mfma_f32_16x16x32_bf16 v[60:63], a[184:187], v[56:59], v[60:63]\n"
"ds_read_b128 v[52:55], v115 offset:14336\n"
"v_mul_f32 v83, v83, v75\n"
"v_mfma_f32_16x16x32_bf16 v[64:67], a[188:191], v[56:59], v[64:67]\n"
"s_waitcnt lgkmcnt(5)\n"
"v_exp_f32 v83, v83\n"
"v_mfma_f32_16x16x32_bf16 v[60:63], a[192:195], v[28:31], v[60:63]\n"
"s_waitcnt vmcnt(17)\n"
"ds_read_b128 v[56:59], v117 offset:14336\n"
"v_add_f32 v83, 1.0, v83\n"
"v_mfma_f32_16x16x32_bf16 v[64:67], a[196:199], v[28:31], v[64:67]\n"
"s_barrier\n"
"v_rcp_f32 v83, v83\n"
"v_mfma_f32_16x16x32_bf16 v[60:63], a[200:203], v[32:35], v[60:63]\n"
"s_add_u32 m0, s44, 49152\n"
"ds_read_b128 v[28:31], v114 offset:0\n"
"ds_read_b64_tr_b16 v[144:145], v120 offset:32768\n"
"v_mfma_f32_16x16x32_bf16 v[64:67], a[204:207], v[32:35], v[64:67]\n"
"s_waitcnt lgkmcnt(6)\n"
"v_mul_f32 v76, v68, v76\n"
"v_mul_f32 v77, v69, v77\n"
"v_mfma_f32_16x16x32_bf16 v[60:63], a[208:211], v[36:39], v[60:63]\n"
"ds_read_b128 v[32:35], v116 offset:0\n"
"buffer_load_dwordx4 v113, s[64:67], s40 offen offset:0 sc1 lds\n"
"v_mfma_f32_16x16x32_bf16 v[64:67], a[212:215], v[36:39], v[64:67]\n"
"ds_read_b64_tr_b16 v[146:147], v120 offset:33792\n"
"v_mul_f32 v78, v70, v78\n"
"v_mfma_f32_16x16x32_bf16 v[60:63], a[216:219], v[40:43], v[60:63]\n"
"ds_read_b128 v[36:39], v114 offset:2048\n"
"v_mul_f32 v79, v71, v79\n"
"v_mfma_f32_16x16x32_bf16 v[64:67], a[220:223], v[40:43], v[64:67]\n"
"s_waitcnt lgkmcnt(7)\n"
"v_mul_f32 v80, v72, v80\n"
"v_mul_f32 v81, v73, v81\n"
"v_mfma_f32_16x16x32_bf16 v[60:63], a[224:227], v[44:47], v[60:63]\n"
"ds_read_b128 v[40:43], v116 offset:2048\n"
"ds_read_b64_tr_b16 v[148:149], v120 offset:32776\n"
"v_mfma_f32_16x16x32_bf16 v[64:67], a[228:231], v[44:47], v[64:67]\n"
"v_mul_f32 v82, v74, v82\n"
"v_mul_f32 v83, v75, v83\n"
"v_mfma_f32_16x16x32_bf16 v[60:63], a[232:235], v[48:51], v[60:63]\n"
"ds_read_b128 v[44:47], v114 offset:4096\n"
"buffer_load_dwordx4 v113, s[64:67], s40 offen offset:1024 sc1 lds\n"
"v_mfma_f32_16x16x32_bf16 v[64:67], a[236:239], v[48:51], v[64:67]\n"
"s_waitcnt lgkmcnt(8)\n"
"ds_read_b64_tr_b16 v[150:151], v120 offset:33800\n"
"v_cvt_pk_bf16_f32 v84, v76, v77\n"
"v_mfma_f32_16x16x32_bf16 v[60:63], a[240:243], v[52:55], v[60:63]\n"
"ds_read_b128 v[48:51], v116 offset:4096\n"
"v_cvt_pk_bf16_f32 v85, v78, v79\n"
"v_mfma_f32_16x16x32_bf16 v[64:67], a[244:247], v[52:55], v[64:67]\n"
"v_cvt_pk_bf16_f32 v86, v80, v81\n"
"v_cvt_pk_bf16_f32 v87, v82, v83\n"
"v_mfma_f32_16x16x32_bf16 v[60:63], a[248:251], v[56:59], v[60:63]\n"
"ds_read_b128 v[52:55], v114 offset:6144\n"
"ds_read_b64_tr_b16 v[152:153], v120 offset:40960\n"
"v_mfma_f32_16x16x32_bf16 v[64:67], a[252:255], v[56:59], v[64:67]\n"
"s_waitcnt lgkmcnt(9)\n"
"v_mfma_f32_16x16x32_bf16 v[68:71], a[0:3], v[28:31], v[88:91]\n"
"ds_read_b128 v[56:59], v116 offset:6144\n"
"buffer_load_dwordx4 v113, s[64:67], s40 offen offset:2048 sc1 lds\n"
"v_mfma_f32_16x16x32_bf16 v[72:75], a[4:7], v[28:31], v[92:95]\n"
"buffer_store_dwordx4 v[84:87], v113, s[68:71], s41 offen sc1\n"
"ds_read_b64_tr_b16 v[154:155], v120 offset:41984\n"
"v_mfma_f32_16x16x32_bf16 v[68:71], a[8:11], v[32:35], v[68:71]\n"
"s_add_u32 s41, s41, 1032192\n"
"ds_read_b128 v[28:31], v114 offset:8192\n"
"v_mfma_f32_16x16x32_bf16 v[72:75], a[12:15], v[32:35], v[72:75]\n"
"s_waitcnt lgkmcnt(9)\n"
"v_mul_f32 v76, v60, v60\n"
"v_mul_f32 v77, v61, v61\n"
"v_mul_f32 v78, v62, v62\n"
"v_mfma_f32_16x16x32_bf16 v[68:71], a[16:19], v[36:39], v[68:71]\n"
"ds_read_b128 v[32:35], v116 offset:8192\n"
"ds_read_b64_tr_b16 v[156:157], v120 offset:40968\n"
"v_fmamk_f32 v76, v76, 0xbdd2d3e2, v112\n"
"v_mfma_f32_16x16x32_bf16 v[72:75], a[20:23], v[36:39], v[72:75]\n"
"v_mul_f32 v76, v76, v60\n"
"v_fmamk_f32 v77, v77, 0xbdd2d3e2, v112\n"
"v_fmamk_f32 v78, v78, 0xbdd2d3e2, v112\n"
"v_mfma_f32_16x16x32_bf16 v[68:71], a[24:27], v[40:43], v[68:71]\n"
"ds_read_b128 v[36:39], v114 offset:10240\n"
"buffer_load_dwordx4 v113, s[64:67], s40 offen offset:3072 sc1 lds\n"
"v_mul_f32 v77, v77, v61\n"
"v_mfma_f32_16x16x32_bf16 v[72:75], a[28:31], v[40:43], v[72:75]\n"
"s_waitcnt lgkmcnt(8)\n"
"ds_read_b64_tr_b16 v[158:159], v120 offset:41992\n"
"v_mul_f32 v78, v78, v62\n"
"v_mfma_f32_16x16x32_bf16 v[68:71], a[32:35], v[44:47], v[68:71]\n"
"s_add_u32 s40, s40, 278528\n"
"ds_read_b128 v[40:43], v116 offset:10240\n"
"v_mul_f32 v79, v63, v63\n"
"v_mfma_f32_16x16x32_bf16 v[72:75], a[36:39], v[44:47], v[72:75]\n"
"v_exp_f32 v76, v76\n"
"v_mfma_f32_16x16x32_bf16 v[68:71], a[40:43], v[48:51], v[68:71]\n"
"ds_read_b128 v[44:47], v114 offset:12288\n"
"v_add_f32 v76, 1.0, v76\n"
"v_mfma_f32_16x16x32_bf16 v[72:75], a[44:47], v[48:51], v[72:75]\n"
"s_waitcnt lgkmcnt(8)\n"
"v_rcp_f32 v76, v76\n"
"v_mfma_f32_16x16x32_bf16 v[68:71], a[48:51], v[52:55], v[68:71]\n"
"ds_read_b128 v[48:51], v116 offset:12288\n"
"v_fmamk_f32 v79, v79, 0xbdd2d3e2, v112\n"
"v_mfma_f32_16x16x32_bf16 v[72:75], a[52:55], v[52:55], v[72:75]\n"
"v_exp_f32 v77, v77\n"
"v_mfma_f32_16x16x32_bf16 v[68:71], a[56:59], v[56:59], v[68:71]\n"
"ds_read_b128 v[52:55], v114 offset:14336\n"
"v_add_f32 v77, 1.0, v77\n"
"v_mfma_f32_16x16x32_bf16 v[72:75], a[60:63], v[56:59], v[72:75]\n"
"s_waitcnt lgkmcnt(7)\n"
"v_rcp_f32 v77, v77\n"
"v_mfma_f32_16x16x32_bf16 v[68:71], a[64:67], v[28:31], v[68:71]\n"
"s_waitcnt vmcnt(18)\n"
"ds_read_b128 v[56:59], v116 offset:14336\n"
"v_mul_f32 v79, v79, v63\n"
"v_mfma_f32_16x16x32_bf16 v[72:75], a[68:71], v[28:31], v[72:75]\n"
"s_barrier\n"
"v_exp_f32 v78, v78\n"
"v_mfma_f32_16x16x32_bf16 v[68:71], a[72:75], v[32:35], v[68:71]\n"
"s_add_u32 m0, s44, 65536\n"
"ds_read_b128 v[28:31], v114 offset:16384\n"
"v_add_f32 v78, 1.0, v78\n"
"v_mfma_f32_16x16x32_bf16 v[72:75], a[76:79], v[32:35], v[72:75]\n"
"s_waitcnt lgkmcnt(5)\n"
"v_rcp_f32 v78, v78\n"
"v_mfma_f32_16x16x32_bf16 v[68:71], a[80:83], v[36:39], v[68:71]\n"
"ds_read_b128 v[32:35], v116 offset:16384\n"
"buffer_load_dwordx4 v113, s[64:67], s40 offen offset:0 sc1 lds\n"
"v_mfma_f32_16x16x32_bf16 v[72:75], a[84:87], v[36:39], v[72:75]\n"
"v_exp_f32 v79, v79\n"
"v_mfma_f32_16x16x32_bf16 v[68:71], a[88:91], v[40:43], v[68:71]\n"
"ds_read_b128 v[36:39], v114 offset:18432\n"
"buffer_load_dwordx4 v113, s[64:67], s40 offen offset:1024 sc1 lds\n"
"v_mfma_f32_16x16x32_bf16 v[72:75], a[92:95], v[40:43], v[72:75]\n"
"s_waitcnt lgkmcnt(5)\n"
"v_add_f32 v79, 1.0, v79\n"
"v_mul_f32 v80, v64, v64\n"
"v_mfma_f32_16x16x32_bf16 v[68:71], a[96:99], v[44:47], v[68:71]\n"
"ds_read_b128 v[40:43], v116 offset:18432\n"
"buffer_load_dwordx4 v113, s[64:67], s40 offen offset:2048 sc1 lds\n"
"v_mfma_f32_16x16x32_bf16 v[72:75], a[100:103], v[44:47], v[72:75]\n"
"v_rcp_f32 v79, v79\n"
"v_mfma_f32_16x16x32_bf16 v[68:71], a[104:107], v[48:51], v[68:71]\n"
"ds_read_b128 v[44:47], v114 offset:20480\n"
"buffer_load_dwordx4 v113, s[64:67], s40 offen offset:3072 sc1 lds\n"
"v_mfma_f32_16x16x32_bf16 v[72:75], a[108:111], v[48:51], v[72:75]\n"
"s_waitcnt lgkmcnt(5)\n"
"v_fmamk_f32 v80, v80, 0xbdd2d3e2, v112\n"
"v_mul_f32 v81, v65, v65\n"
"v_mfma_f32_16x16x32_bf16 v[68:71], a[112:115], v[52:55], v[68:71]\n"
"s_add_u32 s40, s40, 16384\n"
"ds_read_b128 v[48:51], v116 offset:20480\n"
"v_mul_f32 v80, v80, v64\n"
"v_mfma_f32_16x16x32_bf16 v[72:75], a[116:119], v[52:55], v[72:75]\n"
"s_add_u32 s95, s43, 524288\n"
"v_exp_f32 v80, v80\n"
"v_mfma_f32_16x16x32_bf16 v[68:71], a[120:123], v[56:59], v[68:71]\n"
"s_add_u32 m0, s44, 147456\n"
"ds_read_b128 v[52:55], v114 offset:22528\n"
"v_add_f32 v80, 1.0, v80\n"
"v_mfma_f32_16x16x32_bf16 v[72:75], a[124:127], v[56:59], v[72:75]\n"
"s_waitcnt lgkmcnt(5)\n"
"v_rcp_f32 v80, v80\n"
"v_mfma_f32_16x16x32_bf16 v[68:71], a[128:131], v[28:31], v[68:71]\n"
"ds_read_b128 v[56:59], v116 offset:22528\n"
"buffer_load_dwordx4 v122, s[80:83], s95 offen offset:0 lds\n"
"v_mfma_f32_16x16x32_bf16 v[72:75], a[132:135], v[28:31], v[72:75]\n"
"v_fmamk_f32 v81, v81, 0xbdd2d3e2, v112\n"
"v_mul_f32 v82, v66, v66\n"
"v_mfma_f32_16x16x32_bf16 v[68:71], a[136:139], v[32:35], v[68:71]\n"
"ds_read_b128 v[28:31], v114 offset:24576\n"
"buffer_load_dwordx4 v123, s[80:83], s95 offen offset:1024 lds\n"
"v_mfma_f32_16x16x32_bf16 v[72:75], a[140:143], v[32:35], v[72:75]\n"
"s_waitcnt lgkmcnt(5)\n"
"v_mul_f32 v81, v81, v65\n"
"v_fmamk_f32 v82, v82, 0xbdd2d3e2, v112\n"
"v_mfma_f32_16x16x32_bf16 v[68:71], a[144:147], v[36:39], v[68:71]\n"
"ds_read_b128 v[32:35], v116 offset:24576\n"
"buffer_load_dwordx4 v124, s[80:83], s95 offen offset:2048 lds\n"
"v_mfma_f32_16x16x32_bf16 v[72:75], a[148:151], v[36:39], v[72:75]\n"
"v_exp_f32 v81, v81\n"
"v_mfma_f32_16x16x32_bf16 v[68:71], a[152:155], v[40:43], v[68:71]\n"
"ds_read_b128 v[36:39], v114 offset:26624\n"
"buffer_load_dwordx4 v125, s[80:83], s95 offen offset:3072 lds\n"
"v_mfma_f32_16x16x32_bf16 v[72:75], a[156:159], v[40:43], v[72:75]\n"
"s_waitcnt lgkmcnt(5)\n"
"v_add_f32 v81, 1.0, v81\n"
"v_mul_f32 v82, v82, v66\n"
"v_mfma_f32_16x16x32_bf16 v[68:71], a[160:163], v[44:47], v[68:71]\n"
"ds_read_b128 v[40:43], v116 offset:26624\n"
"v_mul_f32 v83, v67, v67\n"
"v_mfma_f32_16x16x32_bf16 v[72:75], a[164:167], v[44:47], v[72:75]\n"
"v_rcp_f32 v81, v81\n"
"v_mfma_f32_16x16x32_bf16 v[68:71], a[168:171], v[48:51], v[68:71]\n"
"ds_read_b128 v[44:47], v114 offset:28672\n"
"v_fmamk_f32 v83, v83, 0xbdd2d3e2, v112\n"
"v_mfma_f32_16x16x32_bf16 v[72:75], a[172:175], v[48:51], v[72:75]\n"
"s_waitcnt lgkmcnt(5)\n"
"v_exp_f32 v82, v82\n"
"v_mfma_f32_16x16x32_bf16 v[68:71], a[176:179], v[52:55], v[68:71]\n"
"ds_read_b128 v[48:51], v116 offset:28672\n"
"v_add_f32 v82, 1.0, v82\n"
"v_mfma_f32_16x16x32_bf16 v[72:75], a[180:183], v[52:55], v[72:75]\n"
"v_rcp_f32 v82, v82\n"
"v_mfma_f32_16x16x32_bf16 v[68:71], a[184:187], v[56:59], v[68:71]\n"
"ds_read_b128 v[52:55], v114 offset:30720\n"
"v_mul_f32 v83, v83, v67\n"
"v_mfma_f32_16x16x32_bf16 v[72:75], a[188:191], v[56:59], v[72:75]\n"
"s_waitcnt lgkmcnt(5)\n"
"v_exp_f32 v83, v83\n"
"v_mfma_f32_16x16x32_bf16 v[68:71], a[192:195], v[28:31], v[68:71]\n"
"s_waitcnt vmcnt(17)\n"
"ds_read_b128 v[56:59], v116 offset:30720\n"
"v_add_f32 v83, 1.0, v83\n"
"v_mfma_f32_16x16x32_bf16 v[72:75], a[196:199], v[28:31], v[72:75]\n"
"s_barrier\n"
"v_rcp_f32 v83, v83\n"
"v_mfma_f32_16x16x32_bf16 v[68:71], a[200:203], v[32:35], v[68:71]\n"
"s_add_u32 m0, s44, 0\n"
"ds_read_b128 v[28:31], v114 offset:32768\n"
"ds_read_b64_tr_b16 v[160:161], v120 offset:49152\n"
"v_mfma_f32_16x16x32_bf16 v[72:75], a[204:207], v[32:35], v[72:75]\n"
"s_waitcnt lgkmcnt(6)\n"
"v_mul_f32 v76, v60, v76\n"
"v_mul_f32 v77, v61, v77\n"
"v_mfma_f32_16x16x32_bf16 v[68:71], a[208:211], v[36:39], v[68:71]\n"
"ds_read_b128 v[32:35], v116 offset:32768\n"
"buffer_load_dwordx4 v113, s[64:67], s40 offen offset:0 sc1 lds\n"
"v_mfma_f32_16x16x32_bf16 v[72:75], a[212:215], v[36:39], v[72:75]\n"
"ds_read_b64_tr_b16 v[162:163], v120 offset:50176\n"
"v_mul_f32 v78, v62, v78\n"
"v_mfma_f32_16x16x32_bf16 v[68:71], a[216:219], v[40:43], v[68:71]\n"
"ds_read_b128 v[36:39], v114 offset:34816\n"
"v_mul_f32 v79, v63, v79\n"
"v_mfma_f32_16x16x32_bf16 v[72:75], a[220:223], v[40:43], v[72:75]\n"
"s_waitcnt lgkmcnt(7)\n"
"v_mul_f32 v80, v64, v80\n"
"v_mul_f32 v81, v65, v81\n"
"v_mfma_f32_16x16x32_bf16 v[68:71], a[224:227], v[44:47], v[68:71]\n"
"ds_read_b128 v[40:43], v116 offset:34816\n"
"ds_read_b64_tr_b16 v[164:165], v120 offset:49160\n"
"v_mfma_f32_16x16x32_bf16 v[72:75], a[228:231], v[44:47], v[72:75]\n"
"v_mul_f32 v82, v66, v82\n"
"v_mul_f32 v83, v67, v83\n"
"v_mfma_f32_16x16x32_bf16 v[68:71], a[232:235], v[48:51], v[68:71]\n"
"ds_read_b128 v[44:47], v114 offset:36864\n"
"buffer_load_dwordx4 v113, s[64:67], s40 offen offset:1024 sc1 lds\n"
"v_mfma_f32_16x16x32_bf16 v[72:75], a[236:239], v[48:51], v[72:75]\n"
"s_waitcnt lgkmcnt(8)\n"
"ds_read_b64_tr_b16 v[166:167], v120 offset:50184\n"
"v_cvt_pk_bf16_f32 v84, v76, v77\n"
"v_mfma_f32_16x16x32_bf16 v[68:71], a[240:243], v[52:55], v[68:71]\n"
"ds_read_b128 v[48:51], v116 offset:36864\n"
"v_cvt_pk_bf16_f32 v85, v78, v79\n"
"v_mfma_f32_16x16x32_bf16 v[72:75], a[244:247], v[52:55], v[72:75]\n"
"v_cvt_pk_bf16_f32 v86, v80, v81\n"
"v_cvt_pk_bf16_f32 v87, v82, v83\n"
"v_mfma_f32_16x16x32_bf16 v[68:71], a[248:251], v[56:59], v[68:71]\n"
"ds_read_b128 v[52:55], v114 offset:38912\n"
"ds_read_b64_tr_b16 v[168:169], v120 offset:57344\n"
"v_mfma_f32_16x16x32_bf16 v[72:75], a[252:255], v[56:59], v[72:75]\n"
"s_waitcnt lgkmcnt(9)\n"
"v_mfma_f32_16x16x32_bf16 v[60:63], a[0:3], v[28:31], v[88:91]\n"
"ds_read_b128 v[56:59], v116 offset:38912\n"
"buffer_load_dwordx4 v113, s[64:67], s40 offen offset:2048 sc1 lds\n"
"v_mfma_f32_16x16x32_bf16 v[64:67], a[4:7], v[28:31], v[92:95]\n"
"buffer_store_dwordx4 v[84:87], v113, s[68:71], s41 offen sc1\n"
"ds_read_b64_tr_b16 v[170:171], v120 offset:58368\n"
"v_mfma_f32_16x16x32_bf16 v[60:63], a[8:11], v[32:35], v[60:63]\n"
"s_add_u32 s41, s41, 1032192\n"
"ds_read_b128 v[28:31], v114 offset:40960\n"
"v_mfma_f32_16x16x32_bf16 v[64:67], a[12:15], v[32:35], v[64:67]\n"
"s_waitcnt lgkmcnt(9)\n"
"v_mul_f32 v76, v68, v68\n"
"v_mul_f32 v77, v69, v69\n"
"v_mul_f32 v78, v70, v70\n"
"v_mfma_f32_16x16x32_bf16 v[60:63], a[16:19], v[36:39], v[60:63]\n"
"ds_read_b128 v[32:35], v116 offset:40960\n"
"ds_read_b64_tr_b16 v[172:173], v120 offset:57352\n"
"v_fmamk_f32 v76, v76, 0xbdd2d3e2, v112\n"
"v_mfma_f32_16x16x32_bf16 v[64:67], a[20:23], v[36:39], v[64:67]\n"
"v_mul_f32 v76, v76, v68\n"
"v_fmamk_f32 v77, v77, 0xbdd2d3e2, v112\n"
"v_fmamk_f32 v78, v78, 0xbdd2d3e2, v112\n"
"v_mfma_f32_16x16x32_bf16 v[60:63], a[24:27], v[40:43], v[60:63]\n"
"ds_read_b128 v[36:39], v114 offset:43008\n"
"buffer_load_dwordx4 v113, s[64:67], s40 offen offset:3072 sc1 lds\n"
"v_mul_f32 v77, v77, v69\n"
"v_mfma_f32_16x16x32_bf16 v[64:67], a[28:31], v[40:43], v[64:67]\n"
"s_waitcnt lgkmcnt(8)\n"
"ds_read_b64_tr_b16 v[174:175], v120 offset:58376\n"
"v_exp_f32 v76, v76\n"
"v_mfma_f32_16x16x32_bf16 v[60:63], a[32:35], v[44:47], v[60:63]\n"
"s_add_u32 s40, s40, 278528\n"
"ds_read_b128 v[40:43], v116 offset:43008\n"
"v_exp_f32 v77, v77\n"
"v_mfma_f32_16x16x32_bf16 v[64:67], a[36:39], v[44:47], v[64:67]\n"
"v_add_f32 v76, 1.0, v76\n"
"v_add_f32 v77, 1.0, v77\n"
"v_mul_f32 v78, v78, v70\n"
"v_mfma_f32_16x16x32_bf16 v[60:63], a[40:43], v[48:51], v[60:63]\n"
"ds_read_b128 v[44:47], v114 offset:45056\n"
"v_rcp_f32 v76, v76\n"
"v_mfma_f32_16x16x32_bf16 v[64:67], a[44:47], v[48:51], v[64:67]\n"
"s_waitcnt lgkmcnt(8)\n"
"v_rcp_f32 v77, v77\n"
"v_mul_f32 v79, v71, v71\n"
"v_mfma_f32_16x16x32_bf16 v[60:63], a[48:51], v[52:55], v[60:63]\n"
"ds_read_b128 v[48:51], v116 offset:45056\n"
"v_exp_f32 v78, v78\n"
"v_mfma_f32_16x16x32_bf16 v[64:67], a[52:55], v[52:55], v[64:67]\n"
"v_add_f32 v78, 1.0, v78\n"
"v_fmamk_f32 v79, v79, 0xbdd2d3e2, v112\n"
"v_mfma_f32_16x16x32_bf16 v[60:63], a[56:59], v[56:59], v[60:63]\n"
"ds_read_b128 v[52:55], v114 offset:47104\n"
"v_mul_f32 v79, v79, v71\n"
"v_mfma_f32_16x16x32_bf16 v[64:67], a[60:63], v[56:59], v[64:67]\n"
"s_waitcnt lgkmcnt(7)\n"
"v_rcp_f32 v78, v78\n"
"v_mfma_f32_16x16x32_bf16 v[60:63], a[64:67], v[28:31], v[60:63]\n"
"s_waitcnt vmcnt(13)\n"
"ds_read_b128 v[56:59], v116 offset:47104\n"
"v_mul_f32 v80, v72, v72\n"
"v_mfma_f32_16x16x32_bf16 v[64:67], a[68:71], v[28:31], v[64:67]\n"
"s_barrier\n"
"v_exp_f32 v79, v79\n"
"v_mfma_f32_16x16x32_bf16 v[60:63], a[72:75], v[32:35], v[60:63]\n"
"s_add_u32 s93, s101, 106496\n"
"s_add_u32 m0, s44, 16384\n"
"ds_read_b128 v[28:31], v114 offset:49152\n"
"v_mfma_f32_16x16x32_bf16 v[64:67], a[76:79], v[32:35], v[64:67]\n"
"s_waitcnt lgkmcnt(5)\n"
"buffer_store_dwordx4 v[24:27], v22, s[72:75], s93 offen sc1\n"
"buffer_load_dwordx4 v113, s[64:67], s40 offen offset:0 sc1 lds\n"
"v_mfma_f32_16x16x32_bf16 v[60:63], a[80:83], v[36:39], v[60:63]\n"
"ds_read_b128 v[32:35], v116 offset:49152\n"
"ds_read_b64_tr_b16 v[176:177], v121 offset:0\n"
"v_mfma_f32_16x16x32_bf16 v[64:67], a[84:87], v[36:39], v[64:67]\n"
"v_add_f32 v79, 1.0, v79\n"
"v_fmamk_f32 v80, v80, 0xbdd2d3e2, v112\n"
"v_mfma_f32_16x16x32_bf16 v[60:63], a[88:91], v[40:43], v[60:63]\n"
"ds_read_b128 v[36:39], v114 offset:51200\n"
"buffer_load_dwordx4 v113, s[64:67], s40 offen offset:1024 sc1 lds\n"
"v_mfma_f32_16x16x32_bf16 v[64:67], a[92:95], v[40:43], v[64:67]\n"
"s_waitcnt lgkmcnt(6)\n"
"ds_read_b64_tr_b16 v[178:179], v121 offset:1024\n"
"v_mul_f32 v80, v80, v72\n"
"v_mfma_f32_16x16x32_bf16 v[60:63], a[96:99], v[44:47], v[60:63]\n"
"ds_read_b128 v[40:43], v116 offset:51200\n"
"buffer_load_dwordx4 v113, s[64:67], s40 offen offset:2048 sc1 lds\n"
"v_mfma_f32_16x16x32_bf16 v[64:67], a[100:103], v[44:47], v[64:67]\n"
"ds_read_b64_tr_b16 v[180:181], v121 offset:8\n"
"v_mul_f32 v81, v73, v73\n"
"v_mfma_f32_16x16x32_bf16 v[60:63], a[104:107], v[48:51], v[60:63]\n"
"ds_read_b128 v[44:47], v114 offset:53248\n"
"buffer_load_dwordx4 v113, s[64:67], s40 offen offset:3072 sc1 lds\n"
"v_mfma_f32_16x16x32_bf16 v[64:67], a[108:111], v[48:51], v[64:67]\n"
"s_waitcnt lgkmcnt(8)\n"
"v_rcp_f32 v79, v79\n"
"v_mfma_f32_16x16x32_bf16 v[60:63], a[112:115], v[52:55], v[60:63]\n"
"s_add_u32 s40, s40, 16384\n"
"ds_read_b128 v[48:51], v116 offset:53248\n"
"ds_read_b64_tr_b16 v[182:183], v121 offset:1032\n"
"v_mfma_f32_16x16x32_bf16 v[64:67], a[116:119], v[52:55], v[64:67]\n"
"s_add_u32 s95, s43, 655360\n"
"v_exp_f32 v80, v80\n"
"v_mfma_f32_16x16x32_bf16 v[60:63], a[120:123], v[56:59], v[60:63]\n"
"s_add_u32 m0, s44, 81920\n"
"ds_read_b128 v[52:55], v114 offset:55296\n"
"ds_read_b64_tr_b16 v[184:185], v121 offset:8192\n"
"v_mfma_f32_16x16x32_bf16 v[64:67], a[124:127], v[56:59], v[64:67]\n"
"s_waitcnt lgkmcnt(10)\n"
"v_add_f32 v80, 1.0, v80\n"
"v_fmamk_f32 v81, v81, 0xbdd2d3e2, v112\n"
"v_mfma_f32_16x16x32_bf16 v[60:63], a[128:131], v[28:31], v[60:63]\n"
"ds_read_b128 v[56:59], v116 offset:55296\n"
"buffer_load_dwordx4 v122, s[80:83], s95 offen offset:0 lds\n"
"v_mfma_f32_16x16x32_bf16 v[64:67], a[132:135], v[28:31], v[64:67]\n"
"ds_read_b64_tr_b16 v[186:187], v121 offset:9216\n"
"v_mul_f32 v81, v81, v73\n"
"v_mfma_f32_16x16x32_bf16 v[60:63], a[136:139], v[32:35], v[60:63]\n"
"ds_read_b128 v[28:31], v114 offset:57344\n"
"buffer_load_dwordx4 v123, s[80:83], s95 offen offset:1024 lds\n"
"v_mfma_f32_16x16x32_bf16 v[64:67], a[140:143], v[32:35], v[64:67]\n"
"s_waitcnt lgkmcnt(9)\n"
"ds_read_b64_tr_b16 v[188:189], v121 offset:8200\n"
"v_mul_f32 v82, v74, v74\n"
"v_mfma_f32_16x16x32_bf16 v[60:63], a[144:147], v[36:39], v[60:63]\n"
"ds_read_b128 v[32:35], v116 offset:57344\n"
"buffer_load_dwordx4 v124, s[80:83], s95 offen offset:2048 lds\n"
"v_mfma_f32_16x16x32_bf16 v[64:67], a[148:151], v[36:39], v[64:67]\n"
"ds_read_b64_tr_b16 v[190:191], v121 offset:9224\n"
"v_fmamk_f32 v82, v82, 0xbdd2d3e2, v112\n"
"v_mfma_f32_16x16x32_bf16 v[60:63], a[152:155], v[40:43], v[60:63]\n"
"ds_read_b128 v[36:39], v114 offset:59392\n"
"buffer_load_dwordx4 v125, s[80:83], s95 offen offset:3072 lds\n"
"v_mfma_f32_16x16x32_bf16 v[64:67], a[156:159], v[40:43], v[64:67]\n"
"s_waitcnt lgkmcnt(10)\n"
"v_rcp_f32 v80, v80\n"
"v_mfma_f32_16x16x32_bf16 v[60:63], a[160:163], v[44:47], v[60:63]\n"
"ds_read_b128 v[40:43], v116 offset:59392\n"
"v_mul_f32 v82, v82, v74\n"
"v_mfma_f32_16x16x32_bf16 v[64:67], a[164:167], v[44:47], v[64:67]\n"
"v_exp_f32 v81, v81\n"
"v_mfma_f32_16x16x32_bf16 v[60:63], a[168:171], v[48:51], v[60:63]\n"
"ds_read_b128 v[44:47], v114 offset:61440\n"
"v_add_f32 v81, 1.0, v81\n"
"v_mfma_f32_16x16x32_bf16 v[64:67], a[172:175], v[48:51], v[64:67]\n"
"s_waitcnt lgkmcnt(8)\n"
"v_rcp_f32 v81, v81\n"
"v_mfma_f32_16x16x32_bf16 v[60:63], a[176:179], v[52:55], v[60:63]\n"
"ds_read_b128 v[48:51], v116 offset:61440\n"
"v_mul_f32 v83, v75, v75\n"
"v_mfma_f32_16x16x32_bf16 v[64:67], a[180:183], v[52:55], v[64:67]\n"
"v_exp_f32 v82, v82\n"
"v_mfma_f32_16x16x32_bf16 v[60:63], a[184:187], v[56:59], v[60:63]\n"
"ds_read_b128 v[52:55], v114 offset:63488\n"
"v_add_f32 v82, 1.0, v82\n"
"v_mfma_f32_16x16x32_bf16 v[64:67], a[188:191], v[56:59], v[64:67]\n"
"s_waitcnt lgkmcnt(6)\n"
"v_rcp_f32 v82, v82\n"
"v_mfma_f32_16x16x32_bf16 v[60:63], a[192:195], v[28:31], v[60:63]\n"
"s_waitcnt vmcnt(18)\n"
"ds_read_b128 v[56:59], v116 offset:63488\n"
"v_fmamk_f32 v83, v83, 0xbdd2d3e2, v112\n"
"v_mfma_f32_16x16x32_bf16 v[64:67], a[196:199], v[28:31], v[64:67]\n"
"s_barrier\n"
"v_mul_f32 v83, v83, v75\n"
"v_mul_f32 v76, v68, v76\n"
"v_mfma_f32_16x16x32_bf16 v[60:63], a[200:203], v[32:35], v[60:63]\n"
"s_add_u32 m0, s44, 32768\n"
"buffer_atomic_add v1, v0, s[72:75], 0 offen offset:0\n"
"ds_read_b128 v[28:31], v115 offset:0\n"
"v_mfma_f32_16x16x32_bf16 v[64:67], a[204:207], v[32:35], v[64:67]\n"
"s_waitcnt lgkmcnt(5)\n"
"v_exp_f32 v83, v83\n"
"v_mfma_f32_16x16x32_bf16 v[60:63], a[208:211], v[36:39], v[60:63]\n"
"ds_read_b128 v[32:35], v117 offset:0\n"
"buffer_load_dwordx4 v113, s[64:67], s40 offen offset:0 sc1 lds\n"
"v_mfma_f32_16x16x32_bf16 v[64:67], a[212:215], v[36:39], v[64:67]\n"
"v_add_f32 v83, 1.0, v83\n"
"v_mul_f32 v77, v69, v77\n"
"v_mfma_f32_16x16x32_bf16 v[60:63], a[216:219], v[40:43], v[60:63]\n"
"ds_read_b128 v[36:39], v115 offset:2048\n"
"v_mul_f32 v78, v70, v78\n"
"v_mfma_f32_16x16x32_bf16 v[64:67], a[220:223], v[40:43], v[64:67]\n"
"s_waitcnt lgkmcnt(5)\n"
"v_rcp_f32 v83, v83\n"
"v_mfma_f32_16x16x32_bf16 v[60:63], a[224:227], v[44:47], v[60:63]\n"
"ds_read_b128 v[40:43], v117 offset:2048\n"
"v_mul_f32 v79, v71, v79\n"
"v_mfma_f32_16x16x32_bf16 v[64:67], a[228:231], v[44:47], v[64:67]\n"
"v_mul_f32 v80, v72, v80\n"
"v_mul_f32 v81, v73, v81\n"
"v_mfma_f32_16x16x32_bf16 v[60:63], a[232:235], v[48:51], v[60:63]\n"
"ds_read_b128 v[44:47], v115 offset:4096\n"
"buffer_load_dwordx4 v113, s[64:67], s40 offen offset:1024 sc1 lds\n"
"v_mfma_f32_16x16x32_bf16 v[64:67], a[236:239], v[48:51], v[64:67]\n"
"s_waitcnt lgkmcnt(5)\n"
"v_mul_f32 v82, v74, v82\n"
"v_mul_f32 v83, v75, v83\n"
"v_mfma_f32_16x16x32_bf16 v[60:63], a[240:243], v[52:55], v[60:63]\n"
"ds_read_b128 v[48:51], v117 offset:4096\n"
"v_cvt_pk_bf16_f32 v84, v76, v77\n"
"v_mfma_f32_16x16x32_bf16 v[64:67], a[244:247], v[52:55], v[64:67]\n"
"v_cvt_pk_bf16_f32 v85, v78, v79\n"
"v_cvt_pk_bf16_f32 v86, v80, v81\n"
"v_mfma_f32_16x16x32_bf16 v[60:63], a[248:251], v[56:59], v[60:63]\n"
"ds_read_b128 v[52:55], v115 offset:6144\n"
"v_cvt_pk_bf16_f32 v87, v82, v83\n"
"v_mfma_f32_16x16x32_bf16 v[64:67], a[252:255], v[56:59], v[64:67]\n"
"s_waitcnt lgkmcnt(5)\n"
"v_mfma_f32_16x16x32_bf16 v[68:71], a[0:3], v[28:31], v[88:91]\n"
"ds_read_b128 v[56:59], v117 offset:6144\n"
"buffer_load_dwordx4 v113, s[64:67], s40 offen offset:2048 sc1 lds\n"
"v_mfma_f32_16x16x32_bf16 v[72:75], a[4:7], v[28:31], v[92:95]\n"
"buffer_store_dwordx4 v[84:87], v113, s[68:71], s41 offen sc1\n"
"v_mfma_f32_16x16x32_bf16 v[68:71], a[8:11], v[32:35], v[68:71]\n"
"s_add_u32 s41, s41, 1032192\n"
"ds_read_b128 v[28:31], v115 offset:8192\n"
"v_mfma_f32_16x16x32_bf16 v[72:75], a[12:15], v[32:35], v[72:75]\n"
"s_waitcnt lgkmcnt(5)\n"
"v_mul_f32 v76, v60, v60\n"
"v_mul_f32 v77, v61, v61\n"
"v_mul_f32 v78, v62, v62\n"
"v_mfma_f32_16x16x32_bf16 v[68:71], a[16:19], v[36:39], v[68:71]\n"
"ds_read_b128 v[32:35], v117 offset:8192\n"
"v_fmamk_f32 v76, v76, 0xbdd2d3e2, v112\n"
"v_fmamk_f32 v77, v77, 0xbdd2d3e2, v112\n"
"v_mfma_f32_16x16x32_bf16 v[72:75], a[20:23], v[36:39], v[72:75]\n"
"v_mul_f32 v76, v76, v60\n"
"v_mul_f32 v77, v77, v61\n"
"v_fmamk_f32 v78, v78, 0xbdd2d3e2, v112\n"
"v_mfma_f32_16x16x32_bf16 v[68:71], a[24:27], v[40:43], v[68:71]\n"
"ds_read_b128 v[36:39], v115 offset:10240\n"
"buffer_load_dwordx4 v113, s[64:67], s40 offen offset:3072 sc1 lds\n"
"v_mul_f32 v78, v78, v62\n"
"v_mfma_f32_16x16x32_bf16 v[72:75], a[28:31], v[40:43], v[72:75]\n"
"s_waitcnt lgkmcnt(5)\n"
"v_exp_f32 v76, v76\n"
"v_mul_f32 v79, v63, v63\n"
"v_mfma_f32_16x16x32_bf16 v[68:71], a[32:35], v[44:47], v[68:71]\n"
"s_add_u32 s40, s40, 278528\n"
"ds_read_b128 v[40:43], v117 offset:10240\n"
"v_exp_f32 v77, v77\n"
"v_mfma_f32_16x16x32_bf16 v[72:75], a[36:39], v[44:47], v[72:75]\n"
"v_exp_f32 v78, v78\n"
"v_add_f32 v76, 1.0, v76\n"
"v_mfma_f32_16x16x32_bf16 v[68:71], a[40:43], v[48:51], v[68:71]\n"
"ds_read_b128 v[44:47], v115 offset:12288\n"
"v_rcp_f32 v76, v76\n"
"v_mfma_f32_16x16x32_bf16 v[72:75], a[44:47], v[48:51], v[72:75]\n"
"s_waitcnt lgkmcnt(5)\n"
"v_add_f32 v77, 1.0, v77\n"
"v_add_f32 v78, 1.0, v78\n"
"v_mfma_f32_16x16x32_bf16 v[68:71], a[48:51], v[52:55], v[68:71]\n"
"ds_read_b128 v[48:51], v117 offset:12288\n"
"v_fmamk_f32 v79, v79, 0xbdd2d3e2, v112\n"
"v_mfma_f32_16x16x32_bf16 v[72:75], a[52:55], v[52:55], v[72:75]\n"
"v_rcp_f32 v77, v77\n"
"v_mfma_f32_16x16x32_bf16 v[68:71], a[56:59], v[56:59], v[68:71]\n"
"ds_read_b128 v[52:55], v115 offset:14336\n"
"v_mul_f32 v79, v79, v63\n"
"v_mfma_f32_16x16x32_bf16 v[72:75], a[60:63], v[56:59], v[72:75]\n"
"s_waitcnt lgkmcnt(5)\n"
"v_rcp_f32 v78, v78\n"
"v_mfma_f32_16x16x32_bf16 v[68:71], a[64:67], v[28:31], v[68:71]\n"
"s_waitcnt vmcnt(15)\n"
"ds_read_b128 v[56:59], v117 offset:14336\n"
"v_mul_f32 v80, v64, v64\n"
"v_mfma_f32_16x16x32_bf16 v[72:75], a[68:71], v[28:31], v[72:75]\n"
"s_barrier\n"
"v_exp_f32 v79, v79\n"
"v_mfma_f32_16x16x32_bf16 v[68:71], a[72:75], v[32:35], v[68:71]\n"
"s_add_u32 m0, s44, 49152\n"
"ds_read_b128 v[28:31], v114 offset:0\n"
"ds_read_b64_tr_b16 v[192:193], v121 offset:16384\n"
"v_mfma_f32_16x16x32_bf16 v[72:75], a[76:79], v[32:35], v[72:75]\n"
"s_waitcnt lgkmcnt(6)\n"
"v_add_f32 v79, 1.0, v79\n"
"v_fmamk_f32 v80, v80, 0xbdd2d3e2, v112\n"
"v_mfma_f32_16x16x32_bf16 v[68:71], a[80:83], v[36:39], v[68:71]\n"
"ds_read_b128 v[32:35], v116 offset:0\n"
"buffer_load_dwordx4 v113, s[64:67], s40 offen offset:0 sc1 lds\n"
"v_mfma_f32_16x16x32_bf16 v[72:75], a[84:87], v[36:39], v[72:75]\n"
"ds_read_b64_tr_b16 v[194:195], v121 offset:17408\n"
"v_mul_f32 v80, v80, v64\n"
"v_mfma_f32_16x16x32_bf16 v[68:71], a[88:91], v[40:43], v[68:71]\n"
"ds_read_b128 v[36:39], v114 offset:2048\n"
"buffer_load_dwordx4 v113, s[64:67], s40 offen offset:1024 sc1 lds\n"
"v_mfma_f32_16x16x32_bf16 v[72:75], a[92:95], v[40:43], v[72:75]\n"
"s_waitcnt lgkmcnt(7)\n"
"ds_read_b64_tr_b16 v[196:197], v121 offset:16392\n"
"v_mul_f32 v81, v65, v65\n"
"v_mfma_f32_16x16x32_bf16 v[68:71], a[96:99], v[44:47], v[68:71]\n"
"ds_read_b128 v[40:43], v116 offset:2048\n"
"buffer_load_dwordx4 v113, s[64:67], s40 offen offset:2048 sc1 lds\n"
"v_mfma_f32_16x16x32_bf16 v[72:75], a[100:103], v[44:47], v[72:75]\n"
"ds_read_b64_tr_b16 v[198:199], v121 offset:17416\n"
"v_fmamk_f32 v81, v81, 0xbdd2d3e2, v112\n"
"v_mfma_f32_16x16x32_bf16 v[68:71], a[104:107], v[48:51], v[68:71]\n"
"ds_read_b128 v[44:47], v114 offset:4096\n"
"buffer_load_dwordx4 v113, s[64:67], s40 offen offset:3072 sc1 lds\n"
"v_mfma_f32_16x16x32_bf16 v[72:75], a[108:111], v[48:51], v[72:75]\n"
"s_waitcnt lgkmcnt(9)\n"
"v_rcp_f32 v79, v79\n"
"v_mfma_f32_16x16x32_bf16 v[68:71], a[112:115], v[52:55], v[68:71]\n"
"s_add_u32 s40, s40, 16384\n"
"ds_read_b128 v[48:51], v116 offset:4096\n"
"ds_read_b64_tr_b16 v[200:201], v121 offset:24576\n"
"v_mfma_f32_16x16x32_bf16 v[72:75], a[116:119], v[52:55], v[72:75]\n"
"s_add_u32 s95, s43, 786432\n"
"v_exp_f32 v80, v80\n"
"v_mfma_f32_16x16x32_bf16 v[68:71], a[120:123], v[56:59], v[68:71]\n"
"s_add_u32 m0, s44, 98304\n"
"ds_read_b128 v[52:55], v114 offset:6144\n"
"ds_read_b64_tr_b16 v[202:203], v121 offset:25600\n"
"v_mfma_f32_16x16x32_bf16 v[72:75], a[124:127], v[56:59], v[72:75]\n"
"s_waitcnt lgkmcnt(10)\n"
"v_add_f32 v80, 1.0, v80\n"
"v_mul_f32 v81, v81, v65\n"
"v_mfma_f32_16x16x32_bf16 v[68:71], a[128:131], v[28:31], v[68:71]\n"
"s_getpc_b64 s[62:63]\n"
"ds_read_b128 v[56:59], v116 offset:6144\n"
"buffer_load_dwordx4 v122, s[80:83], s95 offen offset:0 lds\n"
"v_mfma_f32_16x16x32_bf16 v[72:75], a[132:135], v[28:31], v[72:75]\n"
"global_load_dword v21, v119, s[62:63] offset:3072\n"
"ds_read_b64_tr_b16 v[204:205], v121 offset:24584\n"
"v_mfma_f32_16x16x32_bf16 v[68:71], a[136:139], v[32:35], v[68:71]\n"
"ds_read_b128 v[28:31], v114 offset:8192\n"
"buffer_load_dwordx4 v123, s[80:83], s95 offen offset:1024 lds\n"
"v_mfma_f32_16x16x32_bf16 v[72:75], a[140:143], v[32:35], v[72:75]\n"
"s_waitcnt lgkmcnt(9)\n"
"ds_read_b64_tr_b16 v[206:207], v121 offset:25608\n"
"v_mul_f32 v82, v66, v66\n"
"v_mfma_f32_16x16x32_bf16 v[68:71], a[144:147], v[36:39], v[68:71]\n"
"ds_read_b128 v[32:35], v116 offset:8192\n"
"buffer_load_dwordx4 v124, s[80:83], s95 offen offset:2048 lds\n"
"v_mfma_f32_16x16x32_bf16 v[72:75], a[148:151], v[36:39], v[72:75]\n"
"v_rcp_f32 v80, v80\n"
"v_mfma_f32_16x16x32_bf16 v[68:71], a[152:155], v[40:43], v[68:71]\n"
"ds_read_b128 v[36:39], v114 offset:10240\n"
"buffer_load_dwordx4 v125, s[80:83], s95 offen offset:3072 lds\n"
"v_mfma_f32_16x16x32_bf16 v[72:75], a[156:159], v[40:43], v[72:75]\n"
"s_waitcnt lgkmcnt(9)\n"
"v_exp_f32 v81, v81\n"
"v_mfma_f32_16x16x32_bf16 v[68:71], a[160:163], v[44:47], v[68:71]\n"
"ds_read_b128 v[40:43], v116 offset:10240\n"
"v_add_f32 v81, 1.0, v81\n"
"v_mfma_f32_16x16x32_bf16 v[72:75], a[164:167], v[44:47], v[72:75]\n"
"v_rcp_f32 v81, v81\n"
"v_mfma_f32_16x16x32_bf16 v[68:71], a[168:171], v[48:51], v[68:71]\n"
"ds_read_b128 v[44:47], v114 offset:12288\n"
"v_fmamk_f32 v82, v82, 0xbdd2d3e2, v112\n"
"v_mfma_f32_16x16x32_bf16 v[72:75], a[172:175], v[48:51], v[72:75]\n"
"s_waitcnt lgkmcnt(7)\n"
"v_mul_f32 v82, v82, v66\n"
"v_mul_f32 v83, v67, v67\n"
"v_mfma_f32_16x16x32_bf16 v[68:71], a[176:179], v[52:55], v[68:71]\n"
"ds_read_b128 v[48:51], v116 offset:12288\n"
"v_fmamk_f32 v83, v83, 0xbdd2d3e2, v112\n"
"v_mfma_f32_16x16x32_bf16 v[72:75], a[180:183], v[52:55], v[72:75]\n"
"v_exp_f32 v82, v82\n"
"v_mfma_f32_16x16x32_bf16 v[68:71], a[184:187], v[56:59], v[68:71]\n"
"ds_read_b128 v[52:55], v114 offset:14336\n"
"v_add_f32 v82, 1.0, v82\n"
"v_mfma_f32_16x16x32_bf16 v[72:75], a[188:191], v[56:59], v[72:75]\n"
"s_waitcnt lgkmcnt(5)\n"
"v_rcp_f32 v82, v82\n"
"v_mfma_f32_16x16x32_bf16 v[68:71], a[192:195], v[28:31], v[68:71]\n"
"s_waitcnt vmcnt(19)\n"
"ds_read_b128 v[56:59], v116 offset:14336\n"
"v_mul_f32 v83, v83, v67\n"
"v_mfma_f32_16x16x32_bf16 v[72:75], a[196:199], v[28:31], v[72:75]\n"
"s_barrier\n"
"v_exp_f32 v83, v83\n"
"v_mfma_f32_16x16x32_bf16 v[68:71], a[200:203], v[32:35], v[68:71]\n"
"s_add_u32 m0, s44, 65536\n"
"ds_read_b128 v[28:31], v114 offset:16384\n"
"v_add_f32 v83, 1.0, v83\n"
"v_mfma_f32_16x16x32_bf16 v[72:75], a[204:207], v[32:35], v[72:75]\n"
"s_waitcnt lgkmcnt(5)\n"
"v_rcp_f32 v83, v83\n"
"v_mfma_f32_16x16x32_bf16 v[68:71], a[208:211], v[36:39], v[68:71]\n"
"ds_read_b128 v[32:35], v116 offset:16384\n"
"buffer_load_dwordx4 v113, s[64:67], s40 offen offset:0 sc1 lds\n"
"v_mfma_f32_16x16x32_bf16 v[72:75], a[212:215], v[36:39], v[72:75]\n"
"v_mul_f32 v76, v60, v76\n"
"v_mul_f32 v77, v61, v77\n"
"v_mfma_f32_16x16x32_bf16 v[68:71], a[216:219], v[40:43], v[68:71]\n"
"ds_read_b128 v[36:39], v114 offset:18432\n"
"buffer_load_dwordx4 v113, s[64:67], s40 offen offset:1024 sc1 lds\n"
"v_mfma_f32_16x16x32_bf16 v[72:75], a[220:223], v[40:43], v[72:75]\n"
"s_waitcnt lgkmcnt(5)\n"
"v_mul_f32 v78, v62, v78\n"
"v_mul_f32 v79, v63, v79\n"
"v_mfma_f32_16x16x32_bf16 v[68:71], a[224:227], v[44:47], v[68:71]\n"
"ds_read_b128 v[40:43], v116 offset:18432\n"
"buffer_load_dwordx4 v113, s[64:67], s40 offen offset:2048 sc1 lds\n"
"v_mfma_f32_16x16x32_bf16 v[72:75], a[228:231], v[44:47], v[72:75]\n"
"v_mul_f32 v80, v64, v80\n"
"v_mul_f32 v81, v65, v81\n"
"v_mfma_f32_16x16x32_bf16 v[68:71], a[232:235], v[48:51], v[68:71]\n"
"ds_read_b128 v[44:47], v114 offset:20480\n"
"buffer_load_dwordx4 v113, s[64:67], s40 offen offset:3072 sc1 lds\n"
"v_mfma_f32_16x16x32_bf16 v[72:75], a[236:239], v[48:51], v[72:75]\n"
"s_waitcnt lgkmcnt(5)\n"
"v_mul_f32 v82, v66, v82\n"
"v_mul_f32 v83, v67, v83\n"
"v_mfma_f32_16x16x32_bf16 v[68:71], a[240:243], v[52:55], v[68:71]\n"
"s_add_u32 s40, s40, 278528\n"
"ds_read_b128 v[48:51], v116 offset:20480\n"
"v_cvt_pk_bf16_f32 v84, v76, v77\n"
"v_mfma_f32_16x16x32_bf16 v[72:75], a[244:247], v[52:55], v[72:75]\n"
"s_add_u32 s95, s43, 917504\n"
"v_cvt_pk_bf16_f32 v85, v78, v79\n"
"v_cvt_pk_bf16_f32 v86, v80, v81\n"
"v_mfma_f32_16x16x32_bf16 v[68:71], a[248:251], v[56:59], v[68:71]\n"
"s_add_u32 m0, s44, 114688\n"
"ds_read_b128 v[52:55], v114 offset:22528\n"
"v_cvt_pk_bf16_f32 v87, v82, v83\n"
"v_mfma_f32_16x16x32_bf16 v[72:75], a[252:255], v[56:59], v[72:75]\n"
"s_waitcnt lgkmcnt(5)\n"
"v_mfma_f32_16x16x32_bf16 v[60:63], a[0:3], v[28:31], v[88:91]\n"
"ds_read_b128 v[56:59], v116 offset:22528\n"
"buffer_load_dwordx4 v122, s[80:83], s95 offen offset:0 lds\n"
"v_mfma_f32_16x16x32_bf16 v[64:67], a[4:7], v[28:31], v[92:95]\n"
"buffer_store_dwordx4 v[84:87], v113, s[68:71], s41 offen sc1\n"
"v_mfma_f32_16x16x32_bf16 v[60:63], a[8:11], v[32:35], v[60:63]\n"
"s_add_u32 s41, s41, 1032192\n"
"ds_read_b128 v[28:31], v114 offset:24576\n"
"buffer_load_dwordx4 v123, s[80:83], s95 offen offset:1024 lds\n"
"v_mfma_f32_16x16x32_bf16 v[64:67], a[12:15], v[32:35], v[64:67]\n"
"s_waitcnt lgkmcnt(5)\n"
"v_mul_f32 v76, v68, v68\n"
"v_mul_f32 v77, v69, v69\n"
"v_mul_f32 v78, v70, v70\n"
"v_mfma_f32_16x16x32_bf16 v[60:63], a[16:19], v[36:39], v[60:63]\n"
"ds_read_b128 v[32:35], v116 offset:24576\n"
"buffer_load_dwordx4 v124, s[80:83], s95 offen offset:2048 lds\n"
"v_fmamk_f32 v76, v76, 0xbdd2d3e2, v112\n"
"v_mfma_f32_16x16x32_bf16 v[64:67], a[20:23], v[36:39], v[64:67]\n"
"v_mul_f32 v76, v76, v68\n"
"v_fmamk_f32 v77, v77, 0xbdd2d3e2, v112\n"
"v_fmamk_f32 v78, v78, 0xbdd2d3e2, v112\n"
"v_mfma_f32_16x16x32_bf16 v[60:63], a[24:27], v[40:43], v[60:63]\n"
"ds_read_b128 v[36:39], v114 offset:26624\n"
"buffer_load_dwordx4 v125, s[80:83], s95 offen offset:3072 lds\n"
"v_mul_f32 v77, v77, v69\n"
"v_mfma_f32_16x16x32_bf16 v[64:67], a[28:31], v[40:43], v[64:67]\n"
"s_waitcnt lgkmcnt(5)\n"
"v_exp_f32 v76, v76\n"
"v_mfma_f32_16x16x32_bf16 v[60:63], a[32:35], v[44:47], v[60:63]\n"
"ds_read_b128 v[40:43], v116 offset:26624\n"
"v_add_f32 v76, 1.0, v76\n"
"v_mfma_f32_16x16x32_bf16 v[64:67], a[36:39], v[44:47], v[64:67]\n"
"v_rcp_f32 v76, v76\n"
"v_mfma_f32_16x16x32_bf16 v[60:63], a[40:43], v[48:51], v[60:63]\n"
"ds_read_b128 v[44:47], v114 offset:28672\n"
"v_mul_f32 v78, v78, v70\n"
"v_mfma_f32_16x16x32_bf16 v[64:67], a[44:47], v[48:51], v[64:67]\n"
"s_waitcnt lgkmcnt(5)\n"
"v_exp_f32 v77, v77\n"
"v_mfma_f32_16x16x32_bf16 v[60:63], a[48:51], v[52:55], v[60:63]\n"
"ds_read_b128 v[48:51], v116 offset:28672\n"
"v_add_f32 v77, 1.0, v77\n"
"v_mfma_f32_16x16x32_bf16 v[64:67], a[52:55], v[52:55], v[64:67]\n"
"v_rcp_f32 v77, v77\n"
"v_mfma_f32_16x16x32_bf16 v[60:63], a[56:59], v[56:59], v[60:63]\n"
"ds_read_b128 v[52:55], v114 offset:30720\n"
"v_mul_f32 v79, v71, v71\n"
"v_mfma_f32_16x16x32_bf16 v[64:67], a[60:63], v[56:59], v[64:67]\n"
"s_waitcnt lgkmcnt(5)\n"
"v_exp_f32 v78, v78\n"
"v_mfma_f32_16x16x32_bf16 v[60:63], a[64:67], v[28:31], v[60:63]\n"
"s_waitcnt vmcnt(18)\n"
"ds_read_b128 v[56:59], v116 offset:30720\n"
"v_add_f32 v78, 1.0, v78\n"
"v_mfma_f32_16x16x32_bf16 v[64:67], a[68:71], v[28:31], v[64:67]\n"
"s_barrier\n"
"v_rcp_f32 v78, v78\n"
"v_mfma_f32_16x16x32_bf16 v[60:63], a[72:75], v[32:35], v[60:63]\n"
"s_add_u32 m0, s44, 0\n"
"ds_read_b128 v[28:31], v114 offset:32768\n"
"ds_read_b64_tr_b16 v[208:209], v120 offset:16384\n"
"v_mfma_f32_16x16x32_bf16 v[64:67], a[76:79], v[32:35], v[64:67]\n"
"s_waitcnt lgkmcnt(6)\n"
"v_fmamk_f32 v79, v79, 0xbdd2d3e2, v112\n"
"v_mul_f32 v80, v72, v72\n"
"v_mfma_f32_16x16x32_bf16 v[60:63], a[80:83], v[36:39], v[60:63]\n"
"ds_read_b128 v[32:35], v116 offset:32768\n"
"buffer_load_dwordx4 v113, s[64:67], s40 offen offset:0 sc1 lds\n"
"v_mfma_f32_16x16x32_bf16 v[64:67], a[84:87], v[36:39], v[64:67]\n"
"ds_read_b64_tr_b16 v[210:211], v120 offset:17408\n"
"v_mul_f32 v79, v79, v71\n"
"v_mfma_f32_16x16x32_bf16 v[60:63], a[88:91], v[40:43], v[60:63]\n"
"ds_read_b128 v[36:39], v114 offset:34816\n"
"v_fmamk_f32 v80, v80, 0xbdd2d3e2, v112\n"
"v_mfma_f32_16x16x32_bf16 v[64:67], a[92:95], v[40:43], v[64:67]\n"
"s_waitcnt lgkmcnt(7)\n"
"v_exp_f32 v79, v79\n"
"v_mfma_f32_16x16x32_bf16 v[60:63], a[96:99], v[44:47], v[60:63]\n"
"ds_read_b128 v[40:43], v116 offset:34816\n"
"ds_read_b64_tr_b16 v[212:213], v120 offset:16392\n"
"v_mfma_f32_16x16x32_bf16 v[64:67], a[100:103], v[44:47], v[64:67]\n"
"v_add_f32 v79, 1.0, v79\n"
"v_mul_f32 v80, v80, v72\n"
"v_mfma_f32_16x16x32_bf16 v[60:63], a[104:107], v[48:51], v[60:63]\n"
"ds_read_b128 v[44:47], v114 offset:36864\n"
"buffer_load_dwordx4 v113, s[64:67], s40 offen offset:1024 sc1 lds\n"
"v_mfma_f32_16x16x32_bf16 v[64:67], a[108:111], v[48:51], v[64:67]\n"
"s_waitcnt lgkmcnt(8)\n"
"ds_read_b64_tr_b16 v[214:215], v120 offset:17416\n"
"v_mul_f32 v81, v73, v73\n"
"v_mfma_f32_16x16x32_bf16 v[60:63], a[112:115], v[52:55], v[60:63]\n"
"ds_read_b128 v[48:51], v116 offset:36864\n"
"v_fmamk_f32 v81, v81, 0xbdd2d3e2, v112\n"
"v_mfma_f32_16x16x32_bf16 v[64:67], a[116:119], v[52:55], v[64:67]\n"
"v_rcp_f32 v79, v79\n"
"v_mfma_f32_16x16x32_bf16 v[60:63], a[120:123], v[56:59], v[60:63]\n"
"ds_read_b128 v[52:55], v114 offset:38912\n"
"ds_read_b64_tr_b16 v[216:217], v120 offset:24576\n"
"v_mfma_f32_16x16x32_bf16 v[64:67], a[124:127], v[56:59], v[64:67]\n"
"s_waitcnt lgkmcnt(9)\n"
"v_exp_f32 v80, v80\n"
"v_mfma_f32_16x16x32_bf16 v[60:63], a[128:131], v[28:31], v[60:63]\n"
"ds_read_b128 v[56:59], v116 offset:38912\n"
"buffer_load_dwordx4 v113, s[64:67], s40 offen offset:2048 sc1 lds\n"
"v_mfma_f32_16x16x32_bf16 v[64:67], a[132:135], v[28:31], v[64:67]\n"
"ds_read_b64_tr_b16 v[218:219], v120 offset:25600\n"
"v_add_f32 v80, 1.0, v80\n"
"v_mfma_f32_16x16x32_bf16 v[60:63], a[136:139], v[32:35], v[60:63]\n"
"ds_read_b128 v[28:31], v114 offset:40960\n"
"v_mul_f32 v81, v81, v73\n"
"v_mfma_f32_16x16x32_bf16 v[64:67], a[140:143], v[32:35], v[64:67]\n"
"s_waitcnt lgkmcnt(9)\n"
"v_rcp_f32 v80, v80\n"
"v_mfma_f32_16x16x32_bf16 v[60:63], a[144:147], v[36:39], v[60:63]\n"
"ds_read_b128 v[32:35], v116 offset:40960\n"
"ds_read_b64_tr_b16 v[220:221], v120 offset:24584\n"
"v_mfma_f32_16x16x32_bf16 v[64:67], a[148:151], v[36:39], v[64:67]\n"
"v_exp_f32 v81, v81\n"
"v_mfma_f32_16x16x32_bf16 v[60:63], a[152:155], v[40:43], v[60:63]\n"
"ds_read_b128 v[36:39], v114 offset:43008\n"
"buffer_load_dwordx4 v113, s[64:67], s40 offen offset:3072 sc1 lds\n"
"v_mfma_f32_16x16x32_bf16 v[64:67], a[156:159], v[40:43], v[64:67]\n"
"s_waitcnt lgkmcnt(8)\n"
"ds_read_b64_tr_b16 v[222:223], v120 offset:25608\n"
"v_add_f32 v81, 1.0, v81\n"
"v_mfma_f32_16x16x32_bf16 v[60:63], a[160:163], v[44:47], v[60:63]\n"
"s_add_u32 s40, s40, 16384\n"
"ds_read_b128 v[40:43], v116 offset:43008\n"
"v_mul_f32 v82, v74, v74\n"
"v_mfma_f32_16x16x32_bf16 v[64:67], a[164:167], v[44:47], v[64:67]\n"
"v_rcp_f32 v81, v81\n"
"v_mfma_f32_16x16x32_bf16 v[60:63], a[168:171], v[48:51], v[60:63]\n"
"ds_read_b128 v[44:47], v114 offset:45056\n"
"v_fmamk_f32 v82, v82, 0xbdd2d3e2, v112\n"
"v_mfma_f32_16x16x32_bf16 v[64:67], a[172:175], v[48:51], v[64:67]\n"
"s_waitcnt lgkmcnt(8)\n"
"v_mul_f32 v82, v82, v74\n"
"v_mul_f32 v83, v75, v75\n"
"v_mfma_f32_16x16x32_bf16 v[60:63], a[176:179], v[52:55], v[60:63]\n"
"ds_read_b128 v[48:51], v116 offset:45056\n"
"v_fmamk_f32 v83, v83, 0xbdd2d3e2, v112\n"
"v_mfma_f32_16x16x32_bf16 v[64:67], a[180:183], v[52:55], v[64:67]\n"
"v_exp_f32 v82, v82\n"
"v_mfma_f32_16x16x32_bf16 v[60:63], a[184:187], v[56:59], v[60:63]\n"
"ds_read_b128 v[52:55], v114 offset:47104\n"
"v_add_f32 v82, 1.0, v82\n"
"v_mfma_f32_16x16x32_bf16 v[64:67], a[188:191], v[56:59], v[64:67]\n"
"s_waitcnt lgkmcnt(7)\n"
"v_rcp_f32 v82, v82\n"
"v_mfma_f32_16x16x32_bf16 v[60:63], a[192:195], v[28:31], v[60:63]\n"
"s_waitcnt vmcnt(18)\n"
"ds_read_b128 v[56:59], v116 offset:47104\n"
"v_mul_f32 v83, v83, v75\n"
"v_mfma_f32_16x16x32_bf16 v[64:67], a[196:199], v[28:31], v[64:67]\n"
"s_barrier\n"
"v_exp_f32 v83, v83\n"
"v_mfma_f32_16x16x32_bf16 v[60:63], a[200:203], v[32:35], v[60:63]\n"
"s_add_u32 m0, s44, 16384\n"
"ds_read_b128 v[28:31], v114 offset:49152\n"
"v_add_f32 v83, 1.0, v83\n"
"v_mfma_f32_16x16x32_bf16 v[64:67], a[204:207], v[32:35], v[64:67]\n"
"s_waitcnt lgkmcnt(5)\n"
"v_rcp_f32 v83, v83\n"
"v_mfma_f32_16x16x32_bf16 v[60:63], a[208:211], v[36:39], v[60:63]\n"
"ds_read_b128 v[32:35], v116 offset:49152\n"
"buffer_load_dwordx4 v113, s[64:67], s40 offen offset:0 sc1 lds\n"
"v_mfma_f32_16x16x32_bf16 v[64:67], a[212:215], v[36:39], v[64:67]\n"
"v_mul_f32 v76, v68, v76\n"
"v_mul_f32 v77, v69, v77\n"
"v_mfma_f32_16x16x32_bf16 v[60:63], a[216:219], v[40:43], v[60:63]\n"
"ds_read_b128 v[36:39], v114 offset:51200\n"
"buffer_load_dwordx4 v113, s[64:67], s40 offen offset:1024 sc1 lds\n"
"v_mfma_f32_16x16x32_bf16 v[64:67], a[220:223], v[40:43], v[64:67]\n"
"s_waitcnt lgkmcnt(5)\n"
"v_mul_f32 v78, v70, v78\n"
"v_mul_f32 v79, v71, v79\n"
"v_mfma_f32_16x16x32_bf16 v[60:63], a[224:227], v[44:47], v[60:63]\n"
"ds_read_b128 v[40:43], v116 offset:51200\n"
"buffer_load_dwordx4 v113, s[64:67], s40 offen offset:2048 sc1 lds\n"
"v_mfma_f32_16x16x32_bf16 v[64:67], a[228:231], v[44:47], v[64:67]\n"
"v_mul_f32 v80, v72, v80\n"
"v_mul_f32 v81, v73, v81\n"
"v_mfma_f32_16x16x32_bf16 v[60:63], a[232:235], v[48:51], v[60:63]\n"
"ds_read_b128 v[44:47], v114 offset:53248\n"
"buffer_load_dwordx4 v113, s[64:67], s40 offen offset:3072 sc1 lds\n"
"v_mfma_f32_16x16x32_bf16 v[64:67], a[236:239], v[48:51], v[64:67]\n"
"s_waitcnt lgkmcnt(5)\n"
"v_mul_f32 v82, v74, v82\n"
"v_mul_f32 v83, v75, v83\n"
"v_mfma_f32_16x16x32_bf16 v[60:63], a[240:243], v[52:55], v[60:63]\n"
"s_add_u32 s40, s40, 278528\n"
"ds_read_b128 v[48:51], v116 offset:53248\n"
"v_cvt_pk_bf16_f32 v84, v76, v77\n"
"v_mfma_f32_16x16x32_bf16 v[64:67], a[244:247], v[52:55], v[64:67]\n"
"s_add_u32 s95, s43, 1703936\n"
"v_cvt_pk_bf16_f32 v85, v78, v79\n"
"v_cvt_pk_bf16_f32 v86, v80, v81\n"
"v_mfma_f32_16x16x32_bf16 v[60:63], a[248:251], v[56:59], v[60:63]\n"
"s_add_u32 m0, s44, 131072\n"
"ds_read_b128 v[52:55], v114 offset:55296\n"
"v_cvt_pk_bf16_f32 v87, v82, v83\n"
"v_mfma_f32_16x16x32_bf16 v[64:67], a[252:255], v[56:59], v[64:67]\n"
"s_waitcnt lgkmcnt(5)\n"
"v_mfma_f32_16x16x32_bf16 v[68:71], a[0:3], v[28:31], v[88:91]\n"
"ds_read_b128 v[56:59], v116 offset:55296\n"
"buffer_load_dwordx4 v122, s[80:83], s95 offen offset:0 lds\n"
"v_mfma_f32_16x16x32_bf16 v[72:75], a[4:7], v[28:31], v[92:95]\n"
"buffer_store_dwordx4 v[84:87], v113, s[68:71], s41 offen sc1\n"
"v_mfma_f32_16x16x32_bf16 v[68:71], a[8:11], v[32:35], v[68:71]\n"
"s_add_u32 s41, s41, 1032192\n"
"ds_read_b128 v[28:31], v114 offset:57344\n"
"buffer_load_dwordx4 v123, s[80:83], s95 offen offset:1024 lds\n"
"v_mfma_f32_16x16x32_bf16 v[72:75], a[12:15], v[32:35], v[72:75]\n"
"s_waitcnt lgkmcnt(5)\n"
"v_mul_f32 v76, v60, v60\n"
"v_mul_f32 v77, v61, v61\n"
"v_mul_f32 v78, v62, v62\n"
"v_mfma_f32_16x16x32_bf16 v[68:71], a[16:19], v[36:39], v[68:71]\n"
"ds_read_b128 v[32:35], v116 offset:57344\n"
"buffer_load_dwordx4 v124, s[80:83], s95 offen offset:2048 lds\n"
"v_fmamk_f32 v76, v76, 0xbdd2d3e2, v112\n"
"v_mfma_f32_16x16x32_bf16 v[72:75], a[20:23], v[36:39], v[72:75]\n"
"v_mul_f32 v76, v76, v60\n"
"v_fmamk_f32 v77, v77, 0xbdd2d3e2, v112\n"
"v_fmamk_f32 v78, v78, 0xbdd2d3e2, v112\n"
"v_mfma_f32_16x16x32_bf16 v[68:71], a[24:27], v[40:43], v[68:71]\n"
"ds_read_b128 v[36:39], v114 offset:59392\n"
"buffer_load_dwordx4 v125, s[80:83], s95 offen offset:3072 lds\n"
"v_mul_f32 v77, v77, v61\n"
"v_mfma_f32_16x16x32_bf16 v[72:75], a[28:31], v[40:43], v[72:75]\n"
"s_waitcnt lgkmcnt(5)\n"
"v_exp_f32 v76, v76\n"
"v_mul_f32 v78, v78, v62\n"
"v_mfma_f32_16x16x32_bf16 v[68:71], a[32:35], v[44:47], v[68:71]\n"
"ds_read_b128 v[40:43], v116 offset:59392\n"
"v_exp_f32 v77, v77\n"
"v_mfma_f32_16x16x32_bf16 v[72:75], a[36:39], v[44:47], v[72:75]\n"
"v_exp_f32 v78, v78\n"
"v_add_f32 v76, 1.0, v76\n"
"v_mfma_f32_16x16x32_bf16 v[68:71], a[40:43], v[48:51], v[68:71]\n"
"ds_read_b128 v[44:47], v114 offset:61440\n"
"v_rcp_f32 v76, v76\n"
"v_mfma_f32_16x16x32_bf16 v[72:75], a[44:47], v[48:51], v[72:75]\n"
"s_waitcnt lgkmcnt(5)\n"
"v_add_f32 v77, 1.0, v77\n"
"v_add_f32 v78, 1.0, v78\n"
"v_mul_f32 v79, v63, v63\n"
"v_mfma_f32_16x16x32_bf16 v[68:71], a[48:51], v[52:55], v[68:71]\n"
"ds_read_b128 v[48:51], v116 offset:61440\n"
"v_rcp_f32 v77, v77\n"
"v_mfma_f32_16x16x32_bf16 v[72:75], a[52:55], v[52:55], v[72:75]\n"
"s_mov_b32 s93, 32768\n"
"v_rcp_f32 v78, v78\n"
"v_fmamk_f32 v79, v79, 0xbdd2d3e2, v112\n"
"v_mfma_f32_16x16x32_bf16 v[68:71], a[56:59], v[56:59], v[68:71]\n"
"ds_read_b128 v[52:55], v114 offset:63488\n"
"v_mul_f32 v79, v79, v63\n"
"v_mfma_f32_16x16x32_bf16 v[72:75], a[60:63], v[56:59], v[72:75]\n"
"s_waitcnt lgkmcnt(5)\n"
"v_exp_f32 v79, v79\n"
"v_mfma_f32_16x16x32_bf16 v[68:71], a[64:67], v[28:31], v[68:71]\n"
"s_waitcnt vmcnt(18)\n"
"ds_read_b128 v[56:59], v116 offset:63488\n"
"v_add_f32 v79, 1.0, v79\n"
"v_mfma_f32_16x16x32_bf16 v[72:75], a[68:71], v[28:31], v[72:75]\n"
"s_barrier\n"
"v_rcp_f32 v79, v79\n"
"v_mfma_f32_16x16x32_bf16 v[68:71], a[72:75], v[32:35], v[68:71]\n"
"s_add_u32 m0, s44, 32768\n"
"ds_read_b128 v[28:31], v115 offset:0\n"
"buffer_load_dword v21, v20, s[72:75], s93 offen sc1\n"
"v_mfma_f32_16x16x32_bf16 v[72:75], a[76:79], v[32:35], v[72:75]\n"
"s_waitcnt lgkmcnt(5)\n"
"ds_read_b64_tr_b16 v[224:225], v120 offset:32768\n"
"v_mul_f32 v80, v64, v64\n"
"v_mfma_f32_16x16x32_bf16 v[68:71], a[80:83], v[36:39], v[68:71]\n"
"ds_read_b128 v[32:35], v117 offset:0\n"
"buffer_load_dwordx4 v113, s[64:67], s40 offen offset:0 sc1 lds\n"
"v_mfma_f32_16x16x32_bf16 v[72:75], a[84:87], v[36:39], v[72:75]\n"
"v_fmamk_f32 v80, v80, 0xbdd2d3e2, v112\n"
"v_mul_f32 v81, v65, v65\n"
"v_mfma_f32_16x16x32_bf16 v[68:71], a[88:91], v[40:43], v[68:71]\n"
"ds_read_b128 v[36:39], v115 offset:2048\n"
"ds_read_b64_tr_b16 v[226:227], v120 offset:33792\n"
"v_mfma_f32_16x16x32_bf16 v[72:75], a[92:95], v[40:43], v[72:75]\n"
"s_waitcnt lgkmcnt(7)\n"
"v_mul_f32 v80, v80, v64\n"
"v_fmamk_f32 v81, v81, 0xbdd2d3e2, v112\n"
"v_mfma_f32_16x16x32_bf16 v[68:71], a[96:99], v[44:47], v[68:71]\n"
"ds_read_b128 v[40:43], v117 offset:2048\n"
"ds_read_b64_tr_b16 v[228:229], v120 offset:32776\n"
"v_mfma_f32_16x16x32_bf16 v[72:75], a[100:103], v[44:47], v[72:75]\n"
"v_exp_f32 v80, v80\n"
"v_mfma_f32_16x16x32_bf16 v[68:71], a[104:107], v[48:51], v[68:71]\n"
"ds_read_b128 v[44:47], v115 offset:4096\n"
"buffer_load_dwordx4 v113, s[64:67], s40 offen offset:1024 sc1 lds\n"
"v_mfma_f32_16x16x32_bf16 v[72:75], a[108:111], v[48:51], v[72:75]\n"
"s_waitcnt lgkmcnt(8)\n"
"ds_read_b64_tr_b16 v[230:231], v120 offset:33800\n"
"v_add_f32 v80, 1.0, v80\n"
"v_mfma_f32_16x16x32_bf16 v[68:71], a[112:115], v[52:55], v[68:71]\n"
"ds_read_b128 v[48:51], v117 offset:4096\n"
"v_mul_f32 v81, v81, v65\n"
"v_mfma_f32_16x16x32_bf16 v[72:75], a[116:119], v[52:55], v[72:75]\n"
"v_rcp_f32 v80, v80\n"
"v_mfma_f32_16x16x32_bf16 v[68:71], a[120:123], v[56:59], v[68:71]\n"
"ds_read_b128 v[52:55], v115 offset:6144\n"
"ds_read_b64_tr_b16 v[232:233], v120 offset:40960\n"
"v_mfma_f32_16x16x32_bf16 v[72:75], a[124:127], v[56:59], v[72:75]\n"
"s_waitcnt lgkmcnt(9)\n"
"v_exp_f32 v81, v81\n"
"v_mfma_f32_16x16x32_bf16 v[68:71], a[128:131], v[28:31], v[68:71]\n"
"ds_read_b128 v[56:59], v117 offset:6144\n"
"buffer_load_dwordx4 v113, s[64:67], s40 offen offset:2048 sc1 lds\n"
"v_mfma_f32_16x16x32_bf16 v[72:75], a[132:135], v[28:31], v[72:75]\n"
"ds_read_b64_tr_b16 v[234:235], v120 offset:41984\n"
"v_add_f32 v81, 1.0, v81\n"
"v_mfma_f32_16x16x32_bf16 v[68:71], a[136:139], v[32:35], v[68:71]\n"
"ds_read_b128 v[28:31], v115 offset:8192\n"
"v_mul_f32 v82, v66, v66\n"
"v_mfma_f32_16x16x32_bf16 v[72:75], a[140:143], v[32:35], v[72:75]\n"
"s_waitcnt lgkmcnt(9)\n"
"v_rcp_f32 v81, v81\n"
"v_mfma_f32_16x16x32_bf16 v[68:71], a[144:147], v[36:39], v[68:71]\n"
"ds_read_b128 v[32:35], v117 offset:8192\n"
"ds_read_b64_tr_b16 v[236:237], v120 offset:40968\n"
"v_mfma_f32_16x16x32_bf16 v[72:75], a[148:151], v[36:39], v[72:75]\n"
"v_fmamk_f32 v82, v82, 0xbdd2d3e2, v112\n"
"v_mul_f32 v83, v67, v67\n"
"v_mfma_f32_16x16x32_bf16 v[68:71], a[152:155], v[40:43], v[68:71]\n"
"ds_read_b128 v[36:39], v115 offset:10240\n"
"buffer_load_dwordx4 v113, s[64:67], s40 offen offset:3072 sc1 lds\n"
"v_mfma_f32_16x16x32_bf16 v[72:75], a[156:159], v[40:43], v[72:75]\n"
"s_waitcnt lgkmcnt(8)\n"
"ds_read_b64_tr_b16 v[238:239], v120 offset:41992\n"
"v_mul_f32 v82, v82, v66\n"
"v_mfma_f32_16x16x32_bf16 v[68:71], a[160:163], v[44:47], v[68:71]\n"
"s_add_u32 s40, s40, 16384\n"
"ds_read_b128 v[40:43], v117 offset:10240\n"
"v_fmamk_f32 v83, v83, 0xbdd2d3e2, v112\n"
"v_mfma_f32_16x16x32_bf16 v[72:75], a[164:167], v[44:47], v[72:75]\n"
"v_exp_f32 v82, v82\n"
"v_mfma_f32_16x16x32_bf16 v[68:71], a[168:171], v[48:51], v[68:71]\n"
"ds_read_b128 v[44:47], v115 offset:12288\n"
"v_add_f32 v82, 1.0, v82\n"
"v_mfma_f32_16x16x32_bf16 v[72:75], a[172:175], v[48:51], v[72:75]\n"
"s_waitcnt lgkmcnt(8)\n"
"v_rcp_f32 v82, v82\n"
"v_mfma_f32_16x16x32_bf16 v[68:71], a[176:179], v[52:55], v[68:71]\n"
"ds_read_b128 v[48:51], v117 offset:12288\n"
"v_mul_f32 v83, v83, v67\n"
"v_mfma_f32_16x16x32_bf16 v[72:75], a[180:183], v[52:55], v[72:75]\n"
"v_exp_f32 v83, v83\n"
"v_mfma_f32_16x16x32_bf16 v[68:71], a[184:187], v[56:59], v[68:71]\n"
"ds_read_b128 v[52:55], v115 offset:14336\n"
"v_add_f32 v83, 1.0, v83\n"
"v_mfma_f32_16x16x32_bf16 v[72:75], a[188:191], v[56:59], v[72:75]\n"
"s_waitcnt lgkmcnt(7)\n"
"v_rcp_f32 v83, v83\n"
"v_mfma_f32_16x16x32_bf16 v[68:71], a[192:195], v[28:31], v[68:71]\n"
"s_waitcnt vmcnt(14)\n"
"ds_read_b128 v[56:59], v117 offset:14336\n"
"v_mul_f32 v76, v60, v76\n"
"v_mfma_f32_16x16x32_bf16 v[72:75], a[196:199], v[28:31], v[72:75]\n"
"s_barrier\n"
"v_mul_f32 v77, v61, v77\n"
"v_mul_f32 v78, v62, v78\n"
"v_mfma_f32_16x16x32_bf16 v[68:71], a[200:203], v[32:35], v[68:71]\n"
"s_add_u32 m0, s44, 49152\n"
"ds_read_b128 v[28:31], v114 offset:0\n"
"ds_read_b64_tr_b16 v[240:241], v120 offset:49152\n"
"v_mfma_f32_16x16x32_bf16 v[72:75], a[204:207], v[32:35], v[72:75]\n"
"s_waitcnt lgkmcnt(6)\n"
"v_mul_f32 v79, v63, v79\n"
"v_mul_f32 v80, v64, v80\n"
"v_mfma_f32_16x16x32_bf16 v[68:71], a[208:211], v[36:39], v[68:71]\n"
"ds_read_b128 v[32:35], v116 offset:0\n"
"buffer_load_dwordx4 v113, s[64:67], s40 offen offset:0 sc1 lds\n"
"v_mfma_f32_16x16x32_bf16 v[72:75], a[212:215], v[36:39], v[72:75]\n"
"ds_read_b64_tr_b16 v[242:243], v120 offset:50176\n"
"v_mul_f32 v81, v65, v81\n"
"v_mfma_f32_16x16x32_bf16 v[68:71], a[216:219], v[40:43], v[68:71]\n"
"ds_read_b128 v[36:39], v114 offset:2048\n"
"buffer_load_dwordx4 v113, s[64:67], s40 offen offset:1024 sc1 lds\n"
"v_mfma_f32_16x16x32_bf16 v[72:75], a[220:223], v[40:43], v[72:75]\n"
"s_waitcnt lgkmcnt(7)\n"
"ds_read_b64_tr_b16 v[244:245], v120 offset:49160\n"
"v_mul_f32 v82, v66, v82\n"
"v_mfma_f32_16x16x32_bf16 v[68:71], a[224:227], v[44:47], v[68:71]\n"
"ds_read_b128 v[40:43], v116 offset:2048\n"
"buffer_load_dwordx4 v113, s[64:67], s40 offen offset:2048 sc1 lds\n"
"v_mfma_f32_16x16x32_bf16 v[72:75], a[228:231], v[44:47], v[72:75]\n"
"ds_read_b64_tr_b16 v[246:247], v120 offset:50184\n"
"v_mul_f32 v83, v67, v83\n"
"v_mfma_f32_16x16x32_bf16 v[68:71], a[232:235], v[48:51], v[68:71]\n"
"ds_read_b128 v[44:47], v114 offset:4096\n"
"buffer_load_dwordx4 v113, s[64:67], s40 offen offset:3072 sc1 lds\n"
"v_mfma_f32_16x16x32_bf16 v[72:75], a[236:239], v[48:51], v[72:75]\n"
"s_waitcnt lgkmcnt(9)\n"
"v_cvt_pk_bf16_f32 v84, v76, v77\n"
"v_cvt_pk_bf16_f32 v85, v78, v79\n"
"v_mfma_f32_16x16x32_bf16 v[68:71], a[240:243], v[52:55], v[68:71]\n"
"s_add_u32 s40, s40, 278528\n"
"ds_read_b128 v[48:51], v116 offset:4096\n"
"ds_read_b64_tr_b16 v[248:249], v120 offset:57344\n"
"v_mfma_f32_16x16x32_bf16 v[72:75], a[244:247], v[52:55], v[72:75]\n"
"s_add_u32 s95, s43, 1048576\n"
"v_cvt_pk_bf16_f32 v86, v80, v81\n"
"v_cvt_pk_bf16_f32 v87, v82, v83\n"
"v_mfma_f32_16x16x32_bf16 v[68:71], a[248:251], v[56:59], v[68:71]\n"
"s_add_u32 m0, s44, 147456\n"
"ds_read_b128 v[52:55], v114 offset:6144\n"
"ds_read_b64_tr_b16 v[250:251], v120 offset:58368\n"
"v_mfma_f32_16x16x32_bf16 v[72:75], a[252:255], v[56:59], v[72:75]\n"
"s_waitcnt lgkmcnt(10)\n"
"v_mfma_f32_16x16x32_bf16 v[60:63], a[0:3], v[28:31], v[88:91]\n"
"ds_read_b128 v[56:59], v116 offset:6144\n"
"buffer_load_dwordx4 v122, s[80:83], s95 offen offset:0 lds\n"
"v_mfma_f32_16x16x32_bf16 v[64:67], a[4:7], v[28:31], v[92:95]\n"
"buffer_store_dwordx4 v[84:87], v113, s[68:71], s41 offen sc1\n"
"ds_read_b64_tr_b16 v[252:253], v120 offset:57352\n"
"v_mfma_f32_16x16x32_bf16 v[60:63], a[8:11], v[32:35], v[60:63]\n"
"s_add_u32 s41, s41, 1032192\n"
"ds_read_b128 v[28:31], v114 offset:8192\n"
"buffer_load_dwordx4 v123, s[80:83], s95 offen offset:1024 lds\n"
"v_mfma_f32_16x16x32_bf16 v[64:67], a[12:15], v[32:35], v[64:67]\n"
"s_waitcnt lgkmcnt(9)\n"
"ds_read_b64_tr_b16 v[254:255], v120 offset:58376\n"
"v_mul_f32 v76, v68, v68\n"
"v_mul_f32 v77, v69, v69\n"
"v_mfma_f32_16x16x32_bf16 v[60:63], a[16:19], v[36:39], v[60:63]\n"
"ds_read_b128 v[32:35], v116 offset:8192\n"
"buffer_load_dwordx4 v124, s[80:83], s95 offen offset:2048 lds\n"
"v_fmamk_f32 v76, v76, 0xbdd2d3e2, v112\n"
"v_mfma_f32_16x16x32_bf16 v[64:67], a[20:23], v[36:39], v[64:67]\n"
"v_mul_f32 v76, v76, v68\n"
"v_fmamk_f32 v77, v77, 0xbdd2d3e2, v112\n"
"v_mul_f32 v78, v70, v70\n"
"v_mfma_f32_16x16x32_bf16 v[60:63], a[24:27], v[40:43], v[60:63]\n"
"ds_read_b128 v[36:39], v114 offset:10240\n"
"buffer_load_dwordx4 v125, s[80:83], s95 offen offset:3072 lds\n"
"v_mul_f32 v77, v77, v69\n"
"v_mfma_f32_16x16x32_bf16 v[64:67], a[28:31], v[40:43], v[64:67]\n"
"s_waitcnt lgkmcnt(9)\n"
"v_exp_f32 v76, v76\n"
"v_fmamk_f32 v78, v78, 0xbdd2d3e2, v112\n"
"v_mfma_f32_16x16x32_bf16 v[60:63], a[32:35], v[44:47], v[60:63]\n"
"ds_read_b128 v[40:43], v116 offset:10240\n"
"v_add_f32 v76, 1.0, v76\n"
"v_mfma_f32_16x16x32_bf16 v[64:67], a[36:39], v[44:47], v[64:67]\n"
"v_rcp_f32 v76, v76\n"
"v_mfma_f32_16x16x32_bf16 v[60:63], a[40:43], v[48:51], v[60:63]\n"
"ds_read_b128 v[44:47], v114 offset:12288\n"
"v_mul_f32 v78, v78, v70\n"
"v_mfma_f32_16x16x32_bf16 v[64:67], a[44:47], v[48:51], v[64:67]\n"
"s_waitcnt lgkmcnt(7)\n"
"v_exp_f32 v77, v77\n"
"v_mfma_f32_16x16x32_bf16 v[60:63], a[48:51], v[52:55], v[60:63]\n"
"ds_read_b128 v[48:51], v116 offset:12288\n"
"v_add_f32 v77, 1.0, v77\n"
"v_mfma_f32_16x16x32_bf16 v[64:67], a[52:55], v[52:55], v[64:67]\n"
"v_rcp_f32 v77, v77\n"
"v_mfma_f32_16x16x32_bf16 v[60:63], a[56:59], v[56:59], v[60:63]\n"
"ds_read_b128 v[52:55], v114 offset:14336\n"
"v_mul_f32 v79, v71, v71\n"
"v_mfma_f32_16x16x32_bf16 v[64:67], a[60:63], v[56:59], v[64:67]\n"
"s_waitcnt lgkmcnt(5)\n"
"v_exp_f32 v78, v78\n"
"v_mfma_f32_16x16x32_bf16 v[60:63], a[64:67], v[28:31], v[60:63]\n"
"s_waitcnt vmcnt(19)\n"
"ds_read_b128 v[56:59], v116 offset:14336\n"
"v_add_f32 v78, 1.0, v78\n"
"v_mfma_f32_16x16x32_bf16 v[64:67], a[68:71], v[28:31], v[64:67]\n"
"s_barrier\n"
"v_rcp_f32 v78, v78\n"
"v_mfma_f32_16x16x32_bf16 v[60:63], a[72:75], v[32:35], v[60:63]\n"
"s_add_u32 m0, s44, 65536\n"
"ds_read_b128 v[28:31], v114 offset:16384\n"
"v_fmamk_f32 v79, v79, 0xbdd2d3e2, v112\n"
"v_mfma_f32_16x16x32_bf16 v[64:67], a[76:79], v[32:35], v[64:67]\n"
"s_waitcnt lgkmcnt(5)\n"
"v_mul_f32 v79, v79, v71\n"
"v_mul_f32 v80, v72, v72\n"
"v_mfma_f32_16x16x32_bf16 v[60:63], a[80:83], v[36:39], v[60:63]\n"
"ds_read_b128 v[32:35], v116 offset:16384\n"
"buffer_load_dwordx4 v113, s[64:67], s40 offen offset:0 sc1 lds\n"
"v_mfma_f32_16x16x32_bf16 v[64:67], a[84:87], v[36:39], v[64:67]\n"
"v_exp_f32 v79, v79\n"
"v_mfma_f32_16x16x32_bf16 v[60:63], a[88:91], v[40:43], v[60:63]\n"
"ds_read_b128 v[36:39], v114 offset:18432\n"
"v_add_f32 v79, 1.0, v79\n"
"v_mfma_f32_16x16x32_bf16 v[64:67], a[92:95], v[40:43], v[64:67]\n"
"s_waitcnt lgkmcnt(5)\n"
"v_rcp_f32 v79, v79\n"
"v_mfma_f32_16x16x32_bf16 v[60:63], a[96:99], v[44:47], v[60:63]\n"
"ds_read_b128 v[40:43], v116 offset:18432\n"
"v_fmamk_f32 v80, v80, 0xbdd2d3e2, v112\n"
"v_mfma_f32_16x16x32_bf16 v[64:67], a[100:103], v[44:47], v[64:67]\n"
"v_mul_f32 v80, v80, v72\n"
"v_mul_f32 v81, v73, v73\n"
"v_mfma_f32_16x16x32_bf16 v[60:63], a[104:107], v[48:51], v[60:63]\n"
"ds_read_b128 v[44:47], v114 offset:20480\n"
"buffer_load_dwordx4 v113, s[64:67], s40 offen offset:1024 sc1 lds\n"
"v_mfma_f32_16x16x32_bf16 v[64:67], a[108:111], v[48:51], v[64:67]\n"
"s_waitcnt lgkmcnt(5)\n"
"v_exp_f32 v80, v80\n"
"v_mfma_f32_16x16x32_bf16 v[60:63], a[112:115], v[52:55], v[60:63]\n"
"ds_read_b128 v[48:51], v116 offset:20480\n"
"v_add_f32 v80, 1.0, v80\n"
"v_mfma_f32_16x16x32_bf16 v[64:67], a[116:119], v[52:55], v[64:67]\n"
"v_rcp_f32 v80, v80\n"
"v_mfma_f32_16x16x32_bf16 v[60:63], a[120:123], v[56:59], v[60:63]\n"
"ds_read_b128 v[52:55], v114 offset:22528\n"
"v_fmamk_f32 v81, v81, 0xbdd2d3e2, v112\n"
"v_mfma_f32_16x16x32_bf16 v[64:67], a[124:127], v[56:59], v[64:67]\n"
"s_waitcnt lgkmcnt(5)\n"
"v_mul_f32 v81, v81, v73\n"
"v_mul_f32 v82, v74, v74\n"
"v_mfma_f32_16x16x32_bf16 v[60:63], a[128:131], v[28:31], v[60:63]\n"
"ds_read_b128 v[56:59], v116 offset:22528\n"
"buffer_load_dwordx4 v113, s[64:67], s40 offen offset:2048 sc1 lds\n"
"v_mfma_f32_16x16x32_bf16 v[64:67], a[132:135], v[28:31], v[64:67]\n"
"v_exp_f32 v81, v81\n"
"v_mfma_f32_16x16x32_bf16 v[60:63], a[136:139], v[32:35], v[60:63]\n"
"ds_read_b128 v[28:31], v114 offset:24576\n"
"v_add_f32 v81, 1.0, v81\n"
"v_mfma_f32_16x16x32_bf16 v[64:67], a[140:143], v[32:35], v[64:67]\n"
"s_waitcnt lgkmcnt(5)\n"
"v_rcp_f32 v81, v81\n"
"v_mfma_f32_16x16x32_bf16 v[60:63], a[144:147], v[36:39], v[60:63]\n"
"ds_read_b128 v[32:35], v116 offset:24576\n"
"v_fmamk_f32 v82, v82, 0xbdd2d3e2, v112\n"
"v_mfma_f32_16x16x32_bf16 v[64:67], a[148:151], v[36:39], v[64:67]\n"
"v_mul_f32 v82, v82, v74\n"
"v_mul_f32 v83, v75, v75\n"
"v_mfma_f32_16x16x32_bf16 v[60:63], a[152:155], v[40:43], v[60:63]\n"
"ds_read_b128 v[36:39], v114 offset:26624\n"
"buffer_load_dwordx4 v113, s[64:67], s40 offen offset:3072 sc1 lds\n"
"v_mfma_f32_16x16x32_bf16 v[64:67], a[156:159], v[40:43], v[64:67]\n"
"s_waitcnt lgkmcnt(5)\n"
"v_exp_f32 v82, v82\n"
"v_mfma_f32_16x16x32_bf16 v[60:63], a[160:163], v[44:47], v[60:63]\n"
"s_add_u32 s40, s40, 16384\n"
"ds_read_b128 v[40:43], v116 offset:26624\n"
"v_add_f32 v82, 1.0, v82\n"
"v_mfma_f32_16x16x32_bf16 v[64:67], a[164:167], v[44:47], v[64:67]\n"
"v_rcp_f32 v82, v82\n"
"v_mfma_f32_16x16x32_bf16 v[60:63], a[168:171], v[48:51], v[60:63]\n"
"ds_read_b128 v[44:47], v114 offset:28672\n"
"v_fmamk_f32 v83, v83, 0xbdd2d3e2, v112\n"
"v_mfma_f32_16x16x32_bf16 v[64:67], a[172:175], v[48:51], v[64:67]\n"
"s_waitcnt lgkmcnt(5)\n"
"v_mul_f32 v83, v83, v75\n"
"v_mul_f32 v76, v68, v76\n"
"v_mfma_f32_16x16x32_bf16 v[60:63], a[176:179], v[52:55], v[60:63]\n"
"ds_read_b128 v[48:51], v116 offset:28672\n"
"v_mul_f32 v77, v69, v77\n"
"v_mfma_f32_16x16x32_bf16 v[64:67], a[180:183], v[52:55], v[64:67]\n"
"v_exp_f32 v83, v83\n"
"v_mfma_f32_16x16x32_bf16 v[60:63], a[184:187], v[56:59], v[60:63]\n"
"ds_read_b128 v[52:55], v114 offset:30720\n"
"v_add_f32 v83, 1.0, v83\n"
"v_mfma_f32_16x16x32_bf16 v[64:67], a[188:191], v[56:59], v[64:67]\n"
"s_waitcnt lgkmcnt(5)\n"
"v_rcp_f32 v83, v83\n"
"v_mfma_f32_16x16x32_bf16 v[60:63], a[192:195], v[28:31], v[60:63]\n"
"s_waitcnt vmcnt(13)\n"
"ds_read_b128 v[56:59], v116 offset:30720\n"
"v_mul_f32 v78, v70, v78\n"
"v_mfma_f32_16x16x32_bf16 v[64:67], a[196:199], v[28:31], v[64:67]\n"
"s_barrier\n"
"v_mul_f32 v79, v71, v79\n"
"v_mul_f32 v80, v72, v80\n"
"v_mfma_f32_16x16x32_bf16 v[60:63], a[200:203], v[32:35], v[60:63]\n"
"s_add_u32 m0, s44, 0\n"
"ds_read_b128 v[28:31], v114 offset:32768\n"
"v_cmp_ne_u32 vcc, s50, v21\n"
"v_mfma_f32_16x16x32_bf16 v[64:67], a[204:207], v[32:35], v[64:67]\n"
"s_waitcnt lgkmcnt(5)\n"
"buffer_atomic_add v1, v0, s[72:75], 0 offen offset:256\n"
"ds_read_b64_tr_b16 v[4:5], v121 offset:0\n"
"v_mfma_f32_16x16x32_bf16 v[60:63], a[208:211], v[36:39], v[60:63]\n"
"s_cbranch_vccnz Lslow_x1_K\n"
"Lret_x1_K:\n"
"ds_read_b128 v[32:35], v116 offset:32768\n"
"buffer_load_dwordx4 v113, s[64:67], s40 offen offset:0 sc1 lds\n"
"v_mfma_f32_16x16x32_bf16 v[64:67], a[212:215], v[36:39], v[64:67]\n"
"ds_read_b64_tr_b16 v[6:7], v121 offset:1024\n"
"v_mul_f32 v81, v73, v81\n"
"v_mfma_f32_16x16x32_bf16 v[60:63], a[216:219], v[40:43], v[60:63]\n"
"ds_read_b128 v[36:39], v114 offset:34816\n"
"buffer_load_dwordx4 v113, s[64:67], s40 offen offset:1024 sc1 lds\n"
"v_mfma_f32_16x16x32_bf16 v[64:67], a[220:223], v[40:43], v[64:67]\n"
"s_waitcnt lgkmcnt(7)\n"
"ds_read_b64_tr_b16 v[8:9], v121 offset:8\n"
"v_mul_f32 v82, v74, v82\n"
"v_mfma_f32_16x16x32_bf16 v[60:63], a[224:227], v[44:47], v[60:63]\n"
"ds_read_b128 v[40:43], v116 offset:34816\n"
"buffer_load_dwordx4 v113, s[64:67], s40 offen offset:2048 sc1 lds\n"
"v_mfma_f32_16x16x32_bf16 v[64:67], a[228:231], v[44:47], v[64:67]\n"
"ds_read_b64_tr_b16 v[10:11], v121 offset:1032\n"
"v_mul_f32 v83, v75, v83\n"
"v_mfma_f32_16x16x32_bf16 v[60:63], a[232:235], v[48:51], v[60:63]\n"
"ds_read_b128 v[44:47], v114 offset:36864\n"
"buffer_load_dwordx4 v113, s[64:67], s40 offen offset:3072 sc1 lds\n"
"v_mfma_f32_16x16x32_bf16 v[64:67], a[236:239], v[48:51], v[64:67]\n"
"s_waitcnt lgkmcnt(9)\n"
"v_cvt_pk_bf16_f32 v84, v76, v77\n"
"v_cvt_pk_bf16_f32 v85, v78, v79\n"
"v_mfma_f32_16x16x32_bf16 v[60:63], a[240:243], v[52:55], v[60:63]\n"
"s_add_u32 s40, s40, 278528\n"
"ds_read_b128 v[48:51], v116 offset:36864\n"
"ds_read_b64_tr_b16 v[12:13], v121 offset:8192\n"
"v_mfma_f32_16x16x32_bf16 v[64:67], a[244:247], v[52:55], v[64:67]\n"
"s_add_u32 s95, s43, 1179648\n"
"v_cvt_pk_bf16_f32 v86, v80, v81\n"
"v_cvt_pk_bf16_f32 v87, v82, v83\n"
"v_mfma_f32_16x16x32_bf16 v[60:63], a[248:251], v[56:59], v[60:63]\n"
"s_add_u32 m0, s44, 81920\n"
"ds_read_b128 v[52:55], v114 offset:38912\n"
"ds_read_b64_tr_b16 v[14:15], v121 offset:9216\n"
"v_mfma_f32_16x16x32_bf16 v[64:67], a[252:255], v[56:59], v[64:67]\n"
"s_waitcnt lgkmcnt(10)\n"
"v_mfma_f32_16x16x32_bf16 v[68:71], a[0:3], v[28:31], v[88:91]\n"
"ds_read_b128 v[56:59], v116 offset:38912\n"
"buffer_load_dwordx4 v122, s[80:83], s95 offen offset:0 lds\n"
"v_mfma_f32_16x16x32_bf16 v[72:75], a[4:7], v[28:31], v[92:95]\n"
"buffer_store_dwordx4 v[84:87], v113, s[68:71], s41 offen sc1\n"
"ds_read_b64_tr_b16 v[16:17], v121 offset:8200\n"
"v_mfma_f32_16x16x32_bf16 v[68:71], a[8:11], v[32:35], v[68:71]\n"
"s_add_u32 s41, s41, 1032192\n"
"ds_read_b128 v[28:31], v114 offset:40960\n"
"buffer_load_dwordx4 v123, s[80:83], s95 offen offset:1024 lds\n"
"v_mfma_f32_16x16x32_bf16 v[72:75], a[12:15], v[32:35], v[72:75]\n"
"s_waitcnt lgkmcnt(9)\n"
"ds_read_b64_tr_b16 v[18:19], v121 offset:9224\n"
"v_mul_f32 v76, v60, v60\n"
"v_mfma_f32_16x16x32_bf16 v[68:71], a[16:19], v[36:39], v[68:71]\n"
"ds_read_b128 v[32:35], v116 offset:40960\n"
"buffer_load_dwordx4 v124, s[80:83], s95 offen offset:2048 lds\n"
"v_mfma_f32_16x16x32_bf16 v[72:75], a[20:23], v[36:39], v[72:75]\n"
"v_fmamk_f32 v76, v76, 0xbdd2d3e2, v112\n"
"v_mul_f32 v77, v61, v61\n"
"v_mfma_f32_16x16x32_bf16 v[68:71], a[24:27], v[40:43], v[68:71]\n"
"ds_read_b128 v[36:39], v114 offset:43008\n"
"buffer_load_dwordx4 v125, s[80:83], s95 offen offset:3072 lds\n"
"v_mfma_f32_16x16x32_bf16 v[72:75], a[28:31], v[40:43], v[72:75]\n"
"s_waitcnt lgkmcnt(9)\n"
"v_mul_f32 v76, v76, v60\n"
"v_fmamk_f32 v77, v77, 0xbdd2d3e2, v112\n"
"v_mfma_f32_16x16x32_bf16 v[68:71], a[32:35], v[44:47], v[68:71]\n"
"ds_read_b128 v[40:43], v116 offset:43008\n"
"v_mul_f32 v77, v77, v61\n"
"v_mfma_f32_16x16x32_bf16 v[72:75], a[36:39], v[44:47], v[72:75]\n"
"v_exp_f32 v76, v76\n"
"v_mfma_f32_16x16x32_bf16 v[68:71], a[40:43], v[48:51], v[68:71]\n"
"ds_read_b128 v[44:47], v114 offset:45056\n"
"v_add_f32 v76, 1.0, v76\n"
"v_mfma_f32_16x16x32_bf16 v[72:75], a[44:47], v[48:51], v[72:75]\n"
"s_waitcnt lgkmcnt(7)\n"
"v_rcp_f32 v76, v76\n"
"v_mfma_f32_16x16x32_bf16 v[68:71], a[48:51], v[52:55], v[68:71]\n"
"ds_read_b128 v[48:51], v116 offset:45056\n"
"v_mul_f32 v78, v62, v62\n"
"v_mfma_f32_16x16x32_bf16 v[72:75], a[52:55], v[52:55], v[72:75]\n"
"v_exp_f32 v77, v77\n"
"v_mfma_f32_16x16x32_bf16 v[68:71], a[56:59], v[56:59], v[68:71]\n"
"ds_read_b128 v[52:55], v114 offset:47104\n"
"v_add_f32 v77, 1.0, v77\n"
"v_mfma_f32_16x16x32_bf16 v[72:75], a[60:63], v[56:59], v[72:75]\n"
"s_waitcnt lgkmcnt(5)\n"
"v_rcp_f32 v77, v77\n"
"v_mfma_f32_16x16x32_bf16 v[68:71], a[64:67], v[28:31], v[68:71]\n"
"s_waitcnt vmcnt(19)\n"
"ds_read_b128 v[56:59], v116 offset:47104\n"
"v_fmamk_f32 v78, v78, 0xbdd2d3e2, v112\n"
"v_mfma_f32_16x16x32_bf16 v[72:75], a[68:71], v[28:31], v[72:75]\n"
"s_barrier\n"
"v_mul_f32 v78, v78, v62\n"
"v_mul_f32 v79, v63, v63\n"
"v_mfma_f32_16x16x32_bf16 v[68:71], a[72:75], v[32:35], v[68:71]\n"
"s_add_u32 m0, s44, 16384\n"
"ds_read_b128 v[28:31], v114 offset:49152\n"
"buffer_load_dword v2, v0, s[72:75], 0 offen offset:0 sc1\n"
"v_mfma_f32_16x16x32_bf16 v[72:75], a[76:79], v[32:35], v[72:75]\n"
"s_waitcnt lgkmcnt(5)\n"
"v_exp_f32 v78, v78\n"
"v_mfma_f32_16x16x32_bf16 v[68:71], a[80:83], v[36:39], v[68:71]\n"
"ds_read_b128 v[32:35], v116 offset:49152\n"
"buffer_load_dwordx4 v113, s[64:67], s40 offen offset:0 sc1 lds\n"
"v_mfma_f32_16x16x32_bf16 v[72:75], a[84:87], v[36:39], v[72:75]\n"
"v_add_f32 v78, 1.0, v78\n"
"v_fmamk_f32 v79, v79, 0xbdd2d3e2, v112\n"
"v_mfma_f32_16x16x32_bf16 v[68:71], a[88:91], v[40:43], v[68:71]\n"
"ds_read_b128 v[36:39], v114 offset:51200\n"
"buffer_load_dwordx4 v113, s[64:67], s40 offen offset:1024 sc1 lds\n"
"v_mfma_f32_16x16x32_bf16 v[72:75], a[92:95], v[40:43], v[72:75]\n"
"s_waitcnt lgkmcnt(5)\n"
"v_rcp_f32 v78, v78\n"
"v_mfma_f32_16x16x32_bf16 v[68:71], a[96:99], v[44:47], v[68:71]\n"
"ds_read_b128 v[40:43], v116 offset:51200\n"
"buffer_load_dwordx4 v113, s[64:67], s40 offen offset:2048 sc1 lds\n"
"v_mfma_f32_16x16x32_bf16 v[72:75], a[100:103], v[44:47], v[72:75]\n"
"v_mul_f32 v79, v79, v63\n"
"v_mul_f32 v80, v64, v64\n"
"v_mfma_f32_16x16x32_bf16 v[68:71], a[104:107], v[48:51], v[68:71]\n"
"ds_read_b128 v[44:47], v114 offset:53248\n"
"buffer_load_dwordx4 v113, s[64:67], s40 offen offset:3072 sc1 lds\n"
"v_mfma_f32_16x16x32_bf16 v[72:75], a[108:111], v[48:51], v[72:75]\n"
"s_waitcnt lgkmcnt(5)\n"
"v_exp_f32 v79, v79\n"
"v_mfma_f32_16x16x32_bf16 v[68:71], a[112:115], v[52:55], v[68:71]\n"
"s_add_u32 s40, s40, 16384\n"
"ds_read_b128 v[48:51], v116 offset:53248\n"
"v_add_f32 v79, 1.0, v79\n"
"v_mfma_f32_16x16x32_bf16 v[72:75], a[116:119], v[52:55], v[72:75]\n"
"s_add_u32 s95, s43, 1310720\n"
"v_rcp_f32 v79, v79\n"
"v_mfma_f32_16x16x32_bf16 v[68:71], a[120:123], v[56:59], v[68:71]\n"
"s_add_u32 m0, s44, 98304\n"
"ds_read_b128 v[52:55], v114 offset:55296\n"
"v_fmamk_f32 v80, v80, 0xbdd2d3e2, v112\n"
"v_mfma_f32_16x16x32_bf16 v[72:75], a[124:127], v[56:59], v[72:75]\n"
"s_waitcnt lgkmcnt(5)\n"
"v_mul_f32 v80, v80, v64\n"
"v_mul_f32 v81, v65, v65\n"
"v_mfma_f32_16x16x32_bf16 v[68:71], a[128:131], v[28:31], v[68:71]\n"
"ds_read_b128 v[56:59], v116 offset:55296\n"
"buffer_load_dwordx4 v122, s[80:83], s95 offen offset:0 lds\n"
"v_mfma_f32_16x16x32_bf16 v[72:75], a[132:135], v[28:31], v[72:75]\n"
"v_exp_f32 v80, v80\n"
"v_mfma_f32_16x16x32_bf16 v[68:71], a[136:139], v[32:35], v[68:71]\n"
"ds_read_b128 v[28:31], v114 offset:57344\n"
"buffer_load_dwordx4 v123, s[80:83], s95 offen offset:1024 lds\n"
"v_mfma_f32_16x16x32_bf16 v[72:75], a[140:143], v[32:35], v[72:75]\n"
"s_waitcnt lgkmcnt(5)\n"
"v_add_f32 v80, 1.0, v80\n"
"v_fmamk_f32 v81, v81, 0xbdd2d3e2, v112\n"
"v_mfma_f32_16x16x32_bf16 v[68:71], a[144:147], v[36:39], v[68:71]\n"
"ds_read_b128 v[32:35], v116 offset:57344\n"
"buffer_load_dwordx4 v124, s[80:83], s95 offen offset:2048 lds\n"
"v_mfma_f32_16x16x32_bf16 v[72:75], a[148:151], v[36:39], v[72:75]\n"
"v_rcp_f32 v80, v80\n"
"v_mfma_f32_16x16x32_bf16 v[68:71], a[152:155], v[40:43], v[68:71]\n"
"ds_read_b128 v[36:39], v114 offset:59392\n"
"buffer_load_dwordx4 v125, s[80:83], s95 offen offset:3072 lds\n"
"v_mfma_f32_16x16x32_bf16 v[72:75], a[156:159], v[40:43], v[72:75]\n"
"s_waitcnt lgkmcnt(5)\n"
"v_mul_f32 v81, v81, v65\n"
"v_mul_f32 v82, v66, v66\n"
"v_mfma_f32_16x16x32_bf16 v[68:71], a[160:163], v[44:47], v[68:71]\n"
"ds_read_b128 v[40:43], v116 offset:59392\n"
"v_fmamk_f32 v82, v82, 0xbdd2d3e2, v112\n"
"v_mfma_f32_16x16x32_bf16 v[72:75], a[164:167], v[44:47], v[72:75]\n"
"v_exp_f32 v81, v81\n"
"v_mfma_f32_16x16x32_bf16 v[68:71], a[168:171], v[48:51], v[68:71]\n"
"ds_read_b128 v[44:47], v114 offset:61440\n"
"v_add_f32 v81, 1.0, v81\n"
"v_mfma_f32_16x16x32_bf16 v[72:75], a[172:175], v[48:51], v[72:75]\n"
"s_waitcnt lgkmcnt(5)\n"
"v_rcp_f32 v81, v81\n"
"v_mfma_f32_16x16x32_bf16 v[68:71], a[176:179], v[52:55], v[68:71]\n"
"ds_read_b128 v[48:51], v116 offset:61440\n"
"v_mul_f32 v82, v82, v66\n"
"v_mfma_f32_16x16x32_bf16 v[72:75], a[180:183], v[52:55], v[72:75]\n"
"v_exp_f32 v82, v82\n"
"v_mfma_f32_16x16x32_bf16 v[68:71], a[184:187], v[56:59], v[68:71]\n"
"ds_read_b128 v[52:55], v114 offset:63488\n"
"v_add_f32 v82, 1.0, v82\n"
"v_mfma_f32_16x16x32_bf16 v[72:75], a[188:191], v[56:59], v[72:75]\n"
"s_waitcnt lgkmcnt(5)\n"
"v_rcp_f32 v82, v82\n"
"v_mfma_f32_16x16x32_bf16 v[68:71], a[192:195], v[28:31], v[68:71]\n"
"s_waitcnt vmcnt(19)\n"
"ds_read_b128 v[56:59], v116 offset:63488\n"
"v_mul_f32 v83, v67, v67\n"
"v_mfma_f32_16x16x32_bf16 v[72:75], a[196:199], v[28:31], v[72:75]\n"
"s_barrier\n"
"v_fmamk_f32 v83, v83, 0xbdd2d3e2, v112\n"
"v_mul_f32 v76, v60, v76\n"
"v_mfma_f32_16x16x32_bf16 v[68:71], a[200:203], v[32:35], v[68:71]\n"
"s_add_u32 m0, s44, 32768\n"
"ds_read_b128 v[28:31], v115 offset:0\n"
"v_mul_f32 v83, v83, v67\n"
"v_mfma_f32_16x16x32_bf16 v[72:75], a[204:207], v[32:35], v[72:75]\n"
"s_waitcnt lgkmcnt(5)\n"
"v_exp_f32 v83, v83\n"
"v_mfma_f32_16x16x32_bf16 v[68:71], a[208:211], v[36:39], v[68:71]\n"
"ds_read_b128 v[32:35], v117 offset:0\n"
"buffer_load_dwordx4 v113, s[64:67], s40 offen offset:0 sc1 lds\n"
"v_mfma_f32_16x16x32_bf16 v[72:75], a[212:215], v[36:39], v[72:75]\n"
"v_add_f32 v83, 1.0, v83\n"
"v_mul_f32 v77, v61, v77\n"
"v_mfma_f32_16x16x32_bf16 v[68:71], a[216:219], v[40:43], v[68:71]\n"
"ds_read_b128 v[36:39], v115 offset:2048\n"
"v_mul_f32 v78, v62, v78\n"
"v_mfma_f32_16x16x32_bf16 v[72:75], a[220:223], v[40:43], v[72:75]\n"
"s_waitcnt lgkmcnt(5)\n"
"v_rcp_f32 v83, v83\n"
"v_mfma_f32_16x16x32_bf16 v[68:71], a[224:227], v[44:47], v[68:71]\n"
"ds_read_b128 v[40:43], v117 offset:2048\n"
"v_mul_f32 v79, v63, v79\n"
"v_mfma_f32_16x16x32_bf16 v[72:75], a[228:231], v[44:47], v[72:75]\n"
"v_mul_f32 v80, v64, v80\n"
"v_mul_f32 v81, v65, v81\n"
"v_mfma_f32_16x16x32_bf16 v[68:71], a[232:235], v[48:51], v[68:71]\n"
"ds_read_b128 v[44:47], v115 offset:4096\n"
"buffer_load_dwordx4 v113, s[64:67], s40 offen offset:1024 sc1 lds\n"
"v_mfma_f32_16x16x32_bf16 v[72:75], a[236:239], v[48:51], v[72:75]\n"
"s_waitcnt lgkmcnt(5)\n"
"v_mul_f32 v82, v66, v82\n"
"v_mul_f32 v83, v67, v83\n"
"v_mfma_f32_16x16x32_bf16 v[68:71], a[240:243], v[52:55], v[68:71]\n"
"ds_read_b128 v[48:51], v117 offset:4096\n"
"v_cvt_pk_bf16_f32 v84, v76, v77\n"
"v_mfma_f32_16x16x32_bf16 v[72:75], a[244:247], v[52:55], v[72:75]\n"
"v_cvt_pk_bf16_f32 v85, v78, v79\n"
"v_cvt_pk_bf16_f32 v86, v80, v81\n"
"v_mfma_f32_16x16x32_bf16 v[68:71], a[248:251], v[56:59], v[68:71]\n"
"ds_read_b128 v[52:55], v115 offset:6144\n"
"v_cvt_pk_bf16_f32 v87, v82, v83\n"
"v_mfma_f32_16x16x32_bf16 v[72:75], a[252:255], v[56:59], v[72:75]\n"
"s_waitcnt lgkmcnt(5)\n"
"v_mfma_f32_16x16x32_bf16 v[60:63], a[0:3], v[28:31], v[88:91]\n"
"ds_read_b128 v[56:59], v117 offset:6144\n"
"buffer_load_dwordx4 v113, s[64:67], s40 offen offset:2048 sc1 lds\n"
"v_mfma_f32_16x16x32_bf16 v[64:67], a[4:7], v[28:31], v[92:95]\n"
"buffer_store_dwordx4 v[84:87], v113, s[68:71], s41 offen sc1\n"
"v_mfma_f32_16x16x32_bf16 v[60:63], a[8:11], v[32:35], v[60:63]\n"
"s_add_u32 s41, s41, 1032192\n"
"ds_read_b128 v[28:31], v115 offset:8192\n"
"v_mfma_f32_16x16x32_bf16 v[64:67], a[12:15], v[32:35], v[64:67]\n"
"s_waitcnt lgkmcnt(5)\n"
"v_mul_f32 v76, v68, v68\n"
"v_mul_f32 v77, v69, v69\n"
"v_mfma_f32_16x16x32_bf16 v[60:63], a[16:19], v[36:39], v[60:63]\n"
"ds_read_b128 v[32:35], v117 offset:8192\n"
"v_fmamk_f32 v76, v76, 0xbdd2d3e2, v112\n"
"v_mfma_f32_16x16x32_bf16 v[64:67], a[20:23], v[36:39], v[64:67]\n"
"v_mul_f32 v76, v76, v68\n"
"v_fmamk_f32 v77, v77, 0xbdd2d3e2, v112\n"
"v_mfma_f32_16x16x32_bf16 v[60:63], a[24:27], v[40:43], v[60:63]\n"
"ds_read_b128 v[36:39], v115 offset:10240\n"
"buffer_load_dwordx4 v113, s[64:67], s40 offen offset:3072 sc1 lds\n"
"v_mfma_f32_16x16x32_bf16 v[64:67], a[28:31], v[40:43], v[64:67]\n"
"s_waitcnt lgkmcnt(5)\n"
"v_exp_f32 v76, v76\n"
"v_mfma_f32_16x16x32_bf16 v[60:63], a[32:35], v[44:47], v[60:63]\n"
"s_add_u32 s40, s40, 278528\n"
"ds_read_b128 v[40:43], v117 offset:10240\n"
"v_add_f32 v76, 1.0, v76\n"
"v_mfma_f32_16x16x32_bf16 v[64:67], a[36:39], v[44:47], v[64:67]\n"
"v_rcp_f32 v76, v76\n"
"v_mfma_f32_16x16x32_bf16 v[60:63], a[40:43], v[48:51], v[60:63]\n"
"ds_read_b128 v[44:47], v115 offset:12288\n"
"v_mul_f32 v77, v77, v69\n"
"v_mfma_f32_16x16x32_bf16 v[64:67], a[44:47], v[48:51], v[64:67]\n"
"s_waitcnt lgkmcnt(5)\n"
"v_exp_f32 v77, v77\n"
"v_mfma_f32_16x16x32_bf16 v[60:63], a[48:51], v[52:55], v[60:63]\n"
"ds_read_b128 v[48:51], v117 offset:12288\n"
"v_add_f32 v77, 1.0, v77\n"
"v_mfma_f32_16x16x32_bf16 v[64:67], a[52:55], v[52:55], v[64:67]\n"
"v_rcp_f32 v77, v77\n"
"v_mfma_f32_16x16x32_bf16 v[60:63], a[56:59], v[56:59], v[60:63]\n"
"ds_read_b128 v[52:55], v115 offset:14336\n"
"v_mul_f32 v78, v70, v70\n"
"v_mfma_f32_16x16x32_bf16 v[64:67], a[60:63], v[56:59], v[64:67]\n"
"s_waitcnt lgkmcnt(5)\n"
"v_fmamk_f32 v78, v78, 0xbdd2d3e2, v112\n"
"v_mul_f32 v79, v71, v71\n"
"v_mfma_f32_16x16x32_bf16 v[60:63], a[64:67], v[28:31], v[60:63]\n"
"s_waitcnt vmcnt(19)\n"
"ds_read_b128 v[56:59], v117 offset:14336\n"
"v_mul_f32 v78, v78, v70\n"
"v_mfma_f32_16x16x32_bf16 v[64:67], a[68:71], v[28:31], v[64:67]\n"
"s_barrier\n"
"v_exp_f32 v78, v78\n"
"v_mfma_f32_16x16x32_bf16 v[60:63], a[72:75], v[32:35], v[60:63]\n"
"s_add_u32 m0, s44, 49152\n"
"ds_read_b128 v[28:31], v114 offset:0\n"
"v_add_f32 v78, 1.0, v78\n"
"v_mfma_f32_16x16x32_bf16 v[64:67], a[76:79], v[32:35], v[64:67]\n"
"s_waitcnt lgkmcnt(5)\n"
"v_rcp_f32 v78, v78\n"
"v_mfma_f32_16x16x32_bf16 v[60:63], a[80:83], v[36:39], v[60:63]\n"
"ds_read_b128 v[32:35], v116 offset:0\n"
"buffer_load_dwordx4 v113, s[64:67], s40 offen offset:0 sc1 lds\n"
"v_mfma_f32_16x16x32_bf16 v[64:67], a[84:87], v[36:39], v[64:67]\n"
"v_fmamk_f32 v79, v79, 0xbdd2d3e2, v112\n"
"v_mul_f32 v80, v72, v72\n"
"v_mfma_f32_16x16x32_bf16 v[60:63], a[88:91], v[40:43], v[60:63]\n"
"ds_read_b128 v[36:39], v114 offset:2048\n"
"buffer_load_dwordx4 v113, s[64:67], s40 offen offset:1024 sc1 lds\n"
"v_mfma_f32_16x16x32_bf16 v[64:67], a[92:95], v[40:43], v[64:67]\n"
"s_waitcnt lgkmcnt(5)\n"
"v_mul_f32 v79, v79, v71\n"
"v_fmamk_f32 v80, v80, 0xbdd2d3e2, v112\n"
"v_mfma_f32_16x16x32_bf16 v[60:63], a[96:99], v[44:47], v[60:63]\n"
"ds_read_b128 v[40:43], v116 offset:2048\n"
"buffer_load_dwordx4 v113, s[64:67], s40 offen offset:2048 sc1 lds\n"
"v_mfma_f32_16x16x32_bf16 v[64:67], a[100:103], v[44:47], v[64:67]\n"
"v_exp_f32 v79, v79\n"
"v_mfma_f32_16x16x32_bf16 v[60:63], a[104:107], v[48:51], v[60:63]\n"
"ds_read_b128 v[44:47], v114 offset:4096\n"
"buffer_load_dwordx4 v113, s[64:67], s40 offen offset:3072 sc1 lds\n"
"v_mfma_f32_16x16x32_bf16 v[64:67], a[108:111], v[48:51], v[64:67]\n"
"s_waitcnt lgkmcnt(5)\n"
"v_add_f32 v79, 1.0, v79\n"
"v_mul_f32 v80, v80, v72\n"
"v_mfma_f32_16x16x32_bf16 v[60:63], a[112:115], v[52:55], v[60:63]\n"
"s_add_u32 s40, s40, 16384\n"
"ds_read_b128 v[48:51], v116 offset:4096\n"
"v_mul_f32 v81, v73, v73\n"
"v_mfma_f32_16x16x32_bf16 v[64:67], a[116:119], v[52:55], v[64:67]\n"
"s_add_u32 s95, s43, 1441792\n"
"v_rcp_f32 v79, v79\n"
"v_mfma_f32_16x16x32_bf16 v[60:63], a[120:123], v[56:59], v[60:63]\n"
"s_add_u32 m0, s44, 114688\n"
"ds_read_b128 v[52:55], v114 offset:6144\n"
"v_fmamk_f32 v81, v81, 0xbdd2d3e2, v112\n"
"v_mfma_f32_16x16x32_bf16 v[64:67], a[124:127], v[56:59], v[64:67]\n"
"s_waitcnt lgkmcnt(5)\n"
"v_exp_f32 v80, v80\n"
"v_mfma_f32_16x16x32_bf16 v[60:63], a[128:131], v[28:31], v[60:63]\n"
"s_getpc_b64 s[62:63]\n"
"ds_read_b128 v[56:59], v116 offset:6144\n"
"buffer_load_dwordx4 v122, s[80:83], s95 offen offset:0 lds\n"
"v_mfma_f32_16x16x32_bf16 v[64:67], a[132:135], v[28:31], v[64:67]\n"
"global_load_dword v21, v119, s[62:63] offset:3072\n"
"v_add_f32 v80, 1.0, v80\n"
"v_mfma_f32_16x16x32_bf16 v[60:63], a[136:139], v[32:35], v[60:63]\n"
"ds_read_b128 v[28:31], v114 offset:8192\n"
"buffer_load_dwordx4 v123, s[80:83], s95 offen offset:1024 lds\n"
"v_mfma_f32_16x16x32_bf16 v[64:67], a[140:143], v[32:35], v[64:67]\n"
"s_waitcnt lgkmcnt(5)\n"
"v_rcp_f32 v80, v80\n"
"v_mfma_f32_16x16x32_bf16 v[60:63], a[144:147], v[36:39], v[60:63]\n"
"ds_read_b128 v[32:35], v116 offset:8192\n"
"buffer_load_dwordx4 v124, s[80:83], s95 offen offset:2048 lds\n"
"v_mfma_f32_16x16x32_bf16 v[64:67], a[148:151], v[36:39], v[64:67]\n"
"v_mul_f32 v81, v81, v73\n"
"v_mul_f32 v82, v74, v74\n"
"v_mfma_f32_16x16x32_bf16 v[60:63], a[152:155], v[40:43], v[60:63]\n"
"ds_read_b128 v[36:39], v114 offset:10240\n"
"buffer_load_dwordx4 v125, s[80:83], s95 offen offset:3072 lds\n"
"v_mfma_f32_16x16x32_bf16 v[64:67], a[156:159], v[40:43], v[64:67]\n"
"s_waitcnt lgkmcnt(5)\n"
"v_exp_f32 v81, v81\n"
"v_mfma_f32_16x16x32_bf16 v[60:63], a[160:163], v[44:47], v[60:63]\n"
"ds_read_b128 v[40:43], v116 offset:10240\n"
"v_add_f32 v81, 1.0, v81\n"
"v_mfma_f32_16x16x32_bf16 v[64:67], a[164:167], v[44:47], v[64:67]\n"
"v_rcp_f32 v81, v81\n"
"v_mfma_f32_16x16x32_bf16 v[60:63], a[168:171], v[48:51], v[60:63]\n"
"ds_read_b128 v[44:47], v114 offset:12288\n"
"v_fmamk_f32 v82, v82, 0xbdd2d3e2, v112\n"
"v_mfma_f32_16x16x32_bf16 v[64:67], a[172:175], v[48:51], v[64:67]\n"
"s_waitcnt lgkmcnt(5)\n"
"v_mul_f32 v82, v82, v74\n"
"v_mul_f32 v83, v75, v75\n"
"v_mfma_f32_16x16x32_bf16 v[60:63], a[176:179], v[52:55], v[60:63]\n"
"ds_read_b128 v[48:51], v116 offset:12288\n"
"v_fmamk_f32 v83, v83, 0xbdd2d3e2, v112\n"
"v_mfma_f32_16x16x32_bf16 v[64:67], a[180:183], v[52:55], v[64:67]\n"
"v_exp_f32 v82, v82\n"
"v_mfma_f32_16x16x32_bf16 v[60:63], a[184:187], v[56:59], v[60:63]\n"
"ds_read_b128 v[52:55], v114 offset:14336\n"
"v_add_f32 v82, 1.0, v82\n"
"v_mfma_f32_16x16x32_bf16 v[64:67], a[188:191], v[56:59], v[64:67]\n"
"s_waitcnt lgkmcnt(5)\n"
"v_rcp_f32 v82, v82\n"
"v_mfma_f32_16x16x32_bf16 v[60:63], a[192:195], v[28:31], v[60:63]\n"
"s_waitcnt vmcnt(18)\n"
"ds_read_b128 v[56:59], v116 offset:14336\n"
"v_mul_f32 v83, v83, v75\n"
"v_mfma_f32_16x16x32_bf16 v[64:67], a[196:199], v[28:31], v[64:67]\n"
"s_barrier\n"
"v_exp_f32 v83, v83\n"
"v_mfma_f32_16x16x32_bf16 v[60:63], a[200:203], v[32:35], v[60:63]\n"
"s_add_u32 m0, s44, 65536\n"
"ds_read_b128 v[28:31], v114 offset:16384\n"
"v_readfirstlane_b32 s93, v2\n"
"v_mfma_f32_16x16x32_bf16 v[64:67], a[204:207], v[32:35], v[64:67]\n"
"s_waitcnt lgkmcnt(5)\n"
"buffer_load_dwordx4 v113, s[64:67], s40 offen offset:0 sc1 lds\n"
"v_add_f32 v83, 1.0, v83\n"
"v_mfma_f32_16x16x32_bf16 v[60:63], a[208:211], v[36:39], v[60:63]\n"
"s_cmp_lt_u32 s93, s49\n"
"s_cbranch_scc1 Lslow_h0_K\n"
"Lret_h0_K:\n"
"ds_read_b128 v[32:35], v116 offset:16384\n"
"v_mfma_f32_16x16x32_bf16 v[64:67], a[212:215], v[36:39], v[64:67]\n"
"v_rcp_f32 v83, v83\n"
"v_mfma_f32_16x16x32_bf16 v[60:63], a[216:219], v[40:43], v[60:63]\n"
"ds_read_b128 v[36:39], v114 offset:18432\n"
"v_mul_f32 v76, v68, v76\n"
"v_mfma_f32_16x16x32_bf16 v[64:67], a[220:223], v[40:43], v[64:67]\n"
"s_waitcnt lgkmcnt(5)\n"
"v_mul_f32 v77, v69, v77\n"
"v_mul_f32 v78, v70, v78\n"
"v_mfma_f32_16x16x32_bf16 v[60:63], a[224:227], v[44:47], v[60:63]\n"
"ds_read_b128 v[40:43], v116 offset:18432\n"
"v_mul_f32 v79, v71, v79\n"
"v_mfma_f32_16x16x32_bf16 v[64:67], a[228:231], v[44:47], v[64:67]\n"
"v_mul_f32 v80, v72, v80\n"
"v_mul_f32 v81, v73, v81\n"
"v_mfma_f32_16x16x32_bf16 v[60:63], a[232:235], v[48:51], v[60:63]\n"
"ds_read_b128 v[44:47], v114 offset:20480\n"
"buffer_load_dwordx4 v113, s[64:67], s40 offen offset:1024 sc1 lds\n"
"v_mfma_f32_16x16x32_bf16 v[64:67], a[236:239], v[48:51], v[64:67]\n"
"s_waitcnt lgkmcnt(5)\n"
"v_mul_f32 v82, v74, v82\n"
"v_mul_f32 v83, v75, v83\n"
"v_mfma_f32_16x16x32_bf16 v[60:63], a[240:243], v[52:55], v[60:63]\n"
"ds_read_b128 v[48:51], v116 offset:20480\n"
"v_cvt_pk_bf16_f32 v84, v76, v77\n"
"v_mfma_f32_16x16x32_bf16 v[64:67], a[244:247], v[52:55], v[64:67]\n"
"v_cvt_pk_bf16_f32 v85, v78, v79\n"
"v_cvt_pk_bf16_f32 v86, v80, v81\n"
"v_mfma_f32_16x16x32_bf16 v[60:63], a[248:251], v[56:59], v[60:63]\n"
"ds_read_b128 v[52:55], v114 offset:22528\n"
"v_cvt_pk_bf16_f32 v87, v82, v83\n"
"v_mfma_f32_16x16x32_bf16 v[64:67], a[252:255], v[56:59], v[64:67]\n"
"s_waitcnt lgkmcnt(5)\n"
"v_mfma_f32_16x16x32_bf16 v[68:71], a[0:3], v[28:31], v[88:91]\n"
"ds_read_b128 v[56:59], v116 offset:22528\n"
"buffer_load_dwordx4 v113, s[64:67], s40 offen offset:2048 sc1 lds\n"
"v_mfma_f32_16x16x32_bf16 v[72:75], a[4:7], v[28:31], v[92:95]\n"
"buffer_store_dwordx4 v[84:87], v113, s[68:71], s41 offen sc1\n"
"v_mfma_f32_16x16x32_bf16 v[68:71], a[8:11], v[32:35], v[68:71]\n"
"s_add_u32 s41, s41, 1032192\n"
"ds_read_b128 v[28:31], v114 offset:24576\n"
"v_mfma_f32_16x16x32_bf16 v[72:75], a[12:15], v[32:35], v[72:75]\n"
"s_waitcnt lgkmcnt(5)\n"
"v_mul_f32 v76, v60, v60\n"
"v_mul_f32 v77, v61, v61\n"
"v_mfma_f32_16x16x32_bf16 v[68:71], a[16:19], v[36:39], v[68:71]\n"
"ds_read_b128 v[32:35], v116 offset:24576\n"
"v_fmamk_f32 v76, v76, 0xbdd2d3e2, v112\n"
"v_mfma_f32_16x16x32_bf16 v[72:75], a[20:23], v[36:39], v[72:75]\n"
"v_mul_f32 v76, v76, v60\n"
"v_fmamk_f32 v77, v77, 0xbdd2d3e2, v112\n"
"v_mfma_f32_16x16x32_bf16 v[68:71], a[24:27], v[40:43], v[68:71]\n"
"ds_read_b128 v[36:39], v114 offset:26624\n"
"buffer_load_dwordx4 v113, s[64:67], s40 offen offset:3072 sc1 lds\n"
"v_mfma_f32_16x16x32_bf16 v[72:75], a[28:31], v[40:43], v[72:75]\n"
"s_waitcnt lgkmcnt(5)\n"
"v_exp_f32 v76, v76\n"
"v_mfma_f32_16x16x32_bf16 v[68:71], a[32:35], v[44:47], v[68:71]\n"
"ds_read_b128 v[40:43], v116 offset:26624\n"
"v_add_f32 v76, 1.0, v76\n"
"v_mfma_f32_16x16x32_bf16 v[72:75], a[36:39], v[44:47], v[72:75]\n"
"v_rcp_f32 v76, v76\n"
"v_mfma_f32_16x16x32_bf16 v[68:71], a[40:43], v[48:51], v[68:71]\n"
"ds_read_b128 v[44:47], v114 offset:28672\n"
"v_mul_f32 v77, v77, v61\n"
"v_mfma_f32_16x16x32_bf16 v[72:75], a[44:47], v[48:51], v[72:75]\n"
"s_waitcnt lgkmcnt(5)\n"
"v_exp_f32 v77, v77\n"
"v_mfma_f32_16x16x32_bf16 v[68:71], a[48:51], v[52:55], v[68:71]\n"
"ds_read_b128 v[48:51], v116 offset:28672\n"
"v_add_f32 v77, 1.0, v77\n"
"v_mfma_f32_16x16x32_bf16 v[72:75], a[52:55], v[52:55], v[72:75]\n"
"v_rcp_f32 v77, v77\n"
"v_mfma_f32_16x16x32_bf16 v[68:71], a[56:59], v[56:59], v[68:71]\n"
"ds_read_b128 v[52:55], v114 offset:30720\n"
"v_mul_f32 v78, v62, v62\n"
"v_mfma_f32_16x16x32_bf16 v[72:75], a[60:63], v[56:59], v[72:75]\n"
"s_waitcnt lgkmcnt(5)\n"
"v_fmamk_f32 v78, v78, 0xbdd2d3e2, v112\n"
"v_mul_f32 v79, v63, v63\n"
"v_mfma_f32_16x16x32_bf16 v[68:71], a[64:67], v[28:31], v[68:71]\n"
"s_waitcnt vmcnt(14)\n"
"ds_read_b128 v[56:59], v116 offset:30720\n"
"v_mul_f32 v78, v78, v62\n"
"v_mfma_f32_16x16x32_bf16 v[72:75], a[68:71], v[28:31], v[72:75]\n"
"s_barrier\n"
"v_exp_f32 v78, v78\n"
"v_mfma_f32_16x16x32_bf16 v[68:71], a[72:75], v[32:35], v[68:71]\n"
"s_add_u32 s92, s51, s44\n"
"ds_read_b128 v[28:31], v114 offset:32768\n"
"v_add_f32 v78, 1.0, v78\n"
"v_mfma_f32_16x16x32_bf16 v[72:75], a[76:79], v[32:35], v[72:75]\n"
"s_waitcnt lgkmcnt(5)\n"
"v_rcp_f32 v78, v78\n"
"v_mfma_f32_16x16x32_bf16 v[68:71], a[80:83], v[36:39], v[68:71]\n"
"s_add_u32 m0, s44, 0\n"
"ds_read_b128 v[32:35], v116 offset:32768\n"
"v_fmamk_f32 v79, v79, 0xbdd2d3e2, v112\n"
"v_mfma_f32_16x16x32_bf16 v[72:75], a[84:87], v[36:39], v[72:75]\n"
"v_mul_f32 v79, v79, v63\n"
"v_mul_f32 v80, v64, v64\n"
"v_mfma_f32_16x16x32_bf16 v[68:71], a[88:91], v[40:43], v[68:71]\n"
"ds_read_b128 v[36:39], v114 offset:34816\n"
"buffer_load_dwordx4 v113, s[72:75], s92 offen offset:0 sc1 lds\n"
"v_mfma_f32_16x16x32_bf16 v[72:75], a[92:95], v[40:43], v[72:75]\n"
"s_waitcnt lgkmcnt(5)\n"
"v_exp_f32 v79, v79\n"
"v_mfma_f32_16x16x32_bf16 v[68:71], a[96:99], v[44:47], v[68:71]\n"
"ds_read_b128 v[40:43], v116 offset:34816\n"
"buffer_load_dwordx4 v113, s[72:75], s92 offen offset:1024 sc1 lds\n"
"v_mfma_f32_16x16x32_bf16 v[72:75], a[100:103], v[44:47], v[72:75]\n"
"v_add_f32 v79, 1.0, v79\n"
"v_fmamk_f32 v80, v80, 0xbdd2d3e2, v112\n"
"v_mfma_f32_16x16x32_bf16 v[68:71], a[104:107], v[48:51], v[68:71]\n"
"ds_read_b128 v[44:47], v114 offset:36864\n"
"buffer_load_dwordx4 v113, s[72:75], s92 offen offset:2048 sc1 lds\n"
"v_mfma_f32_16x16x32_bf16 v[72:75], a[108:111], v[48:51], v[72:75]\n"
"s_waitcnt lgkmcnt(5)\n"
"v_rcp_f32 v79, v79\n"
"v_mfma_f32_16x16x32_bf16 v[68:71], a[112:115], v[52:55], v[68:71]\n"
"ds_read_b128 v[48:51], v116 offset:36864\n"
"buffer_load_dwordx4 v113, s[72:75], s92 offen offset:3072 sc1 lds\n"
"v_mfma_f32_16x16x32_bf16 v[72:75], a[116:119], v[52:55], v[72:75]\n"
"s_add_u32 s95, s43, 1572864\n"
"v_mul_f32 v80, v80, v64\n"
"v_mul_f32 v81, v65, v65\n"
"v_mfma_f32_16x16x32_bf16 v[68:71], a[120:123], v[56:59], v[68:71]\n"
"s_add_u32 m0, s44, 131072\n"
"ds_read_b128 v[52:55], v114 offset:38912\n"
"v_fmamk_f32 v81, v81, 0xbdd2d3e2, v112\n"
"v_mfma_f32_16x16x32_bf16 v[72:75], a[124:127], v[56:59], v[72:75]\n"
"s_waitcnt lgkmcnt(5)\n"
"v_exp_f32 v80, v80\n"
"v_mfma_f32_16x16x32_bf16 v[68:71], a[128:131], v[28:31], v[68:71]\n"
"ds_read_b128 v[56:59], v116 offset:38912\n"
"buffer_load_dwordx4 v122, s[80:83], s95 offen offset:0 lds\n"
"v_mfma_f32_16x16x32_bf16 v[72:75], a[132:135], v[28:31], v[72:75]\n"
"v_add_f32 v80, 1.0, v80\n"
"v_mul_f32 v81, v81, v65\n"
"v_mfma_f32_16x16x32_bf16 v[68:71], a[136:139], v[32:35], v[68:71]\n"
"ds_read_b128 v[28:31], v114 offset:40960\n"
"buffer_load_dwordx4 v123, s[80:83], s95 offen offset:1024 lds\n"
"v_mfma_f32_16x16x32_bf16 v[72:75], a[140:143], v[32:35], v[72:75]\n"
"s_waitcnt lgkmcnt(5)\n"
"v_rcp_f32 v80, v80\n"
"v_mfma_f32_16x16x32_bf16 v[68:71], a[144:147], v[36:39], v[68:71]\n"
"ds_read_b128 v[32:35], v116 offset:40960\n"
"buffer_load_dwordx4 v124, s[80:83], s95 offen offset:2048 lds\n"
"v_mfma_f32_16x16x32_bf16 v[72:75], a[148:151], v[36:39], v[72:75]\n"
"v_exp_f32 v81, v81\n"
"v_mfma_f32_16x16x32_bf16 v[68:71], a[152:155], v[40:43], v[68:71]\n"
"ds_read_b128 v[36:39], v114 offset:43008\n"
"buffer_load_dwordx4 v125, s[80:83], s95 offen offset:3072 lds\n"
"v_mfma_f32_16x16x32_bf16 v[72:75], a[156:159], v[40:43], v[72:75]\n"
"s_waitcnt lgkmcnt(5)\n"
"v_add_f32 v81, 1.0, v81\n"
"v_mul_f32 v82, v66, v66\n"
"v_mfma_f32_16x16x32_bf16 v[68:71], a[160:163], v[44:47], v[68:71]\n"
"ds_read_b128 v[40:43], v116 offset:43008\n"
"v_fmamk_f32 v82, v82, 0xbdd2d3e2, v112\n"
"v_mfma_f32_16x16x32_bf16 v[72:75], a[164:167], v[44:47], v[72:75]\n"
"v_rcp_f32 v81, v81\n"
"v_mfma_f32_16x16x32_bf16 v[68:71], a[168:171], v[48:51], v[68:71]\n"
"ds_read_b128 v[44:47], v114 offset:45056\n"
"v_mul_f32 v82, v82, v66\n"
"v_mfma_f32_16x16x32_bf16 v[72:75], a[172:175], v[48:51], v[72:75]\n"
"s_waitcnt lgkmcnt(5)\n"
"v_exp_f32 v82, v82\n"
"v_mfma_f32_16x16x32_bf16 v[68:71], a[176:179], v[52:55], v[68:71]\n"
"ds_read_b128 v[48:51], v116 offset:45056\n"
"v_add_f32 v82, 1.0, v82\n"
"v_mfma_f32_16x16x32_bf16 v[72:75], a[180:183], v[52:55], v[72:75]\n"
"v_rcp_f32 v82, v82\n"
"v_mfma_f32_16x16x32_bf16 v[68:71], a[184:187], v[56:59], v[68:71]\n"
"ds_read_b128 v[52:55], v114 offset:47104\n"
"v_mul_f32 v83, v67, v67\n"
"v_mfma_f32_16x16x32_bf16 v[72:75], a[188:191], v[56:59], v[72:75]\n"
"s_waitcnt lgkmcnt(5)\n"
"v_fmamk_f32 v83, v83, 0xbdd2d3e2, v112\n"
"v_mul_f32 v76, v60, v76\n"
"v_mfma_f32_16x16x32_bf16 v[68:71], a[192:195], v[28:31], v[68:71]\n"
"s_waitcnt vmcnt(18)\n"
"ds_read_b128 v[56:59], v116 offset:47104\n"
"v_mul_f32 v83, v83, v67\n"
"v_mfma_f32_16x16x32_bf16 v[72:75], a[196:199], v[28:31], v[72:75]\n"
"s_barrier\n"
"v_exp_f32 v83, v83\n"
"v_mfma_f32_16x16x32_bf16 v[68:71], a[200:203], v[32:35], v[68:71]\n"
"s_add_u32 s92, s51, s45\n"
"buffer_atomic_add v1, v0, s[72:75], 0 offen offset:512\n"
"ds_read_b128 v[28:31], v114 offset:49152\n"
"v_mfma_f32_16x16x32_bf16 v[72:75], a[204:207], v[32:35], v[72:75]\n"
"s_waitcnt lgkmcnt(5)\n"
"v_add_f32 v83, 1.0, v83\n"
"v_mul_f32 v77, v61, v77\n"
"v_mfma_f32_16x16x32_bf16 v[68:71], a[208:211], v[36:39], v[68:71]\n"
"s_add_u32 m0, s45, 0\n"
"ds_read_b128 v[32:35], v116 offset:49152\n"
"v_mul_f32 v78, v62, v78\n"
"v_mfma_f32_16x16x32_bf16 v[72:75], a[212:215], v[36:39], v[72:75]\n"
"v_rcp_f32 v83, v83\n"
"v_mfma_f32_16x16x32_bf16 v[68:71], a[216:219], v[40:43], v[68:71]\n"
"ds_read_b128 v[36:39], v114 offset:51200\n"
"buffer_load_dwordx4 v113, s[72:75], s92 offen offset:0 sc1 lds\n"
"v_mfma_f32_16x16x32_bf16 v[72:75], a[220:223], v[40:43], v[72:75]\n"
"s_waitcnt lgkmcnt(5)\n"
"v_mul_f32 v79, v63, v79\n"
"v_mul_f32 v80, v64, v80\n"
"v_mfma_f32_16x16x32_bf16 v[68:71], a[224:227], v[44:47], v[68:71]\n"
"ds_read_b128 v[40:43], v116 offset:51200\n"
"v_mul_f32 v81, v65, v81\n"
"v_mfma_f32_16x16x32_bf16 v[72:75], a[228:231], v[44:47], v[72:75]\n"
"v_mul_f32 v82, v66, v82\n"
"v_mul_f32 v83, v67, v83\n"
"v_mfma_f32_16x16x32_bf16 v[68:71], a[232:235], v[48:51], v[68:71]\n"
"ds_read_b128 v[44:47], v114 offset:53248\n"
"v_cvt_pk_bf16_f32 v84, v76, v77\n"
"v_mfma_f32_16x16x32_bf16 v[72:75], a[236:239], v[48:51], v[72:75]\n"
"s_waitcnt lgkmcnt(5)\n"
"v_cvt_pk_bf16_f32 v85, v78, v79\n"
"v_cvt_pk_bf16_f32 v86, v80, v81\n"
"v_mfma_f32_16x16x32_bf16 v[68:71], a[240:243], v[52:55], v[68:71]\n"
"ds_read_b128 v[48:51], v116 offset:53248\n"
"buffer_load_dwordx4 v113, s[72:75], s92 offen offset:1024 sc1 lds\n"
"v_mfma_f32_16x16x32_bf16 v[72:75], a[244:247], v[52:55], v[72:75]\n"
"v_cvt_pk_bf16_f32 v87, v82, v83\n"
"v_mfma_f32_16x16x32_bf16 v[68:71], a[248:251], v[56:59], v[68:71]\n"
"ds_read_b128 v[52:55], v114 offset:55296\n"
"v_mfma_f32_16x16x32_bf16 v[72:75], a[252:255], v[56:59], v[72:75]\n"
"s_waitcnt lgkmcnt(5)\n"
"v_mfma_f32_16x16x32_bf16 v[60:63], a[0:3], v[28:31], v[88:91]\n"
"ds_read_b128 v[56:59], v116 offset:55296\n"
"v_mfma_f32_16x16x32_bf16 v[64:67], a[4:7], v[28:31], v[92:95]\n"
"buffer_store_dwordx4 v[84:87], v113, s[68:71], s41 offen sc1\n"
"v_mfma_f32_16x16x32_bf16 v[60:63], a[8:11], v[32:35], v[60:63]\n"
"s_add_u32 s41, s41, 1032192\n"
"ds_read_b128 v[28:31], v114 offset:57344\n"
"buffer_load_dwordx4 v113, s[72:75], s92 offen offset:2048 sc1 lds\n"
"v_mfma_f32_16x16x32_bf16 v[64:67], a[12:15], v[32:35], v[64:67]\n"
"s_waitcnt lgkmcnt(5)\n"
"v_mul_f32 v76, v68, v68\n"
"v_mul_f32 v77, v69, v69\n"
"v_mul_f32 v78, v70, v70\n"
"v_mfma_f32_16x16x32_bf16 v[60:63], a[16:19], v[36:39], v[60:63]\n"
"s_add_u32 s51, s51, 3670016\n"
"ds_read_b128 v[32:35], v116 offset:57344\n"
"ds_read_b64_tr_b16 a[0:1], v121 offset:16384\n"
"v_fmamk_f32 v76, v76, 0xbdd2d3e2, v112\n"
"v_mfma_f32_16x16x32_bf16 v[64:67], a[20:23], v[36:39], v[64:67]\n"
"v_mul_f32 v76, v76, v68\n"
"v_fmamk_f32 v77, v77, 0xbdd2d3e2, v112\n"
"v_fmamk_f32 v78, v78, 0xbdd2d3e2, v112\n"
"v_mfma_f32_16x16x32_bf16 v[60:63], a[24:27], v[40:43], v[60:63]\n"
"ds_read_b128 v[36:39], v114 offset:59392\n"
"ds_read_b64_tr_b16 a[2:3], v121 offset:17408\n"
"v_mul_f32 v77, v77, v69\n"
"v_mfma_f32_16x16x32_bf16 v[64:67], a[28:31], v[40:43], v[64:67]\n"
"s_waitcnt lgkmcnt(7)\n"
"v_exp_f32 v76, v76\n"
"v_mul_f32 v78, v78, v70\n"
"v_mfma_f32_16x16x32_bf16 v[60:63], a[32:35], v[44:47], v[60:63]\n"
"ds_read_b128 v[40:43], v116 offset:59392\n"
"ds_read_b64_tr_b16 a[4:5], v121 offset:16392\n"
"v_add_f32 v76, 1.0, v76\n"
"v_mfma_f32_16x16x32_bf16 v[64:67], a[36:39], v[44:47], v[64:67]\n"
"v_rcp_f32 v76, v76\n"
"v_mul_f32 v79, v71, v71\n"
"v_mfma_f32_16x16x32_bf16 v[60:63], a[40:43], v[48:51], v[60:63]\n"
"ds_read_b128 v[44:47], v114 offset:61440\n"
"ds_read_b64_tr_b16 a[6:7], v121 offset:17416\n"
"v_fmamk_f32 v79, v79, 0xbdd2d3e2, v112\n"
"v_mfma_f32_16x16x32_bf16 v[64:67], a[44:47], v[48:51], v[64:67]\n"
"s_waitcnt lgkmcnt(9)\n"
"v_exp_f32 v77, v77\n"
"v_mul_f32 v79, v79, v71\n"
"v_mfma_f32_16x16x32_bf16 v[60:63], a[48:51], v[52:55], v[60:63]\n"
"ds_read_b128 v[48:51], v116 offset:61440\n"
"ds_read_b64_tr_b16 a[8:9], v121 offset:24576\n"
"v_add_f32 v77, 1.0, v77\n"
"v_mfma_f32_16x16x32_bf16 v[64:67], a[52:55], v[52:55], v[64:67]\n"
"v_rcp_f32 v77, v77\n"
"v_mul_f32 v80, v72, v72\n"
"v_mfma_f32_16x16x32_bf16 v[60:63], a[56:59], v[56:59], v[60:63]\n"
"ds_read_b128 v[52:55], v114 offset:63488\n"
"ds_read_b64_tr_b16 a[10:11], v121 offset:25600\n"
"v_fmamk_f32 v80, v80, 0xbdd2d3e2, v112\n"
"v_mfma_f32_16x16x32_bf16 v[64:67], a[60:63], v[56:59], v[64:67]\n"
"s_waitcnt lgkmcnt(11)\n"
"v_exp_f32 v78, v78\n"
"v_mul_f32 v80, v80, v72\n"
"v_mfma_f32_16x16x32_bf16 v[60:63], a[64:67], v[28:31], v[60:63]\n"
"s_waitcnt vmcnt(13)\n"
"ds_read_b128 v[56:59], v116 offset:63488\n"
"ds_read_b64_tr_b16 a[12:13], v121 offset:24584\n"
"v_add_f32 v78, 1.0, v78\n"
"v_mfma_f32_16x16x32_bf16 v[64:67], a[68:71], v[28:31], v[64:67]\n"
"s_barrier\n"
"v_rcp_f32 v78, v78\n"
"v_mul_f32 v81, v73, v73\n"
"v_mfma_f32_16x16x32_bf16 v[60:63], a[72:75], v[32:35], v[60:63]\n"
"s_add_u32 s92, s52, s44\n"
"ds_read_b128 v[28:31], v115 offset:0\n"
"buffer_load_dword v2, v0, s[72:75], 0 offen offset:256 sc1\n"
"v_fmamk_f32 v81, v81, 0xbdd2d3e2, v112\n"
"v_mfma_f32_16x16x32_bf16 v[64:67], a[76:79], v[32:35], v[64:67]\n"
"s_waitcnt lgkmcnt(10)\n"
"ds_read_b64_tr_b16 a[14:15], v121 offset:25608\n"
"v_exp_f32 v79, v79\n"
"v_mfma_f32_16x16x32_bf16 v[60:63], a[80:83], v[36:39], v[60:63]\n"
"s_add_u32 m0, s44, 32768\n"
"ds_read_b128 v[32:35], v117 offset:0\n"
"v_add_f32 v79, 1.0, v79\n"
"v_mfma_f32_16x16x32_bf16 v[64:67], a[84:87], v[36:39], v[64:67]\n"
"ds_read_b64_tr_b16 a[16:17], v120 offset:16384\n"
"v_mul_f32 v81, v81, v73\n"
"v_mfma_f32_16x16x32_bf16 v[60:63], a[88:91], v[40:43], v[60:63]\n"
"ds_read_b128 v[36:39], v115 offset:2048\n"
"buffer_load_dwordx4 v113, s[72:75], s92 offen offset:0 sc1 lds\n"
"v_mfma_f32_16x16x32_bf16 v[64:67], a[92:95], v[40:43], v[64:67]\n"
"s_waitcnt lgkmcnt(10)\n"
"v_rcp_f32 v79, v79\n"
"v_mfma_f32_16x16x32_bf16 v[60:63], a[96:99], v[44:47], v[60:63]\n"
"ds_read_b128 v[40:43], v117 offset:2048\n"
"ds_read_b64_tr_b16 a[18:19], v120 offset:17408\n"
"v_mfma_f32_16x16x32_bf16 v[64:67], a[100:103], v[44:47], v[64:67]\n"
"v_exp_f32 v80, v80\n"
"v_mfma_f32_16x16x32_bf16 v[60:63], a[104:107], v[48:51], v[60:63]\n"
"ds_read_b128 v[44:47], v115 offset:4096\n"
"ds_read_b64_tr_b16 a[20:21], v120 offset:16392\n"
"v_mfma_f32_16x16x32_bf16 v[64:67], a[108:111], v[48:51], v[64:67]\n"
"s_waitcnt lgkmcnt(10)\n"
"v_exp_f32 v81, v81\n"
"v_mfma_f32_16x16x32_bf16 v[60:63], a[112:115], v[52:55], v[60:63]\n"
"ds_read_b128 v[48:51], v117 offset:4096\n"
"buffer_load_dwordx4 v113, s[72:75], s92 offen offset:1024 sc1 lds\n"
"v_mfma_f32_16x16x32_bf16 v[64:67], a[116:119], v[52:55], v[64:67]\n"
"ds_read_b64_tr_b16 a[22:23], v120 offset:17416\n"
"v_add_f32 v80, 1.0, v80\n"
"v_mfma_f32_16x16x32_bf16 v[60:63], a[120:123], v[56:59], v[60:63]\n"
"ds_read_b128 v[52:55], v115 offset:6144\n"
"v_add_f32 v81, 1.0, v81\n"
"v_mfma_f32_16x16x32_bf16 v[64:67], a[124:127], v[56:59], v[64:67]\n"
"s_waitcnt lgkmcnt(9)\n"
"v_rcp_f32 v80, v80\n"
"v_mfma_f32_16x16x32_bf16 v[60:63], a[128:131], v[28:31], v[60:63]\n"
"ds_read_b128 v[56:59], v117 offset:6144\n"
"ds_read_b64_tr_b16 a[24:25], v120 offset:24576\n"
"v_mfma_f32_16x16x32_bf16 v[64:67], a[132:135], v[28:31], v[64:67]\n"
"v_rcp_f32 v81, v81\n"
"v_mfma_f32_16x16x32_bf16 v[60:63], a[136:139], v[32:35], v[60:63]\n"
"ds_read_b128 v[28:31], v115 offset:8192\n"
"buffer_load_dwordx4 v113, s[72:75], s92 offen offset:2048 sc1 lds\n"
"v_mfma_f32_16x16x32_bf16 v[64:67], a[140:143], v[32:35], v[64:67]\n"
"s_waitcnt lgkmcnt(9)\n"
"ds_read_b64_tr_b16 a[26:27], v120 offset:25600\n"
"v_mul_f32 v82, v74, v74\n"
"v_mfma_f32_16x16x32_bf16 v[60:63], a[144:147], v[36:39], v[60:63]\n"
"ds_read_b128 v[32:35], v117 offset:8192\n"
"v_fmamk_f32 v82, v82, 0xbdd2d3e2, v112\n"
"v_mfma_f32_16x16x32_bf16 v[64:67], a[148:151], v[36:39], v[64:67]\n"
"v_mul_f32 v82, v82, v74\n"
"v_mul_f32 v83, v75, v75\n"
"v_mfma_f32_16x16x32_bf16 v[60:63], a[152:155], v[40:43], v[60:63]\n"
"ds_read_b128 v[36:39], v115 offset:10240\n"
"ds_read_b64_tr_b16 a[28:29], v120 offset:24584\n"
"v_mfma_f32_16x16x32_bf16 v[64:67], a[156:159], v[40:43], v[64:67]\n"
"s_waitcnt lgkmcnt(9)\n"
"v_exp_f32 v82, v82\n"
"v_mfma_f32_16x16x32_bf16 v[60:63], a[160:163], v[44:47], v[60:63]\n"
"ds_read_b128 v[40:43], v117 offset:10240\n"
"buffer_load_dwordx4 v113, s[72:75], s92 offen offset:3072 sc1 lds\n"
"v_mfma_f32_16x16x32_bf16 v[64:67], a[164:167], v[44:47], v[64:67]\n"
"ds_read_b64_tr_b16 a[30:31], v120 offset:25608\n"
"v_add_f32 v82, 1.0, v82\n"
"v_mfma_f32_16x16x32_bf16 v[60:63], a[168:171], v[48:51], v[60:63]\n"
"ds_read_b128 v[44:47], v115 offset:12288\n"
"v_fmamk_f32 v83, v83, 0xbdd2d3e2, v112\n"
"v_mfma_f32_16x16x32_bf16 v[64:67], a[172:175], v[48:51], v[64:67]\n"
"s_waitcnt lgkmcnt(9)\n"
"v_rcp_f32 v82, v82\n"
"v_mfma_f32_16x16x32_bf16 v[60:63], a[176:179], v[52:55], v[60:63]\n"
"ds_read_b128 v[48:51], v117 offset:12288\n"
"ds_read_b64_tr_b16 a[32:33], v120 offset:32768\n"
"v_mfma_f32_16x16x32_bf16 v[64:67], a[180:183], v[52:55], v[64:67]\n"
"v_mul_f32 v83, v83, v75\n"
"v_mul_f32 v76, v68, v76\n"
"v_mfma_f32_16x16x32_bf16 v[60:63], a[184:187], v[56:59], v[60:63]\n"
"ds_read_b128 v[52:55], v115 offset:14336\n"
"ds_read_b64_tr_b16 a[34:35], v120 offset:33792\n"
"v_mfma_f32_16x16x32_bf16 v[64:67], a[188:191], v[56:59], v[64:67]\n"
"s_waitcnt lgkmcnt(9)\n"
"v_exp_f32 v83, v83\n"
"v_mfma_f32_16x16x32_bf16 v[60:63], a[192:195], v[28:31], v[60:63]\n"
"s_waitcnt vmcnt(14)\n"
"ds_read_b128 v[56:59], v117 offset:14336\n"
"ds_read_b64_tr_b16 a[36:37], v120 offset:32776\n"
"v_mfma_f32_16x16x32_bf16 v[64:67], a[196:199], v[28:31], v[64:67]\n"
"s_barrier\n"
"v_add_f32 v83, 1.0, v83\n"
"v_mul_f32 v77, v69, v77\n"
"v_mfma_f32_16x16x32_bf16 v[60:63], a[200:203], v[32:35], v[60:63]\n"
"s_add_u32 s92, s52, s45\n"
"ds_read_b128 v[28:31], v113 offset:0\n"
"ds_read_b64_tr_b16 a[38:39], v120 offset:33800\n"
"v_mfma_f32_16x16x32_bf16 v[64:67], a[204:207], v[32:35], v[64:67]\n"
"s_waitcnt lgkmcnt(10)\n"
"v_rcp_f32 v83, v83\n"
"v_mfma_f32_16x16x32_bf16 v[60:63], a[208:211], v[36:39], v[60:63]\n"
"s_add_u32 m0, s45, 32768\n"
"ds_read_b128 v[32:35], v113 offset:1024\n"
"ds_read_b64_tr_b16 a[40:41], v120 offset:40960\n"
"v_mfma_f32_16x16x32_bf16 v[64:67], a[212:215], v[36:39], v[64:67]\n"
"v_mul_f32 v78, v70, v78\n"
"v_mul_f32 v79, v71, v79\n"
"v_mfma_f32_16x16x32_bf16 v[60:63], a[216:219], v[40:43], v[60:63]\n"
"ds_read_b128 v[36:39], v113 offset:2048\n"
"buffer_load_dwordx4 v113, s[72:75], s92 offen offset:0 sc1 lds\n"
"v_mfma_f32_16x16x32_bf16 v[64:67], a[220:223], v[40:43], v[64:67]\n"
"s_waitcnt lgkmcnt(10)\n"
"ds_read_b64_tr_b16 a[42:43], v120 offset:41984\n"
"v_mul_f32 v80, v72, v80\n"
"v_mfma_f32_16x16x32_bf16 v[60:63], a[224:227], v[44:47], v[60:63]\n"
"ds_read_b128 v[40:43], v113 offset:3072\n"
"v_mul_f32 v81, v73, v81\n"
"v_mfma_f32_16x16x32_bf16 v[64:67], a[228:231], v[44:47], v[64:67]\n"
"v_mul_f32 v82, v74, v82\n"
"v_mul_f32 v83, v75, v83\n"
"v_mfma_f32_16x16x32_bf16 v[60:63], a[232:235], v[48:51], v[60:63]\n"
"ds_read_b128 v[44:47], v113 offset:4096\n"
"ds_read_b64_tr_b16 a[44:45], v120 offset:40968\n"
"v_mfma_f32_16x16x32_bf16 v[64:67], a[236:239], v[48:51], v[64:67]\n"
"s_waitcnt lgkmcnt(10)\n"
"v_cvt_pk_bf16_f32 v84, v76, v77\n"
"v_cvt_pk_bf16_f32 v85, v78, v79\n"
"v_mfma_f32_16x16x32_bf16 v[60:63], a[240:243], v[52:55], v[60:63]\n"
"ds_read_b128 v[48:51], v113 offset:5120\n"
"buffer_load_dwordx4 v113, s[72:75], s92 offen offset:1024 sc1 lds\n"
"v_mfma_f32_16x16x32_bf16 v[64:67], a[244:247], v[52:55], v[64:67]\n"
"ds_read_b64_tr_b16 a[46:47], v120 offset:41992\n"
"v_cvt_pk_bf16_f32 v86, v80, v81\n"
"v_mfma_f32_16x16x32_bf16 v[60:63], a[248:251], v[56:59], v[60:63]\n"
"ds_read_b128 v[52:55], v113 offset:6144\n"
"v_cvt_pk_bf16_f32 v87, v82, v83\n"
"v_mfma_f32_16x16x32_bf16 v[64:67], a[252:255], v[56:59], v[64:67]\n"
"s_waitcnt lgkmcnt(9)\n"
"v_mfma_f32_16x16x32_bf16 v[68:71], v[128:131], v[28:31], 0\n"
"ds_read_b128 v[56:59], v113 offset:7168\n"
"ds_read_b64_tr_b16 a[48:49], v120 offset:49152\n"
"v_mfma_f32_16x16x32_bf16 v[72:75], v[132:135], v[28:31], 0\n"
"buffer_store_dwordx4 v[84:87], v113, s[68:71], s41 offen sc1\n"
"v_mfma_f32_16x16x32_bf16 v[68:71], v[136:139], v[32:35], v[68:71]\n"
"s_add_u32 s41, s41, 1032192\n"
"ds_read_b128 v[28:31], v113 offset:8192\n"
"buffer_load_dwordx4 v113, s[72:75], s92 offen offset:2048 sc1 lds\n"
"v_mfma_f32_16x16x32_bf16 v[72:75], v[140:143], v[32:35], v[72:75]\n"
"s_waitcnt lgkmcnt(8)\n"
"ds_read_b64_tr_b16 a[50:51], v120 offset:50176\n"
"v_mul_f32 v76, v60, v60\n"
"v_mul_f32 v77, v61, v61\n"
"v_mfma_f32_16x16x32_bf16 v[68:71], v[144:147], v[36:39], v[68:71]\n"
"s_add_u32 s52, s52, 3670016\n"
"ds_read_b128 v[32:35], v113 offset:9216\n"
"v_fmamk_f32 v76, v76, 0xbdd2d3e2, v112\n"
"v_fmamk_f32 v77, v77, 0xbdd2d3e2, v112\n"
"v_mfma_f32_16x16x32_bf16 v[72:75], v[148:151], v[36:39], v[72:75]\n"
"v_mul_f32 v76, v76, v60\n"
"v_mul_f32 v77, v77, v61\n"
"v_mul_f32 v78, v62, v62\n"
"v_mfma_f32_16x16x32_bf16 v[68:71], v[152:155], v[40:43], v[68:71]\n"
"ds_read_b128 v[36:39], v113 offset:10240\n"
"ds_read_b64_tr_b16 a[52:53], v120 offset:49160\n"
"v_fmamk_f32 v78, v78, 0xbdd2d3e2, v112\n"
"v_mfma_f32_16x16x32_bf16 v[72:75], v[156:159], v[40:43], v[72:75]\n"
"s_waitcnt lgkmcnt(9)\n"
"v_exp_f32 v76, v76\n"
"v_mul_f32 v78, v78, v62\n"
"v_mfma_f32_16x16x32_bf16 v[68:71], v[160:163], v[44:47], v[68:71]\n"
"ds_read_b128 v[40:43], v113 offset:11264\n"
"ds_read_b64_tr_b16 a[54:55], v120 offset:50184\n"
"v_add_f32 v76, 1.0, v76\n"
"v_mfma_f32_16x16x32_bf16 v[72:75], v[164:167], v[44:47], v[72:75]\n"
"v_rcp_f32 v76, v76\n"
"v_mul_f32 v79, v63, v63\n"
"v_mfma_f32_16x16x32_bf16 v[68:71], v[168:171], v[48:51], v[68:71]\n"
"ds_read_b128 v[44:47], v113 offset:12288\n"
"ds_read_b64_tr_b16 a[56:57], v120 offset:57344\n"
"v_fmamk_f32 v79, v79, 0xbdd2d3e2, v112\n"
"v_mfma_f32_16x16x32_bf16 v[72:75], v[172:175], v[48:51], v[72:75]\n"
"s_waitcnt lgkmcnt(10)\n"
"v_exp_f32 v77, v77\n"
"v_mul_f32 v79, v79, v63\n"
"v_mfma_f32_16x16x32_bf16 v[68:71], v[176:179], v[52:55], v[68:71]\n"
"ds_read_b128 v[48:51], v113 offset:13312\n"
"ds_read_b64_tr_b16 a[58:59], v120 offset:58368\n"
"v_add_f32 v77, 1.0, v77\n"
"v_mfma_f32_16x16x32_bf16 v[72:75], v[180:183], v[52:55], v[72:75]\n"
"v_rcp_f32 v77, v77\n"
"v_mul_f32 v80, v64, v64\n"
"v_mfma_f32_16x16x32_bf16 v[68:71], v[184:187], v[56:59], v[68:71]\n"
"ds_read_b128 v[52:55], v113 offset:14336\n"
"ds_read_b64_tr_b16 a[60:61], v120 offset:57352\n"
"v_fmamk_f32 v80, v80, 0xbdd2d3e2, v112\n"
"v_mfma_f32_16x16x32_bf16 v[72:75], v[188:191], v[56:59], v[72:75]\n"
"s_waitcnt lgkmcnt(10)\n"
"v_exp_f32 v78, v78\n"
"v_mul_f32 v80, v80, v64\n"
"v_mfma_f32_16x16x32_bf16 v[68:71], v[192:195], v[28:31], v[68:71]\n"
"s_waitcnt vmcnt(9)\n"
"ds_read_b128 v[56:59], v113 offset:15360\n"
"ds_read_b64_tr_b16 a[62:63], v120 offset:58376\n"
"v_add_f32 v78, 1.0, v78\n"
"v_mfma_f32_16x16x32_bf16 v[72:75], v[196:199], v[28:31], v[72:75]\n"
"s_barrier\n"
"v_rcp_f32 v78, v78\n"
"v_mul_f32 v81, v65, v65\n"
"v_mfma_f32_16x16x32_bf16 v[68:71], v[200:203], v[32:35], v[68:71]\n"
"s_add_u32 s92, s53, s44\n"
"ds_read_b128 v[28:31], v113 offset:16384\n"
"ds_read_b64_tr_b16 a[64:65], v121 offset:0\n"
"v_fmamk_f32 v81, v81, 0xbdd2d3e2, v112\n"
"v_mfma_f32_16x16x32_bf16 v[72:75], v[204:207], v[32:35], v[72:75]\n"
"s_waitcnt lgkmcnt(11)\n"
"v_exp_f32 v79, v79\n"
"v_mul_f32 v81, v81, v65\n"
"v_mfma_f32_16x16x32_bf16 v[68:71], v[208:211], v[36:39], v[68:71]\n"
"s_add_u32 m0, s44, 65536\n"
"ds_read_b128 v[32:35], v113 offset:17408\n"
"ds_read_b64_tr_b16 a[66:67], v121 offset:1024\n"
"v_add_f32 v79, 1.0, v79\n"
"v_mfma_f32_16x16x32_bf16 v[72:75], v[212:215], v[36:39], v[72:75]\n"
"v_rcp_f32 v79, v79\n"
"v_mul_f32 v82, v66, v66\n"
"v_mfma_f32_16x16x32_bf16 v[68:71], v[216:219], v[40:43], v[68:71]\n"
"ds_read_b128 v[36:39], v113 offset:18432\n"
"buffer_load_dwordx4 v113, s[72:75], s92 offen offset:0 sc1 lds\n"
"v_fmamk_f32 v82, v82, 0xbdd2d3e2, v112\n"
"v_mfma_f32_16x16x32_bf16 v[72:75], v[220:223], v[40:43], v[72:75]\n"
"s_waitcnt lgkmcnt(10)\n"
"ds_read_b64_tr_b16 a[68:69], v121 offset:8\n"
"v_exp_f32 v80, v80\n"
"v_mfma_f32_16x16x32_bf16 v[68:71], v[224:227], v[44:47], v[68:71]\n"
"ds_read_b128 v[40:43], v113 offset:19456\n"
"v_add_f32 v80, 1.0, v80\n"
"v_mfma_f32_16x16x32_bf16 v[72:75], v[228:231], v[44:47], v[72:75]\n"
"ds_read_b64_tr_b16 a[70:71], v121 offset:1032\n"
"v_mul_f32 v82, v82, v66\n"
"v_mfma_f32_16x16x32_bf16 v[68:71], v[232:235], v[48:51], v[68:71]\n"
"ds_read_b128 v[44:47], v113 offset:20480\n"
"buffer_load_dwordx4 v113, s[72:75], s92 offen offset:1024 sc1 lds\n"
"v_mfma_f32_16x16x32_bf16 v[72:75], v[236:239], v[48:51], v[72:75]\n"
"s_waitcnt lgkmcnt(10)\n"
"v_rcp_f32 v80, v80\n"
"v_mfma_f32_16x16x32_bf16 v[68:71], v[240:243], v[52:55], v[68:71]\n"
"ds_read_b128 v[48:51], v113 offset:21504\n"
"ds_read_b64_tr_b16 a[72:73], v121 offset:8192\n"
"v_mfma_f32_16x16x32_bf16 v[72:75], v[244:247], v[52:55], v[72:75]\n"
"v_exp_f32 v81, v81\n"
"v_mfma_f32_16x16x32_bf16 v[68:71], v[248:251], v[56:59], v[68:71]\n"
"ds_read_b128 v[52:55], v113 offset:22528\n"
"buffer_load_dwordx4 v113, s[72:75], s92 offen offset:2048 sc1 lds\n"
"v_mfma_f32_16x16x32_bf16 v[72:75], v[252:255], v[56:59], v[72:75]\n"
"s_waitcnt lgkmcnt(9)\n"
"ds_read_b64_tr_b16 a[74:75], v121 offset:9216\n"
"v_add_f32 v81, 1.0, v81\n"
"v_mfma_f32_16x16x32_bf16 v[68:71], a[0:3], v[28:31], v[68:71]\n"
"ds_read_b128 v[56:59], v113 offset:23552\n"
"v_mul_f32 v83, v67, v67\n"
"v_mfma_f32_16x16x32_bf16 v[72:75], a[4:7], v[28:31], v[72:75]\n"
"ds_read_b64_tr_b16 a[76:77], v121 offset:8200\n"
"v_fmamk_f32 v83, v83, 0xbdd2d3e2, v112\n"
"v_mfma_f32_16x16x32_bf16 v[68:71], a[8:11], v[32:35], v[68:71]\n"
"ds_read_b128 v[28:31], v113 offset:24576\n"
"buffer_load_dwordx4 v113, s[72:75], s92 offen offset:3072 sc1 lds\n"
"v_mfma_f32_16x16x32_bf16 v[72:75], a[12:15], v[32:35], v[72:75]\n"
"s_waitcnt lgkmcnt(9)\n"
"v_rcp_f32 v81, v81\n"
"v_mfma_f32_16x16x32_bf16 v[68:71], a[16:19], v[36:39], v[68:71]\n"
"ds_read_b128 v[32:35], v113 offset:25600\n"
"ds_read_b64_tr_b16 a[78:79], v121 offset:9224\n"
"v_mfma_f32_16x16x32_bf16 v[72:75], a[20:23], v[36:39], v[72:75]\n"
"v_exp_f32 v82, v82\n"
"v_mfma_f32_16x16x32_bf16 v[68:71], a[24:27], v[40:43], v[68:71]\n"
"ds_read_b128 v[36:39], v113 offset:26624\n"
"v_add_f32 v82, 1.0, v82\n"
"v_mfma_f32_16x16x32_bf16 v[72:75], a[28:31], v[40:43], v[72:75]\n"
"s_waitcnt lgkmcnt(9)\n"
"v_rcp_f32 v82, v82\n"
"v_mfma_f32_16x16x32_bf16 v[68:71], a[32:35], v[44:47], v[68:71]\n"
"s_waitcnt vmcnt(8)\n"
"ds_read_b128 v[40:43], v113 offset:27648\n"
"v_mul_f32 v83, v83, v67\n"
"v_mfma_f32_16x16x32_bf16 v[72:75], a[36:39], v[44:47], v[72:75]\n"
"s_barrier\n"
"v_exp_f32 v83, v83\n"
"v_mfma_f32_16x16x32_bf16 v[68:71], a[40:43], v[48:51], v[68:71]\n"
"s_add_u32 s92, s53, s45\n"
"ds_read_b128 v[44:47], v113 offset:32768\n"
"v_readfirstlane_b32 s93, v2\n"
"v_mfma_f32_16x16x32_bf16 v[72:75], a[44:47], v[48:51], v[72:75]\n"
"s_waitcnt lgkmcnt(7)\n"
"s_add_u32 m0, s45, -16384\n"
"v_add_f32 v83, 1.0, v83\n"
"v_mfma_f32_16x16x32_bf16 v[68:71], a[48:51], v[52:55], v[68:71]\n"
"s_cmp_lt_u32 s93, s49\n"
"s_cbranch_scc1 Lslow_h1_K\n"
"Lret_h1_K:\n"
"ds_read_b128 v[48:51], v113 offset:33792\n"
"v_mfma_f32_16x16x32_bf16 v[72:75], a[52:55], v[52:55], v[72:75]\n"
"buffer_load_dwordx4 v113, s[72:75], s92 offen offset:0 sc1 lds\n"
"v_mul_f32 v76, v60, v76\n"
"v_mfma_f32_16x16x32_bf16 v[68:71], a[56:59], v[56:59], v[68:71]\n"
"ds_read_b128 v[52:55], v113 offset:34816\n"
"v_mul_f32 v77, v61, v77\n"
"v_mfma_f32_16x16x32_bf16 v[72:75], a[60:63], v[56:59], v[72:75]\n"
"s_waitcnt lgkmcnt(6)\n"
"s_waitcnt lgkmcnt(5)\n"
"v_rcp_f32 v83, v83\n"
"v_mfma_f32_16x16x32_bf16 v[68:71], a[64:67], v[28:31], v[68:71]\n"
"ds_read_b128 v[56:59], v113 offset:35840\n"
"v_mul_f32 v78, v62, v78\n"
"v_mfma_f32_16x16x32_bf16 v[72:75], a[68:71], v[28:31], v[72:75]\n"
"v_mul_f32 v79, v63, v79\n"
"v_mul_f32 v80, v64, v80\n"
"v_mfma_f32_16x16x32_bf16 v[68:71], a[72:75], v[32:35], v[68:71]\n"
"ds_read_b128 v[28:31], v113 offset:36864\n"
"v_mul_f32 v81, v65, v81\n"
"v_mfma_f32_16x16x32_bf16 v[72:75], a[76:79], v[32:35], v[72:75]\n"
"s_waitcnt lgkmcnt(5)\n"
"v_mul_f32 v82, v66, v82\n"
"v_mul_f32 v83, v67, v83\n"
"v_mfma_f32_16x16x32_bf16 v[68:71], v[4:7], v[36:39], v[68:71]\n"
"ds_read_b128 v[32:35], v113 offset:37888\n"
"buffer_load_dwordx4 v113, s[72:75], s92 offen offset:1024 sc1 lds\n"
"v_mfma_f32_16x16x32_bf16 v[72:75], v[8:11], v[36:39], v[72:75]\n"
"v_cvt_pk_bf16_f32 v84, v76, v77\n"
"v_cvt_pk_bf16_f32 v85, v78, v79\n"
"v_mfma_f32_16x16x32_bf16 v[68:71], v[12:15], v[40:43], v[68:71]\n"
"ds_read_b128 v[36:39], v113 offset:38912\n"
"v_cvt_pk_bf16_f32 v86, v80, v81\n"
"v_mfma_f32_16x16x32_bf16 v[72:75], v[16:19], v[40:43], v[72:75]\n"
"s_waitcnt lgkmcnt(5)\n"
"v_cvt_pk_bf16_f32 v87, v82, v83\n"
"v_mfma_f32_16x16x32_bf16 v[60:63], v[128:131], v[44:47], 0\n"
"ds_read_b128 v[40:43], v113 offset:39936\n"
"v_mfma_f32_16x16x32_bf16 v[64:67], v[132:135], v[44:47], 0\n"
"buffer_store_dwordx4 v[84:87], v113, s[68:71], s41 offen sc1\n"
"v_mov_b32 v23, 0xfffefffe\n"
"v_mfma_f32_16x16x32_bf16 v[60:63], v[136:139], v[48:51], v[60:63]\n"
"s_add_u32 s41, s41, 1032192\n"
"ds_read_b128 v[44:47], v113 offset:40960\n"
"buffer_load_dwordx4 v113, s[72:75], s92 offen offset:2048 sc1 lds\n"
"v_mfma_f32_16x16x32_bf16 v[64:67], v[140:143], v[48:51], v[64:67]\n"
"s_waitcnt lgkmcnt(5)\n"
"v_mfma_f32_16x16x32_bf16 v[60:63], v[144:147], v[52:55], v[60:63]\n"
"s_add_u32 s53, s53, 3670016\n"
"ds_read_b128 v[48:51], v113 offset:41984\n"
"v_cvt_pk_bf16_f32 v24, v68, v69\n"
"v_mfma_f32_16x16x32_bf16 v[64:67], v[148:151], v[52:55], v[64:67]\n"
"v_cvt_pk_bf16_f32 v25, v70, v71\n"
"v_cvt_pk_bf16_f32 v26, v72, v73\n"
"v_mfma_f32_16x16x32_bf16 v[60:63], v[152:155], v[56:59], v[60:63]\n"
"ds_read_b128 v[52:55], v113 offset:43008\n"
"v_cvt_pk_bf16_f32 v27, v74, v75\n"
"v_mfma_f32_16x16x32_bf16 v[64:67], v[156:159], v[56:59], v[64:67]\n"
"s_waitcnt lgkmcnt(5)\n"
"v_and_or_b32 v24, v24, v23, s37\n"
"v_and_or_b32 v25, v25, v23, s37\n"
"v_mfma_f32_16x16x32_bf16 v[60:63], v[160:163], v[28:31], v[60:63]\n"
"ds_read_b128 v[56:59], v113 offset:44032\n"
"v_and_or_b32 v26, v26, v23, s37\n"
"v_mfma_f32_16x16x32_bf16 v[64:67], v[164:167], v[28:31], v[64:67]\n"
"v_and_or_b32 v27, v27, v23, s37\n"
"v_mfma_f32_16x16x32_bf16 v[60:63], v[168:171], v[32:35], v[60:63]\n"
"buffer_store_dwordx4 v[24:27], v113, s[72:75], s55 offen sc1\n"
"ds_read_b128 v[28:31], v113 offset:45056\n"
"v_mfma_f32_16x16x32_bf16 v[64:67], v[172:175], v[32:35], v[64:67]\n"
"s_waitcnt lgkmcnt(5)\n"
"v_mfma_f32_16x16x32_bf16 v[60:63], v[176:179], v[36:39], v[60:63]\n"
"s_add_u32 s55, s55, 3145728\n"
"ds_read_b128 v[32:35], v113 offset:46080\n"
"v_accvgpr_write_b32 a80, v4\n"
"v_mfma_f32_16x16x32_bf16 v[64:67], v[180:183], v[36:39], v[64:67]\n"
"v_accvgpr_write_b32 a81, v5\n"
"v_accvgpr_write_b32 a82, v6\n"
"v_mfma_f32_16x16x32_bf16 v[60:63], v[184:187], v[40:43], v[60:63]\n"
"ds_read_b128 v[36:39], v113 offset:47104\n"
"v_accvgpr_write_b32 a83, v7\n"
"v_mfma_f32_16x16x32_bf16 v[64:67], v[188:191], v[40:43], v[64:67]\n"
"s_waitcnt lgkmcnt(5)\n"
"v_accvgpr_write_b32 a84, v8\n"
"v_accvgpr_write_b32 a85, v9\n"
"v_mfma_f32_16x16x32_bf16 v[60:63], v[192:195], v[44:47], v[60:63]\n"
"s_waitcnt vmcnt(9)\n"
"ds_read_b128 v[40:43], v113 offset:48128\n"
"v_accvgpr_write_b32 a86, v10\n"
"v_mfma_f32_16x16x32_bf16 v[64:67], v[196:199], v[44:47], v[64:67]\n"
"s_barrier\n"
"v_accvgpr_write_b32 a87, v11\n"
"v_accvgpr_write_b32 a88, v12\n"
"v_mfma_f32_16x16x32_bf16 v[60:63], v[200:203], v[48:51], v[60:63]\n"
"s_add_u32 s92, s51, s44\n"
"ds_read_b128 v[44:47], v113 offset:49152\n"
"v_accvgpr_write_b32 a89, v13\n"
"v_mfma_f32_16x16x32_bf16 v[64:67], v[204:207], v[48:51], v[64:67]\n"
"s_waitcnt lgkmcnt(5)\n"
"v_accvgpr_write_b32 a90, v14\n"
"v_accvgpr_write_b32 a91, v15\n"
"v_mfma_f32_16x16x32_bf16 v[60:63], v[208:211], v[52:55], v[60:63]\n"
"s_add_u32 m0, s44, 16384\n"
"ds_read_b128 v[48:51], v113 offset:50176\n"
"v_accvgpr_write_b32 a92, v16\n"
"v_mfma_f32_16x16x32_bf16 v[64:67], v[212:215], v[52:55], v[64:67]\n"
"v_accvgpr_write_b32 a93, v17\n"
"v_accvgpr_write_b32 a94, v18\n"
"v_mfma_f32_16x16x32_bf16 v[60:63], v[216:219], v[56:59], v[60:63]\n"
"ds_read_b128 v[52:55], v113 offset:51200\n"
"buffer_load_dwordx4 v113, s[72:75], s92 offen offset:0 sc1 lds\n"
"v_mfma_f32_16x16x32_bf16 v[64:67], v[220:223], v[56:59], v[64:67]\n"
"s_waitcnt lgkmcnt(5)\n"
"v_accvgpr_write_b32 a95, v19\n"
"v_mfma_f32_16x16x32_bf16 v[60:63], v[224:227], v[28:31], v[60:63]\n"
"ds_read_b128 v[56:59], v113 offset:52224\n"
"v_mfma_f32_16x16x32_bf16 v[64:67], v[228:231], v[28:31], v[64:67]\n"
"v_mfma_f32_16x16x32_bf16 v[60:63], v[232:235], v[32:35], v[60:63]\n"
"ds_read_b128 v[28:31], v113 offset:53248\n"
"buffer_load_dwordx4 v113, s[72:75], s92 offen offset:1024 sc1 lds\n"
"v_mfma_f32_16x16x32_bf16 v[64:67], v[236:239], v[32:35], v[64:67]\n"
"s_waitcnt lgkmcnt(5)\n"
"v_mfma_f32_16x16x32_bf16 v[60:63], v[240:243], v[36:39], v[60:63]\n"
"ds_read_b128 v[32:35], v113 offset:54272\n"
"v_mfma_f32_16x16x32_bf16 v[64:67], v[244:247], v[36:39], v[64:67]\n"
"v_mfma_f32_16x16x32_bf16 v[60:63], v[248:251], v[40:43], v[60:63]\n"
"s_getpc_b64 s[62:63]\n"
"ds_read_b128 v[36:39], v113 offset:55296\n"
"buffer_load_dwordx4 v113, s[72:75], s92 offen offset:2048 sc1 lds\n"
"v_mfma_f32_16x16x32_bf16 v[64:67], v[252:255], v[40:43], v[64:67]\n"
"s_waitcnt lgkmcnt(5)\n"
"global_load_dword v21, v119, s[62:63] offset:3072\n"
"v_mfma_f32_16x16x32_bf16 v[60:63], a[0:3], v[44:47], v[60:63]\n"
"ds_read_b128 v[40:43], v113 offset:56320\n"
"v_mfma_f32_16x16x32_bf16 v[64:67], a[4:7], v[44:47], v[64:67]\n"
"v_mfma_f32_16x16x32_bf16 v[60:63], a[8:11], v[48:51], v[60:63]\n"
"ds_read_b128 v[44:47], v113 offset:57344\n"
"buffer_load_dwordx4 v113, s[72:75], s92 offen offset:3072 sc1 lds\n"
"v_mfma_f32_16x16x32_bf16 v[64:67], a[12:15], v[48:51], v[64:67]\n"
"s_waitcnt lgkmcnt(5)\n"
"v_mfma_f32_16x16x32_bf16 v[60:63], a[16:19], v[52:55], v[60:63]\n"
"ds_read_b128 v[48:51], v113 offset:58368\n"
"v_mfma_f32_16x16x32_bf16 v[64:67], a[20:23], v[52:55], v[64:67]\n"
"v_mfma_f32_16x16x32_bf16 v[60:63], a[24:27], v[56:59], v[60:63]\n"
"ds_read_b128 v[52:55], v113 offset:59392\n"
"v_mfma_f32_16x16x32_bf16 v[64:67], a[28:31], v[56:59], v[64:67]\n"
"s_waitcnt lgkmcnt(5)\n"
"v_mfma_f32_16x16x32_bf16 v[60:63], a[32:35], v[28:31], v[60:63]\n"
"s_waitcnt vmcnt(10)\n"
"ds_read_b128 v[56:59], v113 offset:60416\n"
"v_mfma_f32_16x16x32_bf16 v[64:67], a[36:39], v[28:31], v[64:67]\n"
"s_barrier\n"
"v_mfma_f32_16x16x32_bf16 v[60:63], a[40:43], v[32:35], v[60:63]\n"
"s_add_u32 s92, s51, s45\n"
"ds_read_b128 v[28:31], v118 offset:0\n"
"v_mfma_f32_16x16x32_bf16 v[64:67], a[44:47], v[32:35], v[64:67]\n"
"s_waitcnt lgkmcnt(5)\n"
"v_mfma_f32_16x16x32_bf16 v[60:63], a[48:51], v[36:39], v[60:63]\n"
"s_add_u32 m0, s45, 16384\n"
"ds_read_b128 v[32:35], v118 offset:1024\n"
"v_mfma_f32_16x16x32_bf16 v[64:67], a[52:55], v[36:39], v[64:67]\n"
"v_mfma_f32_16x16x32_bf16 v[60:63], a[56:59], v[40:43], v[60:63]\n"
"ds_read_b128 v[36:39], v118 offset:2048\n"
"buffer_load_dwordx4 v113, s[72:75], s92 offen offset:0 sc1 lds\n"
"v_mfma_f32_16x16x32_bf16 v[64:67], a[60:63], v[40:43], v[64:67]\n"
"s_waitcnt lgkmcnt(5)\n"
"v_mfma_f32_16x16x32_bf16 v[60:63], a[64:67], v[44:47], v[60:63]\n"
"ds_read_b128 v[40:43], v118 offset:3072\n"
"v_mfma_f32_16x16x32_bf16 v[64:67], a[68:71], v[44:47], v[64:67]\n"
"v_mfma_f32_16x16x32_bf16 v[60:63], a[72:75], v[48:51], v[60:63]\n"
"ds_read_b128 v[44:47], v118 offset:4096\n"
"v_mfma_f32_16x16x32_bf16 v[64:67], a[76:79], v[48:51], v[64:67]\n"
"s_waitcnt lgkmcnt(5)\n"
"v_mfma_f32_16x16x32_bf16 v[60:63], v[4:7], v[52:55], v[60:63]\n"
"ds_read_b128 v[48:51], v118 offset:5120\n"
"buffer_load_dwordx4 v113, s[72:75], s92 offen offset:1024 sc1 lds\n"
"v_mfma_f32_16x16x32_bf16 v[64:67], v[8:11], v[52:55], v[64:67]\n"
"v_mfma_f32_16x16x32_bf16 v[60:63], v[12:15], v[56:59], v[60:63]\n"
"ds_read_b128 v[52:55], v118 offset:6144\n"
"v_mfma_f32_16x16x32_bf16 v[64:67], v[16:19], v[56:59], v[64:67]\n"
"s_waitcnt lgkmcnt(5)\n"
"v_mfma_f32_16x16x32_bf16 v[68:71], v[128:131], v[28:31], 0\n"
"ds_read_b128 v[56:59], v118 offset:7168\n"
"v_mfma_f32_16x16x32_bf16 v[72:75], v[132:135], v[28:31], 0\n"
"v_mfma_f32_16x16x32_bf16 v[68:71], v[136:139], v[32:35], v[68:71]\n"
"ds_read_b128 v[28:31], v118 offset:8192\n"
"buffer_load_dwordx4 v113, s[72:75], s92 offen offset:2048 sc1 lds\n"
"v_mfma_f32_16x16x32_bf16 v[72:75], v[140:143], v[32:35], v[72:75]\n"
"s_waitcnt lgkmcnt(5)\n"
"v_mfma_f32_16x16x32_bf16 v[68:71], v[144:147], v[36:39], v[68:71]\n"
"s_add_u32 s51, s51, 3670016\n"
"ds_read_b128 v[32:35], v118 offset:9216\n"
"v_cvt_pk_bf16_f32 v24, v60, v61\n"
"v_mfma_f32_16x16x32_bf16 v[72:75], v[148:151], v[36:39], v[72:75]\n"
"v_cvt_pk_bf16_f32 v25, v62, v63\n"
"v_cvt_pk_bf16_f32 v26, v64, v65\n"
"v_mfma_f32_16x16x32_bf16 v[68:71], v[152:155], v[40:43], v[68:71]\n"
"ds_read_b128 v[36:39], v118 offset:10240\n"
"v_cvt_pk_bf16_f32 v27, v66, v67\n"
"v_mfma_f32_16x16x32_bf16 v[72:75], v[156:159], v[40:43], v[72:75]\n"
"s_waitcnt lgkmcnt(5)\n"
"v_and_or_b32 v24, v24, v23, s37\n"
"v_and_or_b32 v25, v25, v23, s37\n"
"v_mfma_f32_16x16x32_bf16 v[68:71], v[160:163], v[44:47], v[68:71]\n"
"ds_read_b128 v[40:43], v118 offset:11264\n"
"v_and_or_b32 v26, v26, v23, s37\n"
"v_mfma_f32_16x16x32_bf16 v[72:75], v[164:167], v[44:47], v[72:75]\n"
"v_and_or_b32 v27, v27, v23, s37\n"
"v_mfma_f32_16x16x32_bf16 v[68:71], v[168:171], v[48:51], v[68:71]\n"
"buffer_store_dwordx4 v[24:27], v113, s[72:75], s56 offen sc1\n"
"ds_read_b128 v[44:47], v118 offset:12288\n"
"v_mfma_f32_16x16x32_bf16 v[72:75], v[172:175], v[48:51], v[72:75]\n"
"s_waitcnt lgkmcnt(5)\n"
"v_mfma_f32_16x16x32_bf16 v[68:71], v[176:179], v[52:55], v[68:71]\n"
"s_add_u32 s56, s56, 3145728\n"
"ds_read_b128 v[48:51], v118 offset:13312\n"
"v_mfma_f32_16x16x32_bf16 v[72:75], v[180:183], v[52:55], v[72:75]\n"
"v_mfma_f32_16x16x32_bf16 v[68:71], v[184:187], v[56:59], v[68:71]\n"
"ds_read_b128 v[52:55], v118 offset:14336\n"
"buffer_load_dwordx4 v[4:7], v104, s[72:75], 0 offen offset:0 sc1\n"
"v_mfma_f32_16x16x32_bf16 v[72:75], v[188:191], v[56:59], v[72:75]\n"
"s_waitcnt lgkmcnt(5)\n"
"v_mfma_f32_16x16x32_bf16 v[68:71], v[192:195], v[28:31], v[68:71]\n"
"s_waitcnt vmcnt(11)\n"
"ds_read_b128 v[56:59], v118 offset:15360\n"
"buffer_load_dwordx4 v[8:11], v104, s[72:75], 0 offen offset:16 sc1\n"
"v_mfma_f32_16x16x32_bf16 v[72:75], v[196:199], v[28:31], v[72:75]\n"
"s_barrier\n"
"v_mfma_f32_16x16x32_bf16 v[68:71], v[200:203], v[32:35], v[68:71]\n"
"s_add_u32 s92, s52, s44\n"
"ds_read_b128 v[28:31], v113 offset:0\n"
"buffer_load_dword v2, v0, s[72:75], 0 offen offset:512 sc1\n"
"v_mfma_f32_16x16x32_bf16 v[72:75], v[204:207], v[32:35], v[72:75]\n"
"s_waitcnt lgkmcnt(5)\n"
"global_load_dwordx4 v[100:103], v96, s[90:91]\n"
"v_mfma_f32_16x16x32_bf16 v[68:71], v[208:211], v[36:39], v[68:71]\n"
"s_add_u32 m0, s44, 49152\n"
"ds_read_b128 v[32:35], v113 offset:1024\n"
"v_mov_b32 v16, 0x8000\n"
"v_mfma_f32_16x16x32_bf16 v[72:75], v[212:215], v[36:39], v[72:75]\n"
"v_mov_b32 v17, 0xffff0000\n"
"v_mfma_f32_16x16x32_bf16 v[68:71], v[216:219], v[40:43], v[68:71]\n"
"ds_read_b128 v[36:39], v113 offset:2048\n"
"buffer_load_dwordx4 v113, s[72:75], s92 offen offset:0 sc1 lds\n"
"v_mfma_f32_16x16x32_bf16 v[72:75], v[220:223], v[40:43], v[72:75]\n"
"s_waitcnt lgkmcnt(5)\n"
"v_mfma_f32_16x16x32_bf16 v[68:71], v[224:227], v[44:47], v[68:71]\n"
"ds_read_b128 v[40:43], v113 offset:3072\n"
"v_mfma_f32_16x16x32_bf16 v[72:75], v[228:231], v[44:47], v[72:75]\n"
"v_mfma_f32_16x16x32_bf16 v[68:71], v[232:235], v[48:51], v[68:71]\n"
"ds_read_b128 v[44:47], v113 offset:4096\n"
"buffer_load_dwordx4 v113, s[72:75], s92 offen offset:1024 sc1 lds\n"
"v_mfma_f32_16x16x32_bf16 v[72:75], v[236:239], v[48:51], v[72:75]\n"
"s_waitcnt lgkmcnt(5)\n"
"v_mfma_f32_16x16x32_bf16 v[68:71], v[240:243], v[52:55], v[68:71]\n"
"ds_read_b128 v[48:51], v113 offset:5120\n"
"v_mfma_f32_16x16x32_bf16 v[72:75], v[244:247], v[52:55], v[72:75]\n"
"v_mfma_f32_16x16x32_bf16 v[68:71], v[248:251], v[56:59], v[68:71]\n"
"ds_read_b128 v[52:55], v113 offset:6144\n"
"buffer_load_dwordx4 v113, s[72:75], s92 offen offset:2048 sc1 lds\n"
"v_mfma_f32_16x16x32_bf16 v[72:75], v[252:255], v[56:59], v[72:75]\n"
"s_waitcnt lgkmcnt(5)\n"
"v_mfma_f32_16x16x32_bf16 v[68:71], a[0:3], v[28:31], v[68:71]\n"
"ds_read_b128 v[56:59], v113 offset:7168\n"
"v_mfma_f32_16x16x32_bf16 v[72:75], a[4:7], v[28:31], v[72:75]\n"
"v_mfma_f32_16x16x32_bf16 v[68:71], a[8:11], v[32:35], v[68:71]\n"
"ds_read_b128 v[28:31], v113 offset:8192\n"
"buffer_load_dwordx4 v113, s[72:75], s92 offen offset:3072 sc1 lds\n"
"v_mfma_f32_16x16x32_bf16 v[72:75], a[12:15], v[32:35], v[72:75]\n"
"s_waitcnt lgkmcnt(5)\n"
"v_mfma_f32_16x16x32_bf16 v[68:71], a[16:19], v[36:39], v[68:71]\n"
"ds_read_b128 v[32:35], v113 offset:9216\n"
"v_mfma_f32_16x16x32_bf16 v[72:75], a[20:23], v[36:39], v[72:75]\n"
"v_mfma_f32_16x16x32_bf16 v[68:71], a[24:27], v[40:43], v[68:71]\n"
"ds_read_b128 v[36:39], v113 offset:10240\n"
"v_mfma_f32_16x16x32_bf16 v[72:75], a[28:31], v[40:43], v[72:75]\n"
"s_waitcnt lgkmcnt(5)\n"
"v_mfma_f32_16x16x32_bf16 v[68:71], a[32:35], v[44:47], v[68:71]\n"
"s_waitcnt vmcnt(12)\n"
"ds_read_b128 v[40:43], v113 offset:11264\n"
"v_mfma_f32_16x16x32_bf16 v[72:75], a[36:39], v[44:47], v[72:75]\n"
"s_barrier\n"
"v_mfma_f32_16x16x32_bf16 v[68:71], a[40:43], v[48:51], v[68:71]\n"
"s_add_u32 s92, s52, s45\n"
"buffer_atomic_add v1, v0, s[72:75], 0 offen offset:768\n"
"ds_read_b128 v[44:47], v113 offset:16384\n"
"v_mfma_f32_16x16x32_bf16 v[72:75], a[44:47], v[48:51], v[72:75]\n"
"s_waitcnt lgkmcnt(5)\n"
"v_mfma_f32_16x16x32_bf16 v[68:71], a[48:51], v[52:55], v[68:71]\n"
"s_add_u32 m0, s45, 49152\n"
"ds_read_b128 v[48:51], v113 offset:17408\n"
"v_mfma_f32_16x16x32_bf16 v[72:75], a[52:55], v[52:55], v[72:75]\n"
"v_mfma_f32_16x16x32_bf16 v[68:71], a[56:59], v[56:59], v[68:71]\n"
"ds_read_b128 v[52:55], v113 offset:18432\n"
"buffer_load_dwordx4 v113, s[72:75], s92 offen offset:0 sc1 lds\n"
"v_mfma_f32_16x16x32_bf16 v[72:75], a[60:63], v[56:59], v[72:75]\n"
"s_waitcnt lgkmcnt(5)\n"
"v_mfma_f32_16x16x32_bf16 v[68:71], a[64:67], v[28:31], v[68:71]\n"
"ds_read_b128 v[56:59], v113 offset:19456\n"
"v_mfma_f32_16x16x32_bf16 v[72:75], a[68:71], v[28:31], v[72:75]\n"
"v_mfma_f32_16x16x32_bf16 v[68:71], a[72:75], v[32:35], v[68:71]\n"
"ds_read_b128 v[28:31], v113 offset:20480\n"
"v_mfma_f32_16x16x32_bf16 v[72:75], a[76:79], v[32:35], v[72:75]\n"
"s_waitcnt lgkmcnt(5)\n"
"v_mfma_f32_16x16x32_bf16 v[68:71], a[80:83], v[36:39], v[68:71]\n"
"ds_read_b128 v[32:35], v113 offset:21504\n"
"buffer_load_dwordx4 v113, s[72:75], s92 offen offset:1024 sc1 lds\n"
"v_mfma_f32_16x16x32_bf16 v[72:75], a[84:87], v[36:39], v[72:75]\n"
"v_mfma_f32_16x16x32_bf16 v[68:71], a[88:91], v[40:43], v[68:71]\n"
"ds_read_b128 v[36:39], v113 offset:22528\n"
"v_mfma_f32_16x16x32_bf16 v[72:75], a[92:95], v[40:43], v[72:75]\n"
"s_waitcnt lgkmcnt(5)\n"
"v_mfma_f32_16x16x32_bf16 v[60:63], v[128:131], v[44:47], 0\n"
"ds_read_b128 v[40:43], v113 offset:23552\n"
"v_mfma_f32_16x16x32_bf16 v[64:67], v[132:135], v[44:47], 0\n"
"v_mfma_f32_16x16x32_bf16 v[60:63], v[136:139], v[48:51], v[60:63]\n"
"ds_read_b128 v[44:47], v113 offset:24576\n"
"buffer_load_dwordx4 v113, s[72:75], s92 offen offset:2048 sc1 lds\n"
"v_mfma_f32_16x16x32_bf16 v[64:67], v[140:143], v[48:51], v[64:67]\n"
"s_waitcnt lgkmcnt(5)\n"
"v_mfma_f32_16x16x32_bf16 v[60:63], v[144:147], v[52:55], v[60:63]\n"
"s_add_u32 s52, s52, 3670016\n"
"ds_read_b128 v[48:51], v113 offset:25600\n"
"v_cvt_pk_bf16_f32 v24, v68, v69\n"
"v_mfma_f32_16x16x32_bf16 v[64:67], v[148:151], v[52:55], v[64:67]\n"
"v_cvt_pk_bf16_f32 v25, v70, v71\n"
"v_cvt_pk_bf16_f32 v26, v72, v73\n"
"v_mfma_f32_16x16x32_bf16 v[60:63], v[152:155], v[56:59], v[60:63]\n"
"ds_read_b128 v[52:55], v113 offset:26624\n"
"v_cvt_pk_bf16_f32 v27, v74, v75\n"
"v_mfma_f32_16x16x32_bf16 v[64:67], v[156:159], v[56:59], v[64:67]\n"
"s_waitcnt lgkmcnt(5)\n"
"v_and_or_b32 v24, v24, v23, s37\n"
"v_and_or_b32 v25, v25, v23, s37\n"
"v_mfma_f32_16x16x32_bf16 v[60:63], v[160:163], v[28:31], v[60:63]\n"
"ds_read_b128 v[56:59], v113 offset:27648\n"
"v_and_or_b32 v26, v26, v23, s37\n"
"v_mfma_f32_16x16x32_bf16 v[64:67], v[164:167], v[28:31], v[64:67]\n"
"v_and_or_b32 v27, v27, v23, s37\n"
"v_mfma_f32_16x16x32_bf16 v[60:63], v[168:171], v[32:35], v[60:63]\n"
"buffer_store_dwordx4 v[24:27], v113, s[72:75], s57 offen sc1\n"
"ds_read_b128 v[28:31], v113 offset:28672\n"
"v_mfma_f32_16x16x32_bf16 v[64:67], v[172:175], v[32:35], v[64:67]\n"
"s_waitcnt lgkmcnt(5)\n"
"v_mfma_f32_16x16x32_bf16 v[60:63], v[176:179], v[36:39], v[60:63]\n"
"s_add_u32 s57, s57, 3145728\n"
"ds_read_b128 v[32:35], v113 offset:29696\n"
"v_mfma_f32_16x16x32_bf16 v[64:67], v[180:183], v[36:39], v[64:67]\n"
"v_mfma_f32_16x16x32_bf16 v[60:63], v[184:187], v[40:43], v[60:63]\n"
"ds_read_b128 v[36:39], v113 offset:30720\n"
"v_mfma_f32_16x16x32_bf16 v[64:67], v[188:191], v[40:43], v[64:67]\n"
"s_waitcnt lgkmcnt(5)\n"
"v_mfma_f32_16x16x32_bf16 v[60:63], v[192:195], v[44:47], v[60:63]\n"
"s_waitcnt vmcnt(14)\n"
"ds_read_b128 v[40:43], v113 offset:31744\n"
"v_mfma_f32_16x16x32_bf16 v[64:67], v[196:199], v[44:47], v[64:67]\n"
"s_barrier\n"
"v_mfma_f32_16x16x32_bf16 v[60:63], v[200:203], v[48:51], v[60:63]\n"
"s_add_u32 s92, s53, s44\n"
"ds_read_b128 v[44:47], v113 offset:32768\n"
"v_mfma_f32_16x16x32_bf16 v[64:67], v[204:207], v[48:51], v[64:67]\n"
"s_waitcnt lgkmcnt(5)\n"
"v_mfma_f32_16x16x32_bf16 v[60:63], v[208:211], v[52:55], v[60:63]\n"
"s_add_u32 m0, s44, 0\n"
"ds_read_b128 v[48:51], v113 offset:33792\n"
"v_mfma_f32_16x16x32_bf16 v[64:67], v[212:215], v[52:55], v[64:67]\n"
"v_mfma_f32_16x16x32_bf16 v[60:63], v[216:219], v[56:59], v[60:63]\n"
"ds_read_b128 v[52:55], v113 offset:34816\n"
"buffer_load_dwordx4 v113, s[72:75], s92 offen offset:0 sc1 lds\n"
"v_mfma_f32_16x16x32_bf16 v[64:67], v[220:223], v[56:59], v[64:67]\n"
"s_waitcnt lgkmcnt(5)\n"
"v_mfma_f32_16x16x32_bf16 v[60:63], v[224:227], v[28:31], v[60:63]\n"
"ds_read_b128 v[56:59], v113 offset:35840\n"
"v_mfma_f32_16x16x32_bf16 v[64:67], v[228:231], v[28:31], v[64:67]\n"
"v_mfma_f32_16x16x32_bf16 v[60:63], v[232:235], v[32:35], v[60:63]\n"
"ds_read_b128 v[28:31], v113 offset:36864\n"
"buffer_load_dwordx4 v113, s[72:75], s92 offen offset:1024 sc1 lds\n"
"v_mfma_f32_16x16x32_bf16 v[64:67], v[236:239], v[32:35], v[64:67]\n"
"s_waitcnt lgkmcnt(5)\n"
"v_mfma_f32_16x16x32_bf16 v[60:63], v[240:243], v[36:39], v[60:63]\n"
"ds_read_b128 v[32:35], v113 offset:37888\n"
"v_mfma_f32_16x16x32_bf16 v[64:67], v[244:247], v[36:39], v[64:67]\n"
"v_mfma_f32_16x16x32_bf16 v[60:63], v[248:251], v[40:43], v[60:63]\n"
"ds_read_b128 v[36:39], v113 offset:38912\n"
"buffer_load_dwordx4 v113, s[72:75], s92 offen offset:2048 sc1 lds\n"
"v_mfma_f32_16x16x32_bf16 v[64:67], v[252:255], v[40:43], v[64:67]\n"
"s_waitcnt lgkmcnt(5)\n"
"v_mfma_f32_16x16x32_bf16 v[60:63], a[0:3], v[44:47], v[60:63]\n"
"ds_read_b128 v[40:43], v113 offset:39936\n"
"v_mfma_f32_16x16x32_bf16 v[64:67], a[4:7], v[44:47], v[64:67]\n"
"v_mfma_f32_16x16x32_bf16 v[60:63], a[8:11], v[48:51], v[60:63]\n"
"ds_read_b128 v[44:47], v113 offset:40960\n"
"buffer_load_dwordx4 v113, s[72:75], s92 offen offset:3072 sc1 lds\n"
"v_mfma_f32_16x16x32_bf16 v[64:67], a[12:15], v[48:51], v[64:67]\n"
"s_waitcnt lgkmcnt(5)\n"
"v_mfma_f32_16x16x32_bf16 v[60:63], a[16:19], v[52:55], v[60:63]\n"
"ds_read_b128 v[48:51], v113 offset:41984\n"
"v_mfma_f32_16x16x32_bf16 v[64:67], a[20:23], v[52:55], v[64:67]\n"
"v_mfma_f32_16x16x32_bf16 v[60:63], a[24:27], v[56:59], v[60:63]\n"
"ds_read_b128 v[52:55], v113 offset:43008\n"
"v_mfma_f32_16x16x32_bf16 v[64:67], a[28:31], v[56:59], v[64:67]\n"
"s_waitcnt lgkmcnt(5)\n"
"v_mfma_f32_16x16x32_bf16 v[60:63], a[32:35], v[28:31], v[60:63]\n"
"s_waitcnt vmcnt(9)\n"
"ds_read_b128 v[56:59], v113 offset:44032\n"
"v_mfma_f32_16x16x32_bf16 v[64:67], a[36:39], v[28:31], v[64:67]\n"
"s_barrier\n"
"v_mfma_f32_16x16x32_bf16 v[60:63], a[40:43], v[32:35], v[60:63]\n"
"s_add_u32 s92, s53, s45\n"
"ds_read_b128 v[28:31], v113 offset:49152\n"
"v_readfirstlane_b32 s93, v2\n"
"v_mfma_f32_16x16x32_bf16 v[64:67], a[44:47], v[32:35], v[64:67]\n"
"s_waitcnt lgkmcnt(5)\n"
"s_add_u32 m0, s45, 0\n"
"v_mfma_f32_16x16x32_bf16 v[60:63], a[48:51], v[36:39], v[60:63]\n"
"s_cmp_lt_u32 s93, s49\n"
"s_cbranch_scc1 Lslow_h2_K\n"
"Lret_h2_K:\n"
"ds_read_b128 v[32:35], v113 offset:50176\n"
"v_mfma_f32_16x16x32_bf16 v[64:67], a[52:55], v[36:39], v[64:67]\n"
"buffer_load_dwordx4 v113, s[72:75], s92 offen offset:0 sc1 lds\n"
"v_mfma_f32_16x16x32_bf16 v[60:63], a[56:59], v[40:43], v[60:63]\n"
"ds_read_b128 v[36:39], v113 offset:51200\n"
"v_mfma_f32_16x16x32_bf16 v[64:67], a[60:63], v[40:43], v[64:67]\n"
"s_waitcnt lgkmcnt(5)\n"
"v_mfma_f32_16x16x32_bf16 v[60:63], a[64:67], v[44:47], v[60:63]\n"
"ds_read_b128 v[40:43], v113 offset:52224\n"
"v_mfma_f32_16x16x32_bf16 v[64:67], a[68:71], v[44:47], v[64:67]\n"
"v_mfma_f32_16x16x32_bf16 v[60:63], a[72:75], v[48:51], v[60:63]\n"
"ds_read_b128 v[44:47], v113 offset:53248\n"
"v_mfma_f32_16x16x32_bf16 v[64:67], a[76:79], v[48:51], v[64:67]\n"
"s_waitcnt lgkmcnt(5)\n"
"v_mfma_f32_16x16x32_bf16 v[60:63], a[80:83], v[52:55], v[60:63]\n"
"ds_read_b128 v[48:51], v113 offset:54272\n"
"buffer_load_dwordx4 v113, s[72:75], s92 offen offset:1024 sc1 lds\n"
"v_mfma_f32_16x16x32_bf16 v[64:67], a[84:87], v[52:55], v[64:67]\n"
"v_mfma_f32_16x16x32_bf16 v[60:63], a[88:91], v[56:59], v[60:63]\n"
"ds_read_b128 v[52:55], v113 offset:55296\n"
"v_mfma_f32_16x16x32_bf16 v[64:67], a[92:95], v[56:59], v[64:67]\n"
"s_waitcnt lgkmcnt(5)\n"
"v_mfma_f32_16x16x32_bf16 v[68:71], v[128:131], v[28:31], 0\n"
"ds_read_b128 v[56:59], v113 offset:56320\n"
"v_mfma_f32_16x16x32_bf16 v[72:75], v[132:135], v[28:31], 0\n"
"v_mfma_f32_16x16x32_bf16 v[68:71], v[136:139], v[32:35], v[68:71]\n"
"ds_read_b128 v[28:31], v113 offset:57344\n"
"buffer_load_dwordx4 v113, s[72:75], s92 offen offset:2048 sc1 lds\n"
"v_mfma_f32_16x16x32_bf16 v[72:75], v[140:143], v[32:35], v[72:75]\n"
"s_waitcnt lgkmcnt(5)\n"
"v_xor_b32 v3, s50, v4\n"
"v_bitop3_b32 v3, v3, v5, s50 bitop3:0xf6\n"
"v_mfma_f32_16x16x32_bf16 v[68:71], v[144:147], v[36:39], v[68:71]\n"
"s_add_u32 s53, s53, 3670016\n"
"ds_read_b128 v[32:35], v113 offset:58368\n"
"v_cvt_pk_bf16_f32 v24, v60, v61\n"
"v_mfma_f32_16x16x32_bf16 v[72:75], v[148:151], v[36:39], v[72:75]\n"
"v_cvt_pk_bf16_f32 v25, v62, v63\n"
"v_cvt_pk_bf16_f32 v26, v64, v65\n"
"v_mfma_f32_16x16x32_bf16 v[68:71], v[152:155], v[40:43], v[68:71]\n"
"ds_read_b128 v[36:39], v113 offset:59392\n"
"v_cvt_pk_bf16_f32 v27, v66, v67\n"
"v_mfma_f32_16x16x32_bf16 v[72:75], v[156:159], v[40:43], v[72:75]\n"
"s_waitcnt lgkmcnt(5)\n"
"v_and_or_b32 v24, v24, v23, s37\n"
"v_and_or_b32 v25, v25, v23, s37\n"
"v_mfma_f32_16x16x32_bf16 v[68:71], v[160:163], v[44:47], v[68:71]\n"
"ds_read_b128 v[40:43], v113 offset:60416\n"
"v_and_or_b32 v26, v26, v23, s37\n"
"v_mfma_f32_16x16x32_bf16 v[72:75], v[164:167], v[44:47], v[72:75]\n"
"v_and_or_b32 v27, v27, v23, s37\n"
"v_bitop3_b32 v3, v3, v6, s50 bitop3:0xf6\n"
"v_mfma_f32_16x16x32_bf16 v[68:71], v[168:171], v[48:51], v[68:71]\n"
"buffer_store_dwordx4 v[24:27], v113, s[72:75], s55 offen sc1\n"
"ds_read_b128 v[44:47], v113 offset:61440\n"
"v_mfma_f32_16x16x32_bf16 v[72:75], v[172:175], v[48:51], v[72:75]\n"
"s_waitcnt lgkmcnt(5)\n"
"v_bitop3_b32 v3, v3, v7, s50 bitop3:0xf6\n"
"v_bitop3_b32 v3, v3, v8, s50 bitop3:0xf6\n"
"v_mfma_f32_16x16x32_bf16 v[68:71], v[176:179], v[52:55], v[68:71]\n"
"s_add_u32 s55, s55, 3145728\n"
"ds_read_b128 v[48:51], v113 offset:62464\n"
"v_bitop3_b32 v3, v3, v9, s50 bitop3:0xf6\n"
"v_mfma_f32_16x16x32_bf16 v[72:75], v[180:183], v[52:55], v[72:75]\n"
"v_bitop3_b32 v3, v3, v10, s50 bitop3:0xf6\n"
"v_bitop3_b32 v3, v3, v11, s50 bitop3:0xf6\n"
"v_mfma_f32_16x16x32_bf16 v[68:71], v[184:187], v[56:59], v[68:71]\n"
"ds_read_b128 v[52:55], v113 offset:63488\n"
"v_and_b32 v3, 0xffff, v3\n"
"v_mfma_f32_16x16x32_bf16 v[72:75], v[188:191], v[56:59], v[72:75]\n"
"s_waitcnt lgkmcnt(5)\n"
"v_cmp_ne_u32 vcc, 0, v3\n"
"v_mfma_f32_16x16x32_bf16 v[68:71], v[192:195], v[28:31], v[68:71]\n"
"s_waitcnt vmcnt(9)\n"
"ds_read_b128 v[56:59], v113 offset:64512\n"
"v_mfma_f32_16x16x32_bf16 v[72:75], v[196:199], v[28:31], v[72:75]\n"
"s_barrier\n"
"s_cbranch_vccnz Lslow_gb_K\n"
"Lret_gb_K:\n"
"v_and_b32 v104, 0xffff0000, v4\n"
"v_mfma_f32_16x16x32_bf16 v[68:71], v[200:203], v[32:35], v[68:71]\n"
"s_add_u32 s92, s51, s44\n"
"ds_read_b128 v[28:31], v118 offset:0\n"
"v_and_b32 v105, 0xffff0000, v5\n"
"v_mfma_f32_16x16x32_bf16 v[72:75], v[204:207], v[32:35], v[72:75]\n"
"s_waitcnt lgkmcnt(5)\n"
"v_and_b32 v106, 0xffff0000, v6\n"
"v_and_b32 v107, 0xffff0000, v7\n"
"v_mfma_f32_16x16x32_bf16 v[68:71], v[208:211], v[36:39], v[68:71]\n"
"s_add_u32 m0, s44, 32768\n"
"ds_read_b128 v[32:35], v118 offset:1024\n"
"v_and_b32 v108, 0xffff0000, v8\n"
"v_mfma_f32_16x16x32_bf16 v[72:75], v[212:215], v[36:39], v[72:75]\n"
"v_and_b32 v109, 0xffff0000, v9\n"
"v_and_b32 v110, 0xffff0000, v10\n"
"v_mfma_f32_16x16x32_bf16 v[68:71], v[216:219], v[40:43], v[68:71]\n"
"ds_read_b128 v[36:39], v118 offset:2048\n"
"buffer_load_dwordx4 v113, s[72:75], s92 offen offset:0 sc1 lds\n"
"v_mfma_f32_16x16x32_bf16 v[72:75], v[220:223], v[40:43], v[72:75]\n"
"s_waitcnt lgkmcnt(5)\n"
"v_and_b32 v111, 0xffff0000, v11\n"
"v_lshlrev_b32 v96, 16, v100\n"
"v_mfma_f32_16x16x32_bf16 v[68:71], v[224:227], v[44:47], v[68:71]\n"
"ds_read_b128 v[40:43], v118 offset:3072\n"
"v_and_b32 v97, 0xffff0000, v100\n"
"v_mfma_f32_16x16x32_bf16 v[72:75], v[228:231], v[44:47], v[72:75]\n"
"v_lshlrev_b32 v98, 16, v101\n"
"v_and_b32 v99, 0xffff0000, v101\n"
"v_mfma_f32_16x16x32_bf16 v[68:71], v[232:235], v[48:51], v[68:71]\n"
"ds_read_b128 v[44:47], v118 offset:4096\n"
"buffer_load_dwordx4 v113, s[72:75], s92 offen offset:1024 sc1 lds\n"
"v_mfma_f32_16x16x32_bf16 v[72:75], v[236:239], v[48:51], v[72:75]\n"
"s_waitcnt lgkmcnt(5)\n"
"v_lshlrev_b32 v100, 16, v102\n"
"v_and_b32 v101, 0xffff0000, v102\n"
"v_mfma_f32_16x16x32_bf16 v[68:71], v[240:243], v[52:55], v[68:71]\n"
"ds_read_b128 v[48:51], v118 offset:5120\n"
"v_lshlrev_b32 v102, 16, v103\n"
"v_mfma_f32_16x16x32_bf16 v[72:75], v[244:247], v[52:55], v[72:75]\n"
"v_and_b32 v103, 0xffff0000, v103\n"
"v_mfma_f32_16x16x32_bf16 v[68:71], v[248:251], v[56:59], v[68:71]\n"
"ds_read_b128 v[52:55], v118 offset:6144\n"
"buffer_load_dwordx4 v113, s[72:75], s92 offen offset:2048 sc1 lds\n"
"v_mfma_f32_16x16x32_bf16 v[72:75], v[252:255], v[56:59], v[72:75]\n"
"s_waitcnt lgkmcnt(5)\n"
"v_mfma_f32_16x16x32_bf16 v[68:71], a[0:3], v[28:31], v[68:71]\n"
"ds_read_b128 v[56:59], v118 offset:7168\n"
"v_mfma_f32_16x16x32_bf16 v[72:75], a[4:7], v[28:31], v[72:75]\n"
"v_mfma_f32_16x16x32_bf16 v[68:71], a[8:11], v[32:35], v[68:71]\n"
"ds_read_b128 v[28:31], v118 offset:8192\n"
"buffer_load_dwordx4 v113, s[72:75], s92 offen offset:3072 sc1 lds\n"
"v_mfma_f32_16x16x32_bf16 v[72:75], a[12:15], v[32:35], v[72:75]\n"
"s_waitcnt lgkmcnt(5)\n"
"v_mfma_f32_16x16x32_bf16 v[68:71], a[16:19], v[36:39], v[68:71]\n"
"ds_read_b128 v[32:35], v118 offset:9216\n"
"v_mfma_f32_16x16x32_bf16 v[72:75], a[20:23], v[36:39], v[72:75]\n"
"v_mfma_f32_16x16x32_bf16 v[68:71], a[24:27], v[40:43], v[68:71]\n"
"ds_read_b128 v[36:39], v118 offset:10240\n"
"v_mfma_f32_16x16x32_bf16 v[72:75], a[28:31], v[40:43], v[72:75]\n"
"s_waitcnt lgkmcnt(5)\n"
"v_mfma_f32_16x16x32_bf16 v[68:71], a[32:35], v[44:47], v[68:71]\n"
"s_waitcnt vmcnt(8)\n"
"ds_read_b128 v[40:43], v118 offset:11264\n"
"v_mfma_f32_16x16x32_bf16 v[72:75], a[36:39], v[44:47], v[72:75]\n"
"s_barrier\n"
"v_mfma_f32_16x16x32_bf16 v[68:71], a[40:43], v[48:51], v[68:71]\n"
"s_add_u32 s92, s51, s45\n"
"ds_read_b128 v[44:47], v113 offset:0\n"
"v_mfma_f32_16x16x32_bf16 v[72:75], a[44:47], v[48:51], v[72:75]\n"
"s_waitcnt lgkmcnt(5)\n"
"v_mfma_f32_16x16x32_bf16 v[68:71], a[48:51], v[52:55], v[68:71]\n"
"s_add_u32 m0, s45, 32768\n"
"ds_read_b128 v[48:51], v113 offset:1024\n"
"v_mfma_f32_16x16x32_bf16 v[72:75], a[52:55], v[52:55], v[72:75]\n"
"v_mfma_f32_16x16x32_bf16 v[68:71], a[56:59], v[56:59], v[68:71]\n"
"ds_read_b128 v[52:55], v113 offset:2048\n"
"buffer_load_dwordx4 v113, s[72:75], s92 offen offset:0 sc1 lds\n"
"v_mfma_f32_16x16x32_bf16 v[72:75], a[60:63], v[56:59], v[72:75]\n"
"s_waitcnt lgkmcnt(5)\n"
"v_mfma_f32_16x16x32_bf16 v[68:71], a[64:67], v[28:31], v[68:71]\n"
"ds_read_b128 v[56:59], v113 offset:3072\n"
"v_mfma_f32_16x16x32_bf16 v[72:75], a[68:71], v[28:31], v[72:75]\n"
"v_mfma_f32_16x16x32_bf16 v[68:71], a[72:75], v[32:35], v[68:71]\n"
"ds_read_b128 v[28:31], v113 offset:4096\n"
"v_mfma_f32_16x16x32_bf16 v[72:75], a[76:79], v[32:35], v[72:75]\n"
"s_waitcnt lgkmcnt(5)\n"
"v_mfma_f32_16x16x32_bf16 v[68:71], a[80:83], v[36:39], v[68:71]\n"
"ds_read_b128 v[32:35], v113 offset:5120\n"
"buffer_load_dwordx4 v113, s[72:75], s92 offen offset:1024 sc1 lds\n"
"v_mfma_f32_16x16x32_bf16 v[72:75], a[84:87], v[36:39], v[72:75]\n"
"v_mfma_f32_16x16x32_bf16 v[68:71], a[88:91], v[40:43], v[68:71]\n"
"ds_read_b128 v[36:39], v113 offset:6144\n"
"v_mfma_f32_16x16x32_bf16 v[72:75], a[92:95], v[40:43], v[72:75]\n"
"s_waitcnt lgkmcnt(5)\n"
"v_mfma_f32_16x16x32_bf16 v[60:63], v[128:131], v[44:47], 0\n"
"ds_read_b128 v[40:43], v113 offset:7168\n"
"v_mfma_f32_16x16x32_bf16 v[64:67], v[132:135], v[44:47], 0\n"
"v_mfma_f32_16x16x32_bf16 v[60:63], v[136:139], v[48:51], v[60:63]\n"
"ds_read_b128 v[44:47], v113 offset:8192\n"
"buffer_load_dwordx4 v113, s[72:75], s92 offen offset:2048 sc1 lds\n"
"v_mfma_f32_16x16x32_bf16 v[64:67], v[140:143], v[48:51], v[64:67]\n"
"s_waitcnt lgkmcnt(5)\n"
"v_mfma_f32_16x16x32_bf16 v[60:63], v[144:147], v[52:55], v[60:63]\n"
"s_add_u32 s51, s51, 3670016\n"
"ds_read_b128 v[48:51], v113 offset:9216\n"
"v_cvt_pk_bf16_f32 v24, v68, v69\n"
"v_mfma_f32_16x16x32_bf16 v[64:67], v[148:151], v[52:55], v[64:67]\n"
"v_cvt_pk_bf16_f32 v25, v70, v71\n"
"v_cvt_pk_bf16_f32 v26, v72, v73\n"
"v_mfma_f32_16x16x32_bf16 v[60:63], v[152:155], v[56:59], v[60:63]\n"
"ds_read_b128 v[52:55], v113 offset:10240\n"
"v_cvt_pk_bf16_f32 v27, v74, v75\n"
"v_mfma_f32_16x16x32_bf16 v[64:67], v[156:159], v[56:59], v[64:67]\n"
"s_waitcnt lgkmcnt(5)\n"
"v_and_or_b32 v24, v24, v23, s37\n"
"v_and_or_b32 v25, v25, v23, s37\n"
"v_mfma_f32_16x16x32_bf16 v[60:63], v[160:163], v[28:31], v[60:63]\n"
"ds_read_b128 v[56:59], v113 offset:11264\n"
"v_and_or_b32 v26, v26, v23, s37\n"
"v_mfma_f32_16x16x32_bf16 v[64:67], v[164:167], v[28:31], v[64:67]\n"
"v_and_or_b32 v27, v27, v23, s37\n"
"v_mfma_f32_16x16x32_bf16 v[60:63], v[168:171], v[32:35], v[60:63]\n"
"buffer_store_dwordx4 v[24:27], v113, s[72:75], s56 offen sc1\n"
"ds_read_b128 v[28:31], v113 offset:12288\n"
"v_mfma_f32_16x16x32_bf16 v[64:67], v[172:175], v[32:35], v[64:67]\n"
"s_waitcnt lgkmcnt(5)\n"
"v_mfma_f32_16x16x32_bf16 v[60:63], v[176:179], v[36:39], v[60:63]\n"
"s_add_u32 s56, s56, 3145728\n"
"ds_read_b128 v[32:35], v113 offset:13312\n"
"v_mfma_f32_16x16x32_bf16 v[64:67], v[180:183], v[36:39], v[64:67]\n"
"v_mfma_f32_16x16x32_bf16 v[60:63], v[184:187], v[40:43], v[60:63]\n"
"ds_read_b128 v[36:39], v113 offset:14336\n"
"v_mfma_f32_16x16x32_bf16 v[64:67], v[188:191], v[40:43], v[64:67]\n"
"s_waitcnt lgkmcnt(5)\n"
"v_mfma_f32_16x16x32_bf16 v[60:63], v[192:195], v[44:47], v[60:63]\n"
"s_waitcnt vmcnt(9)\n"
"ds_read_b128 v[40:43], v113 offset:15360\n"
"v_mfma_f32_16x16x32_bf16 v[64:67], v[196:199], v[44:47], v[64:67]\n"
"s_barrier\n"
"v_mfma_f32_16x16x32_bf16 v[60:63], v[200:203], v[48:51], v[60:63]\n"
"s_add_u32 s92, s52, s44\n"
"ds_read_b128 v[44:47], v113 offset:16384\n"
"buffer_load_dword v2, v0, s[72:75], 0 offen offset:768 sc1\n"
"v_mfma_f32_16x16x32_bf16 v[64:67], v[204:207], v[48:51], v[64:67]\n"
"s_waitcnt lgkmcnt(5)\n"
"v_mfma_f32_16x16x32_bf16 v[60:63], v[208:211], v[52:55], v[60:63]\n"
"s_add_u32 m0, s44, 65536\n"
"ds_read_b128 v[48:51], v113 offset:17408\n"
"v_mfma_f32_16x16x32_bf16 v[64:67], v[212:215], v[52:55], v[64:67]\n"
"v_mfma_f32_16x16x32_bf16 v[60:63], v[216:219], v[56:59], v[60:63]\n"
"ds_read_b128 v[52:55], v113 offset:18432\n"
"buffer_load_dwordx4 v113, s[72:75], s92 offen offset:0 sc1 lds\n"
"v_mfma_f32_16x16x32_bf16 v[64:67], v[220:223], v[56:59], v[64:67]\n"
"s_waitcnt lgkmcnt(5)\n"
"v_mfma_f32_16x16x32_bf16 v[60:63], v[224:227], v[28:31], v[60:63]\n"
"ds_read_b128 v[56:59], v113 offset:19456\n"
"v_mfma_f32_16x16x32_bf16 v[64:67], v[228:231], v[28:31], v[64:67]\n"
"v_mfma_f32_16x16x32_bf16 v[60:63], v[232:235], v[32:35], v[60:63]\n"
"ds_read_b128 v[28:31], v113 offset:20480\n"
"buffer_load_dwordx4 v113, s[72:75], s92 offen offset:1024 sc1 lds\n"
"v_mfma_f32_16x16x32_bf16 v[64:67], v[236:239], v[32:35], v[64:67]\n"
"s_waitcnt lgkmcnt(5)\n"
"v_mfma_f32_16x16x32_bf16 v[60:63], v[240:243], v[36:39], v[60:63]\n"
"ds_read_b128 v[32:35], v113 offset:21504\n"
"buffer_load_dwordx4 v[88:91], v126, s[72:75], s47 offen sc1\n"
"v_mfma_f32_16x16x32_bf16 v[64:67], v[244:247], v[36:39], v[64:67]\n"
"s_add_u32 s47, s47, 1048576\n"
"v_mfma_f32_16x16x32_bf16 v[60:63], v[248:251], v[40:43], v[60:63]\n"
"s_getpc_b64 s[62:63]\n"
"ds_read_b128 v[36:39], v113 offset:22528\n"
"buffer_load_dwordx4 v113, s[72:75], s92 offen offset:2048 sc1 lds\n"
"v_mfma_f32_16x16x32_bf16 v[64:67], v[252:255], v[40:43], v[64:67]\n"
"s_waitcnt lgkmcnt(5)\n"
"global_load_dword v21, v119, s[62:63] offset:3072\n"
"v_mfma_f32_16x16x32_bf16 v[60:63], a[0:3], v[44:47], v[60:63]\n"
"s_add_u32 s95, s46, 1048576\n"
"ds_read_b128 v[40:43], v113 offset:23552\n"
"v_mfma_f32_16x16x32_bf16 v[64:67], a[4:7], v[44:47], v[64:67]\n"
"s_add_u32 s61, s46, 2097152\n"
"v_mfma_f32_16x16x32_bf16 v[60:63], a[8:11], v[48:51], v[60:63]\n"
"ds_read_b128 v[44:47], v113 offset:24576\n"
"buffer_load_dwordx4 v113, s[72:75], s92 offen offset:3072 sc1 lds\n"
"v_mfma_f32_16x16x32_bf16 v[64:67], a[12:15], v[48:51], v[64:67]\n"
"s_waitcnt lgkmcnt(5)\n"
"buffer_load_dwordx4 v[4:7], v113, s[72:75], s46 offen sc1\n"
"v_mfma_f32_16x16x32_bf16 v[60:63], a[16:19], v[52:55], v[60:63]\n"
"ds_read_b128 v[48:51], v113 offset:25600\n"
"v_mfma_f32_16x16x32_bf16 v[64:67], a[20:23], v[52:55], v[64:67]\n"
"buffer_load_dwordx4 v[8:11], v113, s[72:75], s95 offen sc1\n"
"v_mfma_f32_16x16x32_bf16 v[60:63], a[24:27], v[56:59], v[60:63]\n"
"ds_read_b128 v[52:55], v113 offset:26624\n"
"v_mfma_f32_16x16x32_bf16 v[64:67], a[28:31], v[56:59], v[64:67]\n"
"s_waitcnt lgkmcnt(5)\n"
"buffer_load_dwordx4 v[12:15], v113, s[72:75], s61 offen sc1\n"
"v_mfma_f32_16x16x32_bf16 v[60:63], a[32:35], v[28:31], v[60:63]\n"
"s_waitcnt vmcnt(14)\n"
"ds_read_b128 v[56:59], v113 offset:27648\n"
"v_mfma_f32_16x16x32_bf16 v[64:67], a[36:39], v[28:31], v[64:67]\n"
"s_barrier\n"
"v_mfma_f32_16x16x32_bf16 v[60:63], a[40:43], v[32:35], v[60:63]\n"
"s_add_u32 s92, s52, s45\n"
"ds_read_b128 v[28:31], v113 offset:32768\n"
"v_mfma_f32_16x16x32_bf16 v[64:67], a[44:47], v[32:35], v[64:67]\n"
"s_waitcnt lgkmcnt(5)\n"
"v_mfma_f32_16x16x32_bf16 v[60:63], a[48:51], v[36:39], v[60:63]\n"
"s_add_u32 m0, s45, -16384\n"
"ds_read_b128 v[32:35], v113 offset:33792\n"
"v_mfma_f32_16x16x32_bf16 v[64:67], a[52:55], v[36:39], v[64:67]\n"
"v_mfma_f32_16x16x32_bf16 v[60:63], a[56:59], v[40:43], v[60:63]\n"
"ds_read_b128 v[36:39], v113 offset:34816\n"
"buffer_load_dwordx4 v113, s[72:75], s92 offen offset:0 sc1 lds\n"
"v_mfma_f32_16x16x32_bf16 v[64:67], a[60:63], v[40:43], v[64:67]\n"
"s_waitcnt lgkmcnt(5)\n"
"v_mfma_f32_16x16x32_bf16 v[60:63], a[64:67], v[44:47], v[60:63]\n"
"ds_read_b128 v[40:43], v113 offset:35840\n"
"v_mfma_f32_16x16x32_bf16 v[64:67], a[68:71], v[44:47], v[64:67]\n"
"v_mfma_f32_16x16x32_bf16 v[60:63], a[72:75], v[48:51], v[60:63]\n"
"ds_read_b128 v[44:47], v113 offset:36864\n"
"v_mfma_f32_16x16x32_bf16 v[64:67], a[76:79], v[48:51], v[64:67]\n"
"s_waitcnt lgkmcnt(5)\n"
"v_mfma_f32_16x16x32_bf16 v[60:63], a[80:83], v[52:55], v[60:63]\n"
"ds_read_b128 v[48:51], v113 offset:37888\n"
"buffer_load_dwordx4 v113, s[72:75], s92 offen offset:1024 sc1 lds\n"
"v_mfma_f32_16x16x32_bf16 v[64:67], a[84:87], v[52:55], v[64:67]\n"
"v_mfma_f32_16x16x32_bf16 v[60:63], a[88:91], v[56:59], v[60:63]\n"
"ds_read_b128 v[52:55], v113 offset:38912\n"
"v_mfma_f32_16x16x32_bf16 v[64:67], a[92:95], v[56:59], v[64:67]\n"
"s_waitcnt lgkmcnt(5)\n"
"v_mfma_f32_16x16x32_bf16 v[68:71], v[128:131], v[28:31], 0\n"
"ds_read_b128 v[56:59], v113 offset:39936\n"
"v_mfma_f32_16x16x32_bf16 v[72:75], v[132:135], v[28:31], 0\n"
"v_mfma_f32_16x16x32_bf16 v[68:71], v[136:139], v[32:35], v[68:71]\n"
"ds_read_b128 v[28:31], v113 offset:40960\n"
"buffer_load_dwordx4 v113, s[72:75], s92 offen offset:2048 sc1 lds\n"
"v_mfma_f32_16x16x32_bf16 v[72:75], v[140:143], v[32:35], v[72:75]\n"
"s_waitcnt lgkmcnt(5)\n"
"v_mfma_f32_16x16x32_bf16 v[68:71], v[144:147], v[36:39], v[68:71]\n"
"s_add_u32 s52, s52, 3670016\n"
"ds_read_b128 v[32:35], v113 offset:41984\n"
"v_cvt_pk_bf16_f32 v24, v60, v61\n"
"v_mfma_f32_16x16x32_bf16 v[72:75], v[148:151], v[36:39], v[72:75]\n"
"v_cvt_pk_bf16_f32 v25, v62, v63\n"
"v_cvt_pk_bf16_f32 v26, v64, v65\n"
"v_mfma_f32_16x16x32_bf16 v[68:71], v[152:155], v[40:43], v[68:71]\n"
"ds_read_b128 v[36:39], v113 offset:43008\n"
"v_cvt_pk_bf16_f32 v27, v66, v67\n"
"v_mfma_f32_16x16x32_bf16 v[72:75], v[156:159], v[40:43], v[72:75]\n"
"s_waitcnt lgkmcnt(5)\n"
"v_and_or_b32 v24, v24, v23, s37\n"
"v_and_or_b32 v25, v25, v23, s37\n"
"v_mfma_f32_16x16x32_bf16 v[68:71], v[160:163], v[44:47], v[68:71]\n"
"ds_read_b128 v[40:43], v113 offset:44032\n"
"v_and_or_b32 v26, v26, v23, s37\n"
"v_mfma_f32_16x16x32_bf16 v[72:75], v[164:167], v[44:47], v[72:75]\n"
"v_and_or_b32 v27, v27, v23, s37\n"
"v_mfma_f32_16x16x32_bf16 v[68:71], v[168:171], v[48:51], v[68:71]\n"
"buffer_store_dwordx4 v[24:27], v113, s[72:75], s57 offen sc1\n"
"ds_read_b128 v[44:47], v113 offset:45056\n"
"v_mfma_f32_16x16x32_bf16 v[72:75], v[172:175], v[48:51], v[72:75]\n"
"s_waitcnt lgkmcnt(5)\n"
"v_mfma_f32_16x16x32_bf16 v[68:71], v[176:179], v[52:55], v[68:71]\n"
"s_add_u32 s57, s57, 3145728\n"
"ds_read_b128 v[48:51], v113 offset:46080\n"
"v_mfma_f32_16x16x32_bf16 v[72:75], v[180:183], v[52:55], v[72:75]\n"
"v_mfma_f32_16x16x32_bf16 v[68:71], v[184:187], v[56:59], v[68:71]\n"
"ds_read_b128 v[52:55], v113 offset:47104\n"
"v_mfma_f32_16x16x32_bf16 v[72:75], v[188:191], v[56:59], v[72:75]\n"
"s_waitcnt lgkmcnt(5)\n"
"v_mfma_f32_16x16x32_bf16 v[68:71], v[192:195], v[28:31], v[68:71]\n"
"s_waitcnt vmcnt(15)\n"
"ds_read_b128 v[56:59], v113 offset:48128\n"
"v_mfma_f32_16x16x32_bf16 v[72:75], v[196:199], v[28:31], v[72:75]\n"
"s_barrier\n"
"v_mfma_f32_16x16x32_bf16 v[68:71], v[200:203], v[32:35], v[68:71]\n"
"s_add_u32 s92, s53, s44\n"
"ds_read_b128 v[28:31], v113 offset:49152\n"
"v_mfma_f32_16x16x32_bf16 v[72:75], v[204:207], v[32:35], v[72:75]\n"
"s_waitcnt lgkmcnt(5)\n"
"v_mfma_f32_16x16x32_bf16 v[68:71], v[208:211], v[36:39], v[68:71]\n"
"s_add_u32 m0, s44, 16384\n"
"ds_read_b128 v[32:35], v113 offset:50176\n"
"v_mfma_f32_16x16x32_bf16 v[72:75], v[212:215], v[36:39], v[72:75]\n"
"v_mfma_f32_16x16x32_bf16 v[68:71], v[216:219], v[40:43], v[68:71]\n"
"ds_read_b128 v[36:39], v113 offset:51200\n"
"buffer_load_dwordx4 v113, s[72:75], s92 offen offset:0 sc1 lds\n"
"v_mfma_f32_16x16x32_bf16 v[72:75], v[220:223], v[40:43], v[72:75]\n"
"s_waitcnt lgkmcnt(5)\n"
"v_mfma_f32_16x16x32_bf16 v[68:71], v[224:227], v[44:47], v[68:71]\n"
"ds_read_b128 v[40:43], v113 offset:52224\n"
"v_mfma_f32_16x16x32_bf16 v[72:75], v[228:231], v[44:47], v[72:75]\n"
"v_mfma_f32_16x16x32_bf16 v[68:71], v[232:235], v[48:51], v[68:71]\n"
"ds_read_b128 v[44:47], v113 offset:53248\n"
"buffer_load_dwordx4 v113, s[72:75], s92 offen offset:1024 sc1 lds\n"
"v_mfma_f32_16x16x32_bf16 v[72:75], v[236:239], v[48:51], v[72:75]\n"
"s_waitcnt lgkmcnt(5)\n"
"v_mfma_f32_16x16x32_bf16 v[68:71], v[240:243], v[52:55], v[68:71]\n"
"ds_read_b128 v[48:51], v113 offset:54272\n"
"v_mfma_f32_16x16x32_bf16 v[72:75], v[244:247], v[52:55], v[72:75]\n"
"v_mfma_f32_16x16x32_bf16 v[68:71], v[248:251], v[56:59], v[68:71]\n"
"ds_read_b128 v[52:55], v113 offset:55296\n"
"buffer_load_dwordx4 v113, s[72:75], s92 offen offset:2048 sc1 lds\n"
"v_mfma_f32_16x16x32_bf16 v[72:75], v[252:255], v[56:59], v[72:75]\n"
"s_waitcnt lgkmcnt(5)\n"
"v_mfma_f32_16x16x32_bf16 v[68:71], a[0:3], v[28:31], v[68:71]\n"
"ds_read_b128 v[56:59], v113 offset:56320\n"
"v_mfma_f32_16x16x32_bf16 v[72:75], a[4:7], v[28:31], v[72:75]\n"
"v_mfma_f32_16x16x32_bf16 v[68:71], a[8:11], v[32:35], v[68:71]\n"
"ds_read_b128 v[28:31], v113 offset:57344\n"
"buffer_load_dwordx4 v113, s[72:75], s92 offen offset:3072 sc1 lds\n"
"v_mfma_f32_16x16x32_bf16 v[72:75], a[12:15], v[32:35], v[72:75]\n"
"s_waitcnt lgkmcnt(5)\n"
"v_mfma_f32_16x16x32_bf16 v[68:71], a[16:19], v[36:39], v[68:71]\n"
"ds_read_b128 v[32:35], v113 offset:58368\n"
"v_mfma_f32_16x16x32_bf16 v[72:75], a[20:23], v[36:39], v[72:75]\n"
"v_mfma_f32_16x16x32_bf16 v[68:71], a[24:27], v[40:43], v[68:71]\n"
"ds_read_b128 v[36:39], v113 offset:59392\n"
"v_mfma_f32_16x16x32_bf16 v[72:75], a[28:31], v[40:43], v[72:75]\n"
"s_waitcnt lgkmcnt(5)\n"
"v_mfma_f32_16x16x32_bf16 v[68:71], a[32:35], v[44:47], v[68:71]\n"
"s_waitcnt vmcnt(11)\n"
"ds_read_b128 v[40:43], v113 offset:60416\n"
"v_mfma_f32_16x16x32_bf16 v[72:75], a[36:39], v[44:47], v[72:75]\n"
"s_barrier\n"
"v_mfma_f32_16x16x32_bf16 v[68:71], a[40:43], v[48:51], v[68:71]\n"
"s_add_u32 s92, s53, s45\n"
"ds_read_b128 v[44:47], v118 offset:0\n"
"v_readfirstlane_b32 s93, v2\n"
"v_mfma_f32_16x16x32_bf16 v[72:75], a[44:47], v[48:51], v[72:75]\n"
"s_waitcnt lgkmcnt(5)\n"
"s_add_u32 m0, s45, 16384\n"
"v_mfma_f32_16x16x32_bf16 v[68:71], a[48:51], v[52:55], v[68:71]\n"
"s_cmp_lt_u32 s93, s49\n"
"s_cbranch_scc1 Lslow_h3_K\n"
"Lret_h3_K:\n"
"ds_read_b128 v[48:51], v118 offset:1024\n"
"v_mfma_f32_16x16x32_bf16 v[72:75], a[52:55], v[52:55], v[72:75]\n"
"buffer_load_dwordx4 v113, s[72:75], s92 offen offset:0 sc1 lds\n"
"v_mfma_f32_16x16x32_bf16 v[68:71], a[56:59], v[56:59], v[68:71]\n"
"ds_read_b128 v[52:55], v118 offset:2048\n"
"v_mfma_f32_16x16x32_bf16 v[72:75], a[60:63], v[56:59], v[72:75]\n"
"s_waitcnt lgkmcnt(5)\n"
"v_mfma_f32_16x16x32_bf16 v[68:71], a[64:67], v[28:31], v[68:71]\n"
"ds_read_b128 v[56:59], v118 offset:3072\n"
"v_mfma_f32_16x16x32_bf16 v[72:75], a[68:71], v[28:31], v[72:75]\n"
"v_mfma_f32_16x16x32_bf16 v[68:71], a[72:75], v[32:35], v[68:71]\n"
"ds_read_b128 v[28:31], v118 offset:4096\n"
"v_mfma_f32_16x16x32_bf16 v[72:75], a[76:79], v[32:35], v[72:75]\n"
"s_waitcnt lgkmcnt(5)\n"
"v_mfma_f32_16x16x32_bf16 v[68:71], a[80:83], v[36:39], v[68:71]\n"
"ds_read_b128 v[32:35], v118 offset:5120\n"
"buffer_load_dwordx4 v113, s[72:75], s92 offen offset:1024 sc1 lds\n"
"v_mfma_f32_16x16x32_bf16 v[72:75], a[84:87], v[36:39], v[72:75]\n"
"v_mfma_f32_16x16x32_bf16 v[68:71], a[88:91], v[40:43], v[68:71]\n"
"ds_read_b128 v[36:39], v118 offset:6144\n"
"v_mfma_f32_16x16x32_bf16 v[72:75], a[92:95], v[40:43], v[72:75]\n"
"s_waitcnt lgkmcnt(5)\n"
"v_mfma_f32_16x16x32_bf16 v[60:63], v[128:131], v[44:47], 0\n"
"ds_read_b128 v[40:43], v118 offset:7168\n"
"v_mfma_f32_16x16x32_bf16 v[64:67], v[132:135], v[44:47], 0\n"
"v_mfma_f32_16x16x32_bf16 v[60:63], v[136:139], v[48:51], v[60:63]\n"
"ds_read_b128 v[44:47], v118 offset:8192\n"
"buffer_load_dwordx4 v113, s[72:75], s92 offen offset:2048 sc1 lds\n"
"v_mfma_f32_16x16x32_bf16 v[64:67], v[140:143], v[48:51], v[64:67]\n"
"s_waitcnt lgkmcnt(5)\n"
"v_mfma_f32_16x16x32_bf16 v[60:63], v[144:147], v[52:55], v[60:63]\n"
"s_add_u32 s53, s53, 3670016\n"
"ds_read_b128 v[48:51], v118 offset:9216\n"
"v_cvt_pk_bf16_f32 v24, v68, v69\n"
"v_mfma_f32_16x16x32_bf16 v[64:67], v[148:151], v[52:55], v[64:67]\n"
"v_cvt_pk_bf16_f32 v25, v70, v71\n"
"v_cvt_pk_bf16_f32 v26, v72, v73\n"
"v_mfma_f32_16x16x32_bf16 v[60:63], v[152:155], v[56:59], v[60:63]\n"
"ds_read_b128 v[52:55], v118 offset:10240\n"
"v_cvt_pk_bf16_f32 v27, v74, v75\n"
"v_mfma_f32_16x16x32_bf16 v[64:67], v[156:159], v[56:59], v[64:67]\n"
"s_waitcnt lgkmcnt(5)\n"
"v_and_or_b32 v24, v24, v23, s37\n"
"v_and_or_b32 v25, v25, v23, s37\n"
"v_mfma_f32_16x16x32_bf16 v[60:63], v[160:163], v[28:31], v[60:63]\n"
"ds_read_b128 v[56:59], v118 offset:11264\n"
"v_and_or_b32 v26, v26, v23, s37\n"
"v_mfma_f32_16x16x32_bf16 v[64:67], v[164:167], v[28:31], v[64:67]\n"
"v_and_or_b32 v27, v27, v23, s37\n"
"v_mfma_f32_16x16x32_bf16 v[60:63], v[168:171], v[32:35], v[60:63]\n"
"buffer_store_dwordx4 v[24:27], v113, s[72:75], s55 offen sc1\n"
"ds_read_b128 v[28:31], v118 offset:12288\n"
"v_mfma_f32_16x16x32_bf16 v[64:67], v[172:175], v[32:35], v[64:67]\n"
"s_waitcnt lgkmcnt(5)\n"
"v_mfma_f32_16x16x32_bf16 v[60:63], v[176:179], v[36:39], v[60:63]\n"
"s_add_u32 s55, s55, 3145728\n"
"ds_read_b128 v[32:35], v118 offset:13312\n"
"v_mfma_f32_16x16x32_bf16 v[64:67], v[180:183], v[36:39], v[64:67]\n"
"v_mfma_f32_16x16x32_bf16 v[60:63], v[184:187], v[40:43], v[60:63]\n"
"ds_read_b128 v[36:39], v118 offset:14336\n"
"v_mfma_f32_16x16x32_bf16 v[64:67], v[188:191], v[40:43], v[64:67]\n"
"s_waitcnt lgkmcnt(5)\n"
"v_mfma_f32_16x16x32_bf16 v[60:63], v[192:195], v[44:47], v[60:63]\n"
"s_waitcnt vmcnt(9)\n"
"ds_read_b128 v[40:43], v118 offset:15360\n"
"v_mfma_f32_16x16x32_bf16 v[64:67], v[196:199], v[44:47], v[64:67]\n"
"s_barrier\n"
"v_xor_b32 v4, s37, v4\n"
"v_mfma_f32_16x16x32_bf16 v[60:63], v[200:203], v[48:51], v[60:63]\n"
"s_add_u32 s92, s51, s44\n"
"ds_read_b128 v[44:47], v113 offset:0\n"
"v_xor_b32 v5, s37, v5\n"
"v_mfma_f32_16x16x32_bf16 v[64:67], v[204:207], v[48:51], v[64:67]\n"
"s_waitcnt lgkmcnt(5)\n"
"v_xor_b32 v6, s37, v6\n"
"v_xor_b32 v7, s37, v7\n"
"v_mfma_f32_16x16x32_bf16 v[60:63], v[208:211], v[52:55], v[60:63]\n"
"s_add_u32 m0, s44, 49152\n"
"ds_read_b128 v[48:51], v113 offset:1024\n"
"v_xor_b32 v8, s37, v8\n"
"v_mfma_f32_16x16x32_bf16 v[64:67], v[212:215], v[52:55], v[64:67]\n"
"v_xor_b32 v9, s37, v9\n"
"v_xor_b32 v10, s37, v10\n"
"v_mfma_f32_16x16x32_bf16 v[60:63], v[216:219], v[56:59], v[60:63]\n"
"ds_read_b128 v[52:55], v113 offset:2048\n"
"buffer_load_dwordx4 v113, s[72:75], s92 offen offset:0 sc1 lds\n"
"v_mfma_f32_16x16x32_bf16 v[64:67], v[220:223], v[56:59], v[64:67]\n"
"s_waitcnt lgkmcnt(5)\n"
"v_xor_b32 v11, s37, v11\n"
"v_xor_b32 v12, s37, v12\n"
"v_mfma_f32_16x16x32_bf16 v[60:63], v[224:227], v[28:31], v[60:63]\n"
"ds_read_b128 v[56:59], v113 offset:3072\n"
"v_xor_b32 v13, s37, v13\n"
"v_mfma_f32_16x16x32_bf16 v[64:67], v[228:231], v[28:31], v[64:67]\n"
"v_xor_b32 v14, s37, v14\n"
"v_xor_b32 v15, s37, v15\n"
"v_mfma_f32_16x16x32_bf16 v[60:63], v[232:235], v[32:35], v[60:63]\n"
"ds_read_b128 v[28:31], v113 offset:4096\n"
"buffer_load_dwordx4 v113, s[72:75], s92 offen offset:1024 sc1 lds\n"
"v_mfma_f32_16x16x32_bf16 v[64:67], v[236:239], v[32:35], v[64:67]\n"
"s_waitcnt lgkmcnt(5)\n"
"v_or3_b32 v3, v4, v5, v6\n"
"v_or3_b32 v3, v3, v7, v8\n"
"v_mfma_f32_16x16x32_bf16 v[60:63], v[240:243], v[36:39], v[60:63]\n"
"ds_read_b128 v[32:35], v113 offset:5120\n"
"v_or3_b32 v3, v3, v9, v10\n"
"v_mfma_f32_16x16x32_bf16 v[64:67], v[244:247], v[36:39], v[64:67]\n"
"v_or3_b32 v3, v3, v11, v12\n"
"v_or3_b32 v3, v3, v13, v14\n"
"v_mfma_f32_16x16x32_bf16 v[60:63], v[248:251], v[40:43], v[60:63]\n"
"ds_read_b128 v[36:39], v113 offset:6144\n"
"buffer_load_dwordx4 v113, s[72:75], s92 offen offset:2048 sc1 lds\n"
"v_mfma_f32_16x16x32_bf16 v[64:67], v[252:255], v[40:43], v[64:67]\n"
"s_waitcnt lgkmcnt(5)\n"
"v_bitop3_b32 v3, v3, v15, v23 bitop3:0x54\n"
"v_cmp_ne_u32 vcc, 0, v3\n"
"v_mfma_f32_16x16x32_bf16 v[60:63], a[0:3], v[44:47], v[60:63]\n"
"ds_read_b128 v[40:43], v113 offset:7168\n"
"v_lshlrev_b32 v76, 16, v88\n"
"v_mfma_f32_16x16x32_bf16 v[64:67], a[4:7], v[44:47], v[64:67]\n"
"s_cbranch_vccnz Lslow_p0_K\n"
"Lret_p0_K:\n"
"v_and_b32 v77, 0xffff0000, v88\n"
"v_lshlrev_b32 v78, 16, v89\n"
"v_mfma_f32_16x16x32_bf16 v[60:63], a[8:11], v[48:51], v[60:63]\n"
"s_add_u32 s46, s46, 3145728\n"
"ds_read_b128 v[44:47], v113 offset:8192\n"
"buffer_load_dwordx4 v113, s[72:75], s92 offen offset:3072 sc1 lds\n"
"v_mfma_f32_16x16x32_bf16 v[64:67], a[12:15], v[48:51], v[64:67]\n"
"s_waitcnt lgkmcnt(5)\n"
"v_and_b32 v79, 0xffff0000, v89\n"
"v_lshlrev_b32 v80, 16, v90\n"
"v_mfma_f32_16x16x32_bf16 v[60:63], a[16:19], v[52:55], v[60:63]\n"
"ds_read_b128 v[48:51], v113 offset:9216\n"
"v_and_b32 v81, 0xffff0000, v90\n"
"v_mfma_f32_16x16x32_bf16 v[64:67], a[20:23], v[52:55], v[64:67]\n"
"v_lshlrev_b32 v82, 16, v91\n"
"v_and_b32 v83, 0xffff0000, v91\n"
"v_mfma_f32_16x16x32_bf16 v[60:63], a[24:27], v[56:59], v[60:63]\n"
"ds_read_b128 v[52:55], v113 offset:10240\n"
"v_lshl_or_b32 v3, v4, 16, v16\n"
"v_mfma_f32_16x16x32_bf16 v[64:67], a[28:31], v[56:59], v[64:67]\n"
"s_waitcnt lgkmcnt(5)\n"
"v_and_or_b32 v4, v4, v17, v16\n"
"v_fmac_f32 v76, v104, v3\n"
"v_mfma_f32_16x16x32_bf16 v[60:63], a[32:35], v[28:31], v[60:63]\n"
"s_waitcnt vmcnt(8)\n"
"ds_read_b128 v[56:59], v113 offset:11264\n"
"v_fmac_f32 v77, v105, v4\n"
"v_mfma_f32_16x16x32_bf16 v[64:67], a[36:39], v[28:31], v[64:67]\n"
"s_barrier\n"
"v_lshl_or_b32 v3, v5, 16, v16\n"
"v_and_or_b32 v5, v5, v17, v16\n"
"v_mfma_f32_16x16x32_bf16 v[60:63], a[40:43], v[32:35], v[60:63]\n"
"s_add_u32 s92, s51, s45\n"
"ds_read_b128 v[28:31], v113 offset:16384\n"
"v_fmac_f32 v78, v106, v3\n"
"v_mfma_f32_16x16x32_bf16 v[64:67], a[44:47], v[32:35], v[64:67]\n"
"s_waitcnt lgkmcnt(5)\n"
"v_fmac_f32 v79, v107, v5\n"
"v_lshl_or_b32 v3, v6, 16, v16\n"
"v_mfma_f32_16x16x32_bf16 v[60:63], a[48:51], v[36:39], v[60:63]\n"
"s_add_u32 m0, s45, 49152\n"
"ds_read_b128 v[32:35], v113 offset:17408\n"
"v_and_or_b32 v6, v6, v17, v16\n"
"v_mfma_f32_16x16x32_bf16 v[64:67], a[52:55], v[36:39], v[64:67]\n"
"v_fmac_f32 v80, v108, v3\n"
"v_fmac_f32 v81, v109, v6\n"
"v_mfma_f32_16x16x32_bf16 v[60:63], a[56:59], v[40:43], v[60:63]\n"
"ds_read_b128 v[36:39], v113 offset:18432\n"
"buffer_load_dwordx4 v113, s[72:75], s92 offen offset:0 sc1 lds\n"
"v_mfma_f32_16x16x32_bf16 v[64:67], a[60:63], v[40:43], v[64:67]\n"
"s_waitcnt lgkmcnt(5)\n"
"v_lshl_or_b32 v3, v7, 16, v16\n"
"v_and_or_b32 v7, v7, v17, v16\n"
"v_mfma_f32_16x16x32_bf16 v[60:63], a[64:67], v[44:47], v[60:63]\n"
"ds_read_b128 v[40:43], v113 offset:19456\n"
"v_fmac_f32 v82, v110, v3\n"
"v_mfma_f32_16x16x32_bf16 v[64:67], a[68:71], v[44:47], v[64:67]\n"
"v_fmac_f32 v83, v111, v7\n"
"v_lshl_or_b32 v3, v8, 16, v16\n"
"v_mfma_f32_16x16x32_bf16 v[60:63], a[72:75], v[48:51], v[60:63]\n"
"ds_read_b128 v[44:47], v113 offset:20480\n"
"v_and_or_b32 v8, v8, v17, v16\n"
"v_mfma_f32_16x16x32_bf16 v[64:67], a[76:79], v[48:51], v[64:67]\n"
"s_waitcnt lgkmcnt(5)\n"
"v_fmac_f32 v76, v104, v3\n"
"v_fmac_f32 v77, v105, v8\n"
"v_mfma_f32_16x16x32_bf16 v[60:63], a[80:83], v[52:55], v[60:63]\n"
"ds_read_b128 v[48:51], v113 offset:21504\n"
"buffer_load_dwordx4 v113, s[72:75], s92 offen offset:1024 sc1 lds\n"
"v_mfma_f32_16x16x32_bf16 v[64:67], a[84:87], v[52:55], v[64:67]\n"
"v_lshl_or_b32 v3, v9, 16, v16\n"
"v_and_or_b32 v9, v9, v17, v16\n"
"v_mfma_f32_16x16x32_bf16 v[60:63], a[88:91], v[56:59], v[60:63]\n"
"ds_read_b128 v[52:55], v113 offset:22528\n"
"v_fmac_f32 v78, v106, v3\n"
"v_mfma_f32_16x16x32_bf16 v[64:67], a[92:95], v[56:59], v[64:67]\n"
"s_waitcnt lgkmcnt(5)\n"
"v_fmac_f32 v79, v107, v9\n"
"v_lshl_or_b32 v3, v10, 16, v16\n"
"v_mfma_f32_16x16x32_bf16 v[68:71], v[128:131], v[28:31], 0\n"
"ds_read_b128 v[56:59], v113 offset:23552\n"
"v_and_or_b32 v10, v10, v17, v16\n"
"v_mfma_f32_16x16x32_bf16 v[72:75], v[132:135], v[28:31], 0\n"
"v_fmac_f32 v80, v108, v3\n"
"v_fmac_f32 v81, v109, v10\n"
"v_mfma_f32_16x16x32_bf16 v[68:71], v[136:139], v[32:35], v[68:71]\n"
"ds_read_b128 v[28:31], v113 offset:24576\n"
"buffer_load_dwordx4 v113, s[72:75], s92 offen offset:2048 sc1 lds\n"
"v_mfma_f32_16x16x32_bf16 v[72:75], v[140:143], v[32:35], v[72:75]\n"
"s_waitcnt lgkmcnt(5)\n"
"v_lshl_or_b32 v3, v11, 16, v16\n"
"v_and_or_b32 v11, v11, v17, v16\n"
"v_mfma_f32_16x16x32_bf16 v[68:71], v[144:147], v[36:39], v[68:71]\n"
"s_add_u32 s51, s51, 3670016\n"
"ds_read_b128 v[32:35], v113 offset:25600\n"
"v_cvt_pk_bf16_f32 v24, v60, v61\n"
"v_mfma_f32_16x16x32_bf16 v[72:75], v[148:151], v[36:39], v[72:75]\n"
"v_cvt_pk_bf16_f32 v25, v62, v63\n"
"v_cvt_pk_bf16_f32 v26, v64, v65\n"
"v_mfma_f32_16x16x32_bf16 v[68:71], v[152:155], v[40:43], v[68:71]\n"
"ds_read_b128 v[36:39], v113 offset:26624\n"
"v_cvt_pk_bf16_f32 v27, v66, v67\n"
"v_mfma_f32_16x16x32_bf16 v[72:75], v[156:159], v[40:43], v[72:75]\n"
"s_waitcnt lgkmcnt(5)\n"
"v_and_or_b32 v24, v24, v23, s37\n"
"v_and_or_b32 v25, v25, v23, s37\n"
"v_mfma_f32_16x16x32_bf16 v[68:71], v[160:163], v[44:47], v[68:71]\n"
"ds_read_b128 v[40:43], v113 offset:27648\n"
"v_and_or_b32 v26, v26, v23, s37\n"
"v_mfma_f32_16x16x32_bf16 v[72:75], v[164:167], v[44:47], v[72:75]\n"
"v_and_or_b32 v27, v27, v23, s37\n"
"v_fmac_f32 v82, v110, v3\n"
"v_mfma_f32_16x16x32_bf16 v[68:71], v[168:171], v[48:51], v[68:71]\n"
"buffer_store_dwordx4 v[24:27], v113, s[72:75], s56 offen sc1\n"
"ds_read_b128 v[44:47], v113 offset:28672\n"
"v_mfma_f32_16x16x32_bf16 v[72:75], v[172:175], v[48:51], v[72:75]\n"
"s_waitcnt lgkmcnt(5)\n"
"v_fmac_f32 v83, v111, v11\n"
"v_lshl_or_b32 v3, v12, 16, v16\n"
"v_mfma_f32_16x16x32_bf16 v[68:71], v[176:179], v[52:55], v[68:71]\n"
"s_add_u32 s56, s56, 3145728\n"
"ds_read_b128 v[48:51], v113 offset:29696\n"
"v_and_or_b32 v12, v12, v17, v16\n"
"v_mfma_f32_16x16x32_bf16 v[72:75], v[180:183], v[52:55], v[72:75]\n"
"v_fmac_f32 v76, v104, v3\n"
"v_fmac_f32 v77, v105, v12\n"
"v_mfma_f32_16x16x32_bf16 v[68:71], v[184:187], v[56:59], v[68:71]\n"
"ds_read_b128 v[52:55], v113 offset:30720\n"
"v_lshl_or_b32 v3, v13, 16, v16\n"
"v_mfma_f32_16x16x32_bf16 v[72:75], v[188:191], v[56:59], v[72:75]\n"
"s_waitcnt lgkmcnt(5)\n"
"v_and_or_b32 v13, v13, v17, v16\n"
"v_fmac_f32 v78, v106, v3\n"
"v_mfma_f32_16x16x32_bf16 v[68:71], v[192:195], v[28:31], v[68:71]\n"
"s_waitcnt vmcnt(9)\n"
"ds_read_b128 v[56:59], v113 offset:31744\n"
"v_fmac_f32 v79, v107, v13\n"
"v_mfma_f32_16x16x32_bf16 v[72:75], v[196:199], v[28:31], v[72:75]\n"
"s_barrier\n"
"v_lshl_or_b32 v3, v14, 16, v16\n"
"v_and_or_b32 v14, v14, v17, v16\n"
"v_mfma_f32_16x16x32_bf16 v[68:71], v[200:203], v[32:35], v[68:71]\n"
"s_add_u32 s92, s52, s44\n"
"ds_read_b128 v[28:31], v113 offset:32768\n"
"v_fmac_f32 v80, v108, v3\n"
"v_mfma_f32_16x16x32_bf16 v[72:75], v[204:207], v[32:35], v[72:75]\n"
"s_waitcnt lgkmcnt(5)\n"
"v_fmac_f32 v81, v109, v14\n"
"v_lshl_or_b32 v3, v15, 16, v16\n"
"v_mfma_f32_16x16x32_bf16 v[68:71], v[208:211], v[36:39], v[68:71]\n"
"s_add_u32 m0, s44, 0\n"
"ds_read_b128 v[32:35], v113 offset:33792\n"
"v_and_or_b32 v15, v15, v17, v16\n"
"v_mfma_f32_16x16x32_bf16 v[72:75], v[212:215], v[36:39], v[72:75]\n"
"v_fmac_f32 v82, v110, v3\n"
"v_fmac_f32 v83, v111, v15\n"
"v_mfma_f32_16x16x32_bf16 v[68:71], v[216:219], v[40:43], v[68:71]\n"
"ds_read_b128 v[36:39], v113 offset:34816\n"
"buffer_load_dwordx4 v113, s[72:75], s92 offen offset:0 sc1 lds\n"
"v_mfma_f32_16x16x32_bf16 v[72:75], v[220:223], v[40:43], v[72:75]\n"
"s_waitcnt lgkmcnt(5)\n"
"buffer_load_dwordx4 v[88:91], v126, s[72:75], s47 offen sc1\n"
"v_mfma_f32_16x16x32_bf16 v[68:71], v[224:227], v[44:47], v[68:71]\n"
"s_add_u32 s47, s47, 1048576\n"
"ds_read_b128 v[40:43], v113 offset:35840\n"
"v_mfma_f32_16x16x32_bf16 v[72:75], v[228:231], v[44:47], v[72:75]\n"
"s_add_u32 s95, s46, 1048576\n"
"v_mfma_f32_16x16x32_bf16 v[68:71], v[232:235], v[48:51], v[68:71]\n"
"s_add_u32 s61, s46, 2097152\n"
"ds_read_b128 v[44:47], v113 offset:36864\n"
"buffer_load_dwordx4 v113, s[72:75], s92 offen offset:1024 sc1 lds\n"
"v_mfma_f32_16x16x32_bf16 v[72:75], v[236:239], v[48:51], v[72:75]\n"
"s_waitcnt lgkmcnt(5)\n"
"v_mfma_f32_16x16x32_bf16 v[68:71], v[240:243], v[52:55], v[68:71]\n"
"ds_read_b128 v[48:51], v113 offset:37888\n"
"buffer_load_dwordx4 v[4:7], v113, s[72:75], s46 offen sc1\n"
"v_mfma_f32_16x16x32_bf16 v[72:75], v[244:247], v[52:55], v[72:75]\n"
"v_mfma_f32_16x16x32_bf16 v[68:71], v[248:251], v[56:59], v[68:71]\n"
"ds_read_b128 v[52:55], v113 offset:38912\n"
"buffer_load_dwordx4 v113, s[72:75], s92 offen offset:2048 sc1 lds\n"
"v_mfma_f32_16x16x32_bf16 v[72:75], v[252:255], v[56:59], v[72:75]\n"
"s_waitcnt lgkmcnt(5)\n"
"buffer_load_dwordx4 v[8:11], v113, s[72:75], s95 offen sc1\n"
"v_mfma_f32_16x16x32_bf16 v[68:71], a[0:3], v[28:31], v[68:71]\n"
"ds_read_b128 v[56:59], v113 offset:39936\n"
"v_mfma_f32_16x16x32_bf16 v[72:75], a[4:7], v[28:31], v[72:75]\n"
"buffer_load_dwordx4 v[12:15], v113, s[72:75], s61 offen sc1\n"
"v_mfma_f32_16x16x32_bf16 v[68:71], a[8:11], v[32:35], v[68:71]\n"
"ds_read_b128 v[28:31], v113 offset:40960\n"
"buffer_load_dwordx4 v113, s[72:75], s92 offen offset:3072 sc1 lds\n"
"v_mfma_f32_16x16x32_bf16 v[72:75], a[12:15], v[32:35], v[72:75]\n"
"s_waitcnt lgkmcnt(5)\n"
"v_mfma_f32_16x16x32_bf16 v[68:71], a[16:19], v[36:39], v[68:71]\n"
"ds_read_b128 v[32:35], v113 offset:41984\n"
"v_mfma_f32_16x16x32_bf16 v[72:75], a[20:23], v[36:39], v[72:75]\n"
"v_mfma_f32_16x16x32_bf16 v[68:71], a[24:27], v[40:43], v[68:71]\n"
"ds_read_b128 v[36:39], v113 offset:43008\n"
"v_mfma_f32_16x16x32_bf16 v[72:75], a[28:31], v[40:43], v[72:75]\n"
"s_waitcnt lgkmcnt(5)\n"
"v_mfma_f32_16x16x32_bf16 v[68:71], a[32:35], v[44:47], v[68:71]\n"
"s_waitcnt vmcnt(12)\n"
"ds_read_b128 v[40:43], v113 offset:44032\n"
"v_mfma_f32_16x16x32_bf16 v[72:75], a[36:39], v[44:47], v[72:75]\n"
"s_barrier\n"
"v_mfma_f32_16x16x32_bf16 v[68:71], a[40:43], v[48:51], v[68:71]\n"
"s_add_u32 s92, s52, s45\n"
"ds_read_b128 v[44:47], v113 offset:49152\n"
"v_mfma_f32_16x16x32_bf16 v[72:75], a[44:47], v[48:51], v[72:75]\n"
"s_waitcnt lgkmcnt(5)\n"
"v_mfma_f32_16x16x32_bf16 v[68:71], a[48:51], v[52:55], v[68:71]\n"
"s_add_u32 m0, s45, 0\n"
"ds_read_b128 v[48:51], v113 offset:50176\n"
"v_mfma_f32_16x16x32_bf16 v[72:75], a[52:55], v[52:55], v[72:75]\n"
"v_mfma_f32_16x16x32_bf16 v[68:71], a[56:59], v[56:59], v[68:71]\n"
"ds_read_b128 v[52:55], v113 offset:51200\n"
"buffer_load_dwordx4 v113, s[72:75], s92 offen offset:0 sc1 lds\n"
"v_mfma_f32_16x16x32_bf16 v[72:75], a[60:63], v[56:59], v[72:75]\n"
"s_waitcnt lgkmcnt(5)\n"
"v_mfma_f32_16x16x32_bf16 v[68:71], a[64:67], v[28:31], v[68:71]\n"
"ds_read_b128 v[56:59], v113 offset:52224\n"
"v_mfma_f32_16x16x32_bf16 v[72:75], a[68:71], v[28:31], v[72:75]\n"
"v_mfma_f32_16x16x32_bf16 v[68:71], a[72:75], v[32:35], v[68:71]\n"
"ds_read_b128 v[28:31], v113 offset:53248\n"
"v_mfma_f32_16x16x32_bf16 v[72:75], a[76:79], v[32:35], v[72:75]\n"
"s_waitcnt lgkmcnt(5)\n"
"v_mfma_f32_16x16x32_bf16 v[68:71], a[80:83], v[36:39], v[68:71]\n"
"ds_read_b128 v[32:35], v113 offset:54272\n"
"buffer_load_dwordx4 v113, s[72:75], s92 offen offset:1024 sc1 lds\n"
"v_mfma_f32_16x16x32_bf16 v[72:75], a[84:87], v[36:39], v[72:75]\n"
"v_mfma_f32_16x16x32_bf16 v[68:71], a[88:91], v[40:43], v[68:71]\n"
"ds_read_b128 v[36:39], v113 offset:55296\n"
"v_mfma_f32_16x16x32_bf16 v[72:75], a[92:95], v[40:43], v[72:75]\n"
"s_waitcnt lgkmcnt(5)\n"
"v_mfma_f32_16x16x32_bf16 v[60:63], v[128:131], v[44:47], 0\n"
"ds_read_b128 v[40:43], v113 offset:56320\n"
"v_mfma_f32_16x16x32_bf16 v[64:67], v[132:135], v[44:47], 0\n"
"v_mfma_f32_16x16x32_bf16 v[60:63], v[136:139], v[48:51], v[60:63]\n"
"ds_read_b128 v[44:47], v113 offset:57344\n"
"buffer_load_dwordx4 v113, s[72:75], s92 offen offset:2048 sc1 lds\n"
"v_mfma_f32_16x16x32_bf16 v[64:67], v[140:143], v[48:51], v[64:67]\n"
"s_waitcnt lgkmcnt(5)\n"
"v_mfma_f32_16x16x32_bf16 v[60:63], v[144:147], v[52:55], v[60:63]\n"
"s_add_u32 s52, s52, 3670016\n"
"ds_read_b128 v[48:51], v113 offset:58368\n"
"v_cvt_pk_bf16_f32 v24, v68, v69\n"
"v_mfma_f32_16x16x32_bf16 v[64:67], v[148:151], v[52:55], v[64:67]\n"
"v_cvt_pk_bf16_f32 v25, v70, v71\n"
"v_cvt_pk_bf16_f32 v26, v72, v73\n"
"v_mfma_f32_16x16x32_bf16 v[60:63], v[152:155], v[56:59], v[60:63]\n"
"ds_read_b128 v[52:55], v113 offset:59392\n"
"v_cvt_pk_bf16_f32 v27, v74, v75\n"
"v_mfma_f32_16x16x32_bf16 v[64:67], v[156:159], v[56:59], v[64:67]\n"
"s_waitcnt lgkmcnt(5)\n"
"v_and_or_b32 v24, v24, v23, s37\n"
"v_and_or_b32 v25, v25, v23, s37\n"
"v_mfma_f32_16x16x32_bf16 v[60:63], v[160:163], v[28:31], v[60:63]\n"
"ds_read_b128 v[56:59], v113 offset:60416\n"
"v_and_or_b32 v26, v26, v23, s37\n"
"v_mfma_f32_16x16x32_bf16 v[64:67], v[164:167], v[28:31], v[64:67]\n"
"v_and_or_b32 v27, v27, v23, s37\n"
"v_mfma_f32_16x16x32_bf16 v[60:63], v[168:171], v[32:35], v[60:63]\n"
"buffer_store_dwordx4 v[24:27], v113, s[72:75], s57 offen sc1\n"
"ds_read_b128 v[28:31], v113 offset:61440\n"
"v_mfma_f32_16x16x32_bf16 v[64:67], v[172:175], v[32:35], v[64:67]\n"
"s_waitcnt lgkmcnt(5)\n"
"v_mfma_f32_16x16x32_bf16 v[60:63], v[176:179], v[36:39], v[60:63]\n"
"s_add_u32 s57, s57, 3145728\n"
"ds_read_b128 v[32:35], v113 offset:62464\n"
"v_mfma_f32_16x16x32_bf16 v[64:67], v[180:183], v[36:39], v[64:67]\n"
"v_mfma_f32_16x16x32_bf16 v[60:63], v[184:187], v[40:43], v[60:63]\n"
"ds_read_b128 v[36:39], v113 offset:63488\n"
"v_mfma_f32_16x16x32_bf16 v[64:67], v[188:191], v[40:43], v[64:67]\n"
"s_waitcnt lgkmcnt(5)\n"
"v_mfma_f32_16x16x32_bf16 v[60:63], v[192:195], v[44:47], v[60:63]\n"
"s_waitcnt vmcnt(13)\n"
"ds_read_b128 v[40:43], v113 offset:64512\n"
"v_mfma_f32_16x16x32_bf16 v[64:67], v[196:199], v[44:47], v[64:67]\n"
"s_barrier\n"
"v_mfma_f32_16x16x32_bf16 v[60:63], v[200:203], v[48:51], v[60:63]\n"
"s_add_u32 s92, s53, s44\n"
"ds_read_b128 v[44:47], v118 offset:0\n"
"v_mfma_f32_16x16x32_bf16 v[64:67], v[204:207], v[48:51], v[64:67]\n"
"s_waitcnt lgkmcnt(5)\n"
"v_mfma_f32_16x16x32_bf16 v[60:63], v[208:211], v[52:55], v[60:63]\n"
"s_add_u32 m0, s44, 32768\n"
"ds_read_b128 v[48:51], v118 offset:1024\n"
"v_mfma_f32_16x16x32_bf16 v[64:67], v[212:215], v[52:55], v[64:67]\n"
"v_mfma_f32_16x16x32_bf16 v[60:63], v[216:219], v[56:59], v[60:63]\n"
"ds_read_b128 v[52:55], v118 offset:2048\n"
"buffer_load_dwordx4 v113, s[72:75], s92 offen offset:0 sc1 lds\n"
"v_mfma_f32_16x16x32_bf16 v[64:67], v[220:223], v[56:59], v[64:67]\n"
"s_waitcnt lgkmcnt(5)\n"
"v_mfma_f32_16x16x32_bf16 v[60:63], v[224:227], v[28:31], v[60:63]\n"
"ds_read_b128 v[56:59], v118 offset:3072\n"
"v_mfma_f32_16x16x32_bf16 v[64:67], v[228:231], v[28:31], v[64:67]\n"
"v_mfma_f32_16x16x32_bf16 v[60:63], v[232:235], v[32:35], v[60:63]\n"
"ds_read_b128 v[28:31], v118 offset:4096\n"
"buffer_load_dwordx4 v113, s[72:75], s92 offen offset:1024 sc1 lds\n"
"v_mfma_f32_16x16x32_bf16 v[64:67], v[236:239], v[32:35], v[64:67]\n"
"s_waitcnt lgkmcnt(5)\n"
"v_mfma_f32_16x16x32_bf16 v[60:63], v[240:243], v[36:39], v[60:63]\n"
"ds_read_b128 v[32:35], v118 offset:5120\n"
"v_mfma_f32_16x16x32_bf16 v[64:67], v[244:247], v[36:39], v[64:67]\n"
"v_mfma_f32_16x16x32_bf16 v[60:63], v[248:251], v[40:43], v[60:63]\n"
"ds_read_b128 v[36:39], v118 offset:6144\n"
"buffer_load_dwordx4 v113, s[72:75], s92 offen offset:2048 sc1 lds\n"
"v_mfma_f32_16x16x32_bf16 v[64:67], v[252:255], v[40:43], v[64:67]\n"
"s_waitcnt lgkmcnt(5)\n"
"v_mfma_f32_16x16x32_bf16 v[60:63], a[0:3], v[44:47], v[60:63]\n"
"ds_read_b128 v[40:43], v118 offset:7168\n"
"v_mfma_f32_16x16x32_bf16 v[64:67], a[4:7], v[44:47], v[64:67]\n"
"v_mfma_f32_16x16x32_bf16 v[60:63], a[8:11], v[48:51], v[60:63]\n"
"ds_read_b128 v[44:47], v118 offset:8192\n"
"buffer_load_dwordx4 v113, s[72:75], s92 offen offset:3072 sc1 lds\n"
"v_mfma_f32_16x16x32_bf16 v[64:67], a[12:15], v[48:51], v[64:67]\n"
"s_waitcnt lgkmcnt(5)\n"
"v_mfma_f32_16x16x32_bf16 v[60:63], a[16:19], v[52:55], v[60:63]\n"
"ds_read_b128 v[48:51], v118 offset:9216\n"
"v_mfma_f32_16x16x32_bf16 v[64:67], a[20:23], v[52:55], v[64:67]\n"
"v_mfma_f32_16x16x32_bf16 v[60:63], a[24:27], v[56:59], v[60:63]\n"
"ds_read_b128 v[52:55], v118 offset:10240\n"
"v_mfma_f32_16x16x32_bf16 v[64:67], a[28:31], v[56:59], v[64:67]\n"
"s_waitcnt lgkmcnt(5)\n"
"v_mfma_f32_16x16x32_bf16 v[60:63], a[32:35], v[28:31], v[60:63]\n"
"s_waitcnt vmcnt(8)\n"
"ds_read_b128 v[56:59], v118 offset:11264\n"
"v_mfma_f32_16x16x32_bf16 v[64:67], a[36:39], v[28:31], v[64:67]\n"
"s_barrier\n"
"v_mfma_f32_16x16x32_bf16 v[60:63], a[40:43], v[32:35], v[60:63]\n"
"s_add_u32 s92, s53, s45\n"
"ds_read_b128 v[28:31], v113 offset:0\n"
"v_mfma_f32_16x16x32_bf16 v[64:67], a[44:47], v[32:35], v[64:67]\n"
"s_waitcnt lgkmcnt(5)\n"
"v_mfma_f32_16x16x32_bf16 v[60:63], a[48:51], v[36:39], v[60:63]\n"
"s_add_u32 m0, s45, 32768\n"
"ds_read_b128 v[32:35], v113 offset:1024\n"
"v_mfma_f32_16x16x32_bf16 v[64:67], a[52:55], v[36:39], v[64:67]\n"
"v_mfma_f32_16x16x32_bf16 v[60:63], a[56:59], v[40:43], v[60:63]\n"
"ds_read_b128 v[36:39], v113 offset:2048\n"
"buffer_load_dwordx4 v113, s[72:75], s92 offen offset:0 sc1 lds\n"
"v_mfma_f32_16x16x32_bf16 v[64:67], a[60:63], v[40:43], v[64:67]\n"
"s_waitcnt lgkmcnt(5)\n"
"v_mfma_f32_16x16x32_bf16 v[60:63], a[64:67], v[44:47], v[60:63]\n"
"ds_read_b128 v[40:43], v113 offset:3072\n"
"v_mfma_f32_16x16x32_bf16 v[64:67], a[68:71], v[44:47], v[64:67]\n"
"v_mfma_f32_16x16x32_bf16 v[60:63], a[72:75], v[48:51], v[60:63]\n"
"ds_read_b128 v[44:47], v113 offset:4096\n"
"v_mfma_f32_16x16x32_bf16 v[64:67], a[76:79], v[48:51], v[64:67]\n"
"s_waitcnt lgkmcnt(5)\n"
"v_mfma_f32_16x16x32_bf16 v[60:63], a[80:83], v[52:55], v[60:63]\n"
"ds_read_b128 v[48:51], v113 offset:5120\n"
"buffer_load_dwordx4 v113, s[72:75], s92 offen offset:1024 sc1 lds\n"
"v_mfma_f32_16x16x32_bf16 v[64:67], a[84:87], v[52:55], v[64:67]\n"
"v_mfma_f32_16x16x32_bf16 v[60:63], a[88:91], v[56:59], v[60:63]\n"
"ds_read_b128 v[52:55], v113 offset:6144\n"
"v_mfma_f32_16x16x32_bf16 v[64:67], a[92:95], v[56:59], v[64:67]\n"
"s_waitcnt lgkmcnt(5)\n"
"v_mfma_f32_16x16x32_bf16 v[68:71], v[128:131], v[28:31], 0\n"
"ds_read_b128 v[56:59], v113 offset:7168\n"
"v_mfma_f32_16x16x32_bf16 v[72:75], v[132:135], v[28:31], 0\n"
"v_mfma_f32_16x16x32_bf16 v[68:71], v[136:139], v[32:35], v[68:71]\n"
"ds_read_b128 v[28:31], v113 offset:8192\n"
"buffer_load_dwordx4 v113, s[72:75], s92 offen offset:2048 sc1 lds\n"
"v_mfma_f32_16x16x32_bf16 v[72:75], v[140:143], v[32:35], v[72:75]\n"
"s_waitcnt lgkmcnt(5)\n"
"v_mfma_f32_16x16x32_bf16 v[68:71], v[144:147], v[36:39], v[68:71]\n"
"s_add_u32 s53, s53, 3670016\n"
"ds_read_b128 v[32:35], v113 offset:9216\n"
"v_cvt_pk_bf16_f32 v24, v60, v61\n"
"v_mfma_f32_16x16x32_bf16 v[72:75], v[148:151], v[36:39], v[72:75]\n"
"v_cvt_pk_bf16_f32 v25, v62, v63\n"
"v_cvt_pk_bf16_f32 v26, v64, v65\n"
"v_mfma_f32_16x16x32_bf16 v[68:71], v[152:155], v[40:43], v[68:71]\n"
"ds_read_b128 v[36:39], v113 offset:10240\n"
"v_cvt_pk_bf16_f32 v27, v66, v67\n"
"v_mfma_f32_16x16x32_bf16 v[72:75], v[156:159], v[40:43], v[72:75]\n"
"s_waitcnt lgkmcnt(5)\n"
"v_and_or_b32 v24, v24, v23, s37\n"
"v_and_or_b32 v25, v25, v23, s37\n"
"v_mfma_f32_16x16x32_bf16 v[68:71], v[160:163], v[44:47], v[68:71]\n"
"ds_read_b128 v[40:43], v113 offset:11264\n"
"v_and_or_b32 v26, v26, v23, s37\n"
"v_mfma_f32_16x16x32_bf16 v[72:75], v[164:167], v[44:47], v[72:75]\n"
"v_and_or_b32 v27, v27, v23, s37\n"
"v_mfma_f32_16x16x32_bf16 v[68:71], v[168:171], v[48:51], v[68:71]\n"
"buffer_store_dwordx4 v[24:27], v113, s[72:75], s55 offen sc1\n"
"ds_read_b128 v[44:47], v113 offset:12288\n"
"v_mfma_f32_16x16x32_bf16 v[72:75], v[172:175], v[48:51], v[72:75]\n"
"s_waitcnt lgkmcnt(5)\n"
"v_mfma_f32_16x16x32_bf16 v[68:71], v[176:179], v[52:55], v[68:71]\n"
"s_add_u32 s55, s55, 3145728\n"
"ds_read_b128 v[48:51], v113 offset:13312\n"
"v_mfma_f32_16x16x32_bf16 v[72:75], v[180:183], v[52:55], v[72:75]\n"
"v_xor_b32 v4, s37, v4\n"
"v_xor_b32 v5, s37, v5\n"
"v_mfma_f32_16x16x32_bf16 v[68:71], v[184:187], v[56:59], v[68:71]\n"
"ds_read_b128 v[52:55], v113 offset:14336\n"
"v_xor_b32 v6, s37, v6\n"
"v_mfma_f32_16x16x32_bf16 v[72:75], v[188:191], v[56:59], v[72:75]\n"
"s_waitcnt lgkmcnt(5)\n"
"v_xor_b32 v7, s37, v7\n"
"v_xor_b32 v8, s37, v8\n"
"v_mfma_f32_16x16x32_bf16 v[68:71], v[192:195], v[28:31], v[68:71]\n"
"s_waitcnt vmcnt(9)\n"
"ds_read_b128 v[56:59], v113 offset:15360\n"
"v_xor_b32 v9, s37, v9\n"
"v_mfma_f32_16x16x32_bf16 v[72:75], v[196:199], v[28:31], v[72:75]\n"
"s_barrier\n"
"v_xor_b32 v10, s37, v10\n"
"v_xor_b32 v11, s37, v11\n"
"v_mfma_f32_16x16x32_bf16 v[68:71], v[200:203], v[32:35], v[68:71]\n"
"s_add_u32 s92, s54, s44\n"
"ds_read_b128 v[28:31], v113 offset:16384\n"
"v_xor_b32 v12, s37, v12\n"
"v_mfma_f32_16x16x32_bf16 v[72:75], v[204:207], v[32:35], v[72:75]\n"
"s_waitcnt lgkmcnt(5)\n"
"v_xor_b32 v13, s37, v13\n"
"v_xor_b32 v14, s37, v14\n"
"v_mfma_f32_16x16x32_bf16 v[68:71], v[208:211], v[36:39], v[68:71]\n"
"s_add_u32 m0, s44, 65536\n"
"ds_read_b128 v[32:35], v113 offset:17408\n"
"v_xor_b32 v15, s37, v15\n"
"v_mfma_f32_16x16x32_bf16 v[72:75], v[212:215], v[36:39], v[72:75]\n"
"v_or3_b32 v3, v4, v5, v6\n"
"v_or3_b32 v3, v3, v7, v8\n"
"v_mfma_f32_16x16x32_bf16 v[68:71], v[216:219], v[40:43], v[68:71]\n"
"ds_read_b128 v[36:39], v113 offset:18432\n"
"buffer_load_dwordx4 v113, s[72:75], s92 offen offset:0 sc1 lds\n"
"v_mfma_f32_16x16x32_bf16 v[72:75], v[220:223], v[40:43], v[72:75]\n"
"s_waitcnt lgkmcnt(5)\n"
"v_or3_b32 v3, v3, v9, v10\n"
"v_or3_b32 v3, v3, v11, v12\n"
"v_mfma_f32_16x16x32_bf16 v[68:71], v[224:227], v[44:47], v[68:71]\n"
"ds_read_b128 v[40:43], v113 offset:19456\n"
"v_or3_b32 v3, v3, v13, v14\n"
"v_mfma_f32_16x16x32_bf16 v[72:75], v[228:231], v[44:47], v[72:75]\n"
"v_bitop3_b32 v3, v3, v15, v23 bitop3:0x54\n"
"v_cmp_ne_u32 vcc, 0, v3\n"
"v_mfma_f32_16x16x32_bf16 v[68:71], v[232:235], v[48:51], v[68:71]\n"
"ds_read_b128 v[44:47], v113 offset:20480\n"
"buffer_load_dwordx4 v113, s[72:75], s92 offen offset:1024 sc1 lds\n"
"v_mfma_f32_16x16x32_bf16 v[72:75], v[236:239], v[48:51], v[72:75]\n"
"s_waitcnt lgkmcnt(5)\n"
"v_lshlrev_b32 v114, 16, v88\n"
"v_and_b32 v115, 0xffff0000, v88\n"
"v_mfma_f32_16x16x32_bf16 v[68:71], v[240:243], v[52:55], v[68:71]\n"
"s_cbranch_vccnz Lslow_p1_K\n"
"Lret_p1_K:\n"
"ds_read_b128 v[48:51], v113 offset:21504\n"
"v_lshlrev_b32 v116, 16, v89\n"
"v_mfma_f32_16x16x32_bf16 v[72:75], v[244:247], v[52:55], v[72:75]\n"
"s_add_u32 s46, s46, 3145728\n"
"v_and_b32 v117, 0xffff0000, v89\n"
"v_lshlrev_b32 v122, 16, v90\n"
"v_mfma_f32_16x16x32_bf16 v[68:71], v[248:251], v[56:59], v[68:71]\n"
"ds_read_b128 v[52:55], v113 offset:22528\n"
"buffer_load_dwordx4 v113, s[72:75], s92 offen offset:2048 sc1 lds\n"
"v_mfma_f32_16x16x32_bf16 v[72:75], v[252:255], v[56:59], v[72:75]\n"
"s_waitcnt lgkmcnt(5)\n"
"v_and_b32 v123, 0xffff0000, v90\n"
"v_lshlrev_b32 v124, 16, v91\n"
"v_mfma_f32_16x16x32_bf16 v[68:71], a[0:3], v[28:31], v[68:71]\n"
"ds_read_b128 v[56:59], v113 offset:23552\n"
"v_and_b32 v125, 0xffff0000, v91\n"
"v_mfma_f32_16x16x32_bf16 v[72:75], a[4:7], v[28:31], v[72:75]\n"
"v_lshl_or_b32 v3, v4, 16, v16\n"
"v_and_or_b32 v4, v4, v17, v16\n"
"v_mfma_f32_16x16x32_bf16 v[68:71], a[8:11], v[32:35], v[68:71]\n"
"ds_read_b128 v[28:31], v113 offset:24576\n"
"buffer_load_dwordx4 v113, s[72:75], s92 offen offset:3072 sc1 lds\n"
"v_mfma_f32_16x16x32_bf16 v[72:75], a[12:15], v[32:35], v[72:75]\n"
"s_waitcnt lgkmcnt(5)\n"
"v_fmac_f32 v114, v104, v3\n"
"v_fmac_f32 v115, v105, v4\n"
"v_mfma_f32_16x16x32_bf16 v[68:71], a[16:19], v[36:39], v[68:71]\n"
"ds_read_b128 v[32:35], v113 offset:25600\n"
"v_lshl_or_b32 v3, v5, 16, v16\n"
"v_mfma_f32_16x16x32_bf16 v[72:75], a[20:23], v[36:39], v[72:75]\n"
"v_and_or_b32 v5, v5, v17, v16\n"
"v_fmac_f32 v116, v106, v3\n"
"v_mfma_f32_16x16x32_bf16 v[68:71], a[24:27], v[40:43], v[68:71]\n"
"ds_read_b128 v[36:39], v113 offset:26624\n"
"v_fmac_f32 v117, v107, v5\n"
"v_mfma_f32_16x16x32_bf16 v[72:75], a[28:31], v[40:43], v[72:75]\n"
"s_waitcnt lgkmcnt(5)\n"
"v_lshl_or_b32 v3, v6, 16, v16\n"
"v_and_or_b32 v6, v6, v17, v16\n"
"v_mfma_f32_16x16x32_bf16 v[68:71], a[32:35], v[44:47], v[68:71]\n"
"s_waitcnt vmcnt(8)\n"
"ds_read_b128 v[40:43], v113 offset:27648\n"
"v_fmac_f32 v122, v108, v3\n"
"v_mfma_f32_16x16x32_bf16 v[72:75], a[36:39], v[44:47], v[72:75]\n"
"s_barrier\n"
"v_fmac_f32 v123, v109, v6\n"
"v_lshl_or_b32 v3, v7, 16, v16\n"
"v_mfma_f32_16x16x32_bf16 v[68:71], a[40:43], v[48:51], v[68:71]\n"
"s_add_u32 s92, s54, s45\n"
"ds_read_b128 v[44:47], v113 offset:32768\n"
"v_and_or_b32 v7, v7, v17, v16\n"
"v_mfma_f32_16x16x32_bf16 v[72:75], a[44:47], v[48:51], v[72:75]\n"
"s_waitcnt lgkmcnt(5)\n"
"v_fmac_f32 v124, v110, v3\n"
"v_fmac_f32 v125, v111, v7\n"
"v_mfma_f32_16x16x32_bf16 v[68:71], a[48:51], v[52:55], v[68:71]\n"
"s_add_u32 m0, s45, -16384\n"
"ds_read_b128 v[48:51], v113 offset:33792\n"
"v_lshl_or_b32 v3, v8, 16, v16\n"
"v_mfma_f32_16x16x32_bf16 v[72:75], a[52:55], v[52:55], v[72:75]\n"
"v_and_or_b32 v8, v8, v17, v16\n"
"v_fmac_f32 v114, v104, v3\n"
"v_mfma_f32_16x16x32_bf16 v[68:71], a[56:59], v[56:59], v[68:71]\n"
"ds_read_b128 v[52:55], v113 offset:34816\n"
"buffer_load_dwordx4 v113, s[72:75], s92 offen offset:0 sc1 lds\n"
"v_mfma_f32_16x16x32_bf16 v[72:75], a[60:63], v[56:59], v[72:75]\n"
"s_waitcnt lgkmcnt(5)\n"
"v_fmac_f32 v115, v105, v8\n"
"v_lshl_or_b32 v3, v9, 16, v16\n"
"v_mfma_f32_16x16x32_bf16 v[68:71], a[64:67], v[28:31], v[68:71]\n"
"ds_read_b128 v[56:59], v113 offset:35840\n"
"v_and_or_b32 v9, v9, v17, v16\n"
"v_mfma_f32_16x16x32_bf16 v[72:75], a[68:71], v[28:31], v[72:75]\n"
"v_fmac_f32 v116, v106, v3\n"
"v_fmac_f32 v117, v107, v9\n"
"v_mfma_f32_16x16x32_bf16 v[68:71], a[72:75], v[32:35], v[68:71]\n"
"ds_read_b128 v[28:31], v113 offset:36864\n"
"v_lshl_or_b32 v3, v10, 16, v16\n"
"v_mfma_f32_16x16x32_bf16 v[72:75], a[76:79], v[32:35], v[72:75]\n"
"s_waitcnt lgkmcnt(5)\n"
"v_and_or_b32 v10, v10, v17, v16\n"
"v_fmac_f32 v122, v108, v3\n"
"v_mfma_f32_16x16x32_bf16 v[68:71], a[80:83], v[36:39], v[68:71]\n"
"ds_read_b128 v[32:35], v113 offset:37888\n"
"buffer_load_dwordx4 v113, s[72:75], s92 offen offset:1024 sc1 lds\n"
"v_mfma_f32_16x16x32_bf16 v[72:75], a[84:87], v[36:39], v[72:75]\n"
"v_fmac_f32 v123, v109, v10\n"
"v_lshl_or_b32 v3, v11, 16, v16\n"
"v_mfma_f32_16x16x32_bf16 v[68:71], a[88:91], v[40:43], v[68:71]\n"
"ds_read_b128 v[36:39], v113 offset:38912\n"
"v_and_or_b32 v11, v11, v17, v16\n"
"v_mfma_f32_16x16x32_bf16 v[72:75], a[92:95], v[40:43], v[72:75]\n"
"s_waitcnt lgkmcnt(5)\n"
"v_fmac_f32 v124, v110, v3\n"
"v_fmac_f32 v125, v111, v11\n"
"v_mfma_f32_16x16x32_bf16 v[60:63], v[128:131], v[44:47], 0\n"
"ds_read_b128 v[40:43], v113 offset:39936\n"
"v_lshl_or_b32 v3, v12, 16, v16\n"
"v_mfma_f32_16x16x32_bf16 v[64:67], v[132:135], v[44:47], 0\n"
"v_and_or_b32 v12, v12, v17, v16\n"
"v_fmac_f32 v114, v104, v3\n"
"v_mfma_f32_16x16x32_bf16 v[60:63], v[136:139], v[48:51], v[60:63]\n"
"ds_read_b128 v[44:47], v113 offset:40960\n"
"buffer_load_dwordx4 v113, s[72:75], s92 offen offset:2048 sc1 lds\n"
"v_mfma_f32_16x16x32_bf16 v[64:67], v[140:143], v[48:51], v[64:67]\n"
"s_waitcnt lgkmcnt(5)\n"
"v_fmac_f32 v115, v105, v12\n"
"v_lshl_or_b32 v3, v13, 16, v16\n"
"v_mfma_f32_16x16x32_bf16 v[60:63], v[144:147], v[52:55], v[60:63]\n"
"s_add_u32 s54, s54, 3670016\n"
"ds_read_b128 v[48:51], v113 offset:41984\n"
"v_cvt_pk_bf16_f32 v24, v68, v69\n"
"v_mfma_f32_16x16x32_bf16 v[64:67], v[148:151], v[52:55], v[64:67]\n"
"v_cvt_pk_bf16_f32 v25, v70, v71\n"
"v_cvt_pk_bf16_f32 v26, v72, v73\n"
"v_mfma_f32_16x16x32_bf16 v[60:63], v[152:155], v[56:59], v[60:63]\n"
"ds_read_b128 v[52:55], v113 offset:43008\n"
"v_cvt_pk_bf16_f32 v27, v74, v75\n"
"v_mfma_f32_16x16x32_bf16 v[64:67], v[156:159], v[56:59], v[64:67]\n"
"s_waitcnt lgkmcnt(5)\n"
"v_and_or_b32 v24, v24, v23, s37\n"
"v_and_or_b32 v25, v25, v23, s37\n"
"v_mfma_f32_16x16x32_bf16 v[60:63], v[160:163], v[28:31], v[60:63]\n"
"ds_read_b128 v[56:59], v113 offset:44032\n"
"v_and_or_b32 v26, v26, v23, s37\n"
"v_mfma_f32_16x16x32_bf16 v[64:67], v[164:167], v[28:31], v[64:67]\n"
"v_and_or_b32 v27, v27, v23, s37\n"
"v_and_or_b32 v13, v13, v17, v16\n"
"v_mfma_f32_16x16x32_bf16 v[60:63], v[168:171], v[32:35], v[60:63]\n"
"buffer_store_dwordx4 v[24:27], v113, s[72:75], s56 offen sc1\n"
"ds_read_b128 v[28:31], v113 offset:45056\n"
"v_mfma_f32_16x16x32_bf16 v[64:67], v[172:175], v[32:35], v[64:67]\n"
"s_waitcnt lgkmcnt(5)\n"
"v_fmac_f32 v116, v106, v3\n"
"v_fmac_f32 v117, v107, v13\n"
"v_mfma_f32_16x16x32_bf16 v[60:63], v[176:179], v[36:39], v[60:63]\n"
"s_add_u32 s56, s56, 3145728\n"
"ds_read_b128 v[32:35], v113 offset:46080\n"
"v_lshl_or_b32 v3, v14, 16, v16\n"
"v_mfma_f32_16x16x32_bf16 v[64:67], v[180:183], v[36:39], v[64:67]\n"
"v_and_or_b32 v14, v14, v17, v16\n"
"v_fmac_f32 v122, v108, v3\n"
"v_mfma_f32_16x16x32_bf16 v[60:63], v[184:187], v[40:43], v[60:63]\n"
"ds_read_b128 v[36:39], v113 offset:47104\n"
"v_fmac_f32 v123, v109, v14\n"
"v_mfma_f32_16x16x32_bf16 v[64:67], v[188:191], v[40:43], v[64:67]\n"
"s_waitcnt lgkmcnt(5)\n"
"v_lshl_or_b32 v3, v15, 16, v16\n"
"v_and_or_b32 v15, v15, v17, v16\n"
"v_mfma_f32_16x16x32_bf16 v[60:63], v[192:195], v[44:47], v[60:63]\n"
"s_waitcnt vmcnt(9)\n"
"ds_read_b128 v[40:43], v113 offset:48128\n"
"v_fmac_f32 v124, v110, v3\n"
"v_mfma_f32_16x16x32_bf16 v[64:67], v[196:199], v[44:47], v[64:67]\n"
"s_barrier\n"
"v_fmac_f32 v125, v111, v15\n"
"v_mfma_f32_16x16x32_bf16 v[60:63], v[200:203], v[48:51], v[60:63]\n"
"s_add_u32 s92, s54, s44\n"
"ds_read_b128 v[44:47], v113 offset:49152\n"
"v_mfma_f32_16x16x32_bf16 v[64:67], v[204:207], v[48:51], v[64:67]\n"
"s_waitcnt lgkmcnt(5)\n"
"buffer_load_dwordx4 v[88:91], v126, s[72:75], s47 offen sc1\n"
"v_mfma_f32_16x16x32_bf16 v[60:63], v[208:211], v[52:55], v[60:63]\n"
"s_add_u32 m0, s44, 16384\n"
"ds_read_b128 v[48:51], v113 offset:50176\n"
"v_mfma_f32_16x16x32_bf16 v[64:67], v[212:215], v[52:55], v[64:67]\n"
"s_add_u32 s47, s47, 1048576\n"
"v_mfma_f32_16x16x32_bf16 v[60:63], v[216:219], v[56:59], v[60:63]\n"
"s_add_u32 s95, s46, 1048576\n"
"ds_read_b128 v[52:55], v113 offset:51200\n"
"buffer_load_dwordx4 v113, s[72:75], s92 offen offset:0 sc1 lds\n"
"v_mfma_f32_16x16x32_bf16 v[64:67], v[220:223], v[56:59], v[64:67]\n"
"s_waitcnt lgkmcnt(5)\n"
"v_mfma_f32_16x16x32_bf16 v[60:63], v[224:227], v[28:31], v[60:63]\n"
"s_add_u32 s61, s46, 2097152\n"
"ds_read_b128 v[56:59], v113 offset:52224\n"
"v_mfma_f32_16x16x32_bf16 v[64:67], v[228:231], v[28:31], v[64:67]\n"
"v_mfma_f32_16x16x32_bf16 v[60:63], v[232:235], v[32:35], v[60:63]\n"
"ds_read_b128 v[28:31], v113 offset:53248\n"
"buffer_load_dwordx4 v113, s[72:75], s92 offen offset:1024 sc1 lds\n"
"v_mfma_f32_16x16x32_bf16 v[64:67], v[236:239], v[32:35], v[64:67]\n"
"s_waitcnt lgkmcnt(5)\n"
"buffer_load_dwordx4 v[4:7], v113, s[72:75], s46 offen sc1\n"
"v_mfma_f32_16x16x32_bf16 v[60:63], v[240:243], v[36:39], v[60:63]\n"
"ds_read_b128 v[32:35], v113 offset:54272\n"
"v_mfma_f32_16x16x32_bf16 v[64:67], v[244:247], v[36:39], v[64:67]\n"
"buffer_load_dwordx4 v[8:11], v113, s[72:75], s95 offen sc1\n"
"v_mfma_f32_16x16x32_bf16 v[60:63], v[248:251], v[40:43], v[60:63]\n"
"ds_read_b128 v[36:39], v113 offset:55296\n"
"buffer_load_dwordx4 v113, s[72:75], s92 offen offset:2048 sc1 lds\n"
"v_mfma_f32_16x16x32_bf16 v[64:67], v[252:255], v[40:43], v[64:67]\n"
"s_waitcnt lgkmcnt(5)\n"
"buffer_load_dwordx4 v[12:15], v113, s[72:75], s61 offen sc1\n"
"v_mfma_f32_16x16x32_bf16 v[60:63], a[0:3], v[44:47], v[60:63]\n"
"ds_read_b128 v[40:43], v113 offset:56320\n"
"v_mfma_f32_16x16x32_bf16 v[64:67], a[4:7], v[44:47], v[64:67]\n"
"v_mfma_f32_16x16x32_bf16 v[60:63], a[8:11], v[48:51], v[60:63]\n"
"ds_read_b128 v[44:47], v113 offset:57344\n"
"buffer_load_dwordx4 v113, s[72:75], s92 offen offset:3072 sc1 lds\n"
"v_mfma_f32_16x16x32_bf16 v[64:67], a[12:15], v[48:51], v[64:67]\n"
"s_waitcnt lgkmcnt(5)\n"
"v_mfma_f32_16x16x32_bf16 v[60:63], a[16:19], v[52:55], v[60:63]\n"
"ds_read_b128 v[48:51], v113 offset:58368\n"
"v_mfma_f32_16x16x32_bf16 v[64:67], a[20:23], v[52:55], v[64:67]\n"
"v_mfma_f32_16x16x32_bf16 v[60:63], a[24:27], v[56:59], v[60:63]\n"
"ds_read_b128 v[52:55], v113 offset:59392\n"
"v_mfma_f32_16x16x32_bf16 v[64:67], a[28:31], v[56:59], v[64:67]\n"
"s_waitcnt lgkmcnt(5)\n"
"v_mfma_f32_16x16x32_bf16 v[60:63], a[32:35], v[28:31], v[60:63]\n"
"s_waitcnt vmcnt(12)\n"
"ds_read_b128 v[56:59], v113 offset:60416\n"
"v_mfma_f32_16x16x32_bf16 v[64:67], a[36:39], v[28:31], v[64:67]\n"
"s_barrier\n"
"v_mfma_f32_16x16x32_bf16 v[60:63], a[40:43], v[32:35], v[60:63]\n"
"s_add_u32 s92, s54, s45\n"
"ds_read_b128 v[28:31], v118 offset:0\n"
"v_mfma_f32_16x16x32_bf16 v[64:67], a[44:47], v[32:35], v[64:67]\n"
"s_waitcnt lgkmcnt(5)\n"
"v_mfma_f32_16x16x32_bf16 v[60:63], a[48:51], v[36:39], v[60:63]\n"
"s_add_u32 m0, s45, 16384\n"
"ds_read_b128 v[32:35], v118 offset:1024\n"
"v_mfma_f32_16x16x32_bf16 v[64:67], a[52:55], v[36:39], v[64:67]\n"
"v_mfma_f32_16x16x32_bf16 v[60:63], a[56:59], v[40:43], v[60:63]\n"
"ds_read_b128 v[36:39], v118 offset:2048\n"
"buffer_load_dwordx4 v113, s[72:75], s92 offen offset:0 sc1 lds\n"
"v_mfma_f32_16x16x32_bf16 v[64:67], a[60:63], v[40:43], v[64:67]\n"
"s_waitcnt lgkmcnt(5)\n"
"v_mfma_f32_16x16x32_bf16 v[60:63], a[64:67], v[44:47], v[60:63]\n"
"ds_read_b128 v[40:43], v118 offset:3072\n"
"v_mfma_f32_16x16x32_bf16 v[64:67], a[68:71], v[44:47], v[64:67]\n"
"v_mfma_f32_16x16x32_bf16 v[60:63], a[72:75], v[48:51], v[60:63]\n"
"ds_read_b128 v[44:47], v118 offset:4096\n"
"v_mfma_f32_16x16x32_bf16 v[64:67], a[76:79], v[48:51], v[64:67]\n"
"s_waitcnt lgkmcnt(5)\n"
"v_mfma_f32_16x16x32_bf16 v[60:63], a[80:83], v[52:55], v[60:63]\n"
"ds_read_b128 v[48:51], v118 offset:5120\n"
"buffer_load_dwordx4 v113, s[72:75], s92 offen offset:1024 sc1 lds\n"
"v_mfma_f32_16x16x32_bf16 v[64:67], a[84:87], v[52:55], v[64:67]\n"
"v_mfma_f32_16x16x32_bf16 v[60:63], a[88:91], v[56:59], v[60:63]\n"
"ds_read_b128 v[52:55], v118 offset:6144\n"
"v_mfma_f32_16x16x32_bf16 v[64:67], a[92:95], v[56:59], v[64:67]\n"
"s_waitcnt lgkmcnt(5)\n"
"v_mfma_f32_16x16x32_bf16 v[68:71], v[128:131], v[28:31], v[96:99]\n"
"ds_read_b128 v[56:59], v118 offset:7168\n"
"v_mfma_f32_16x16x32_bf16 v[72:75], v[132:135], v[28:31], v[100:103]\n"
"v_mfma_f32_16x16x32_bf16 v[68:71], v[136:139], v[32:35], v[68:71]\n"
"ds_read_b128 v[28:31], v118 offset:8192\n"
"buffer_load_dwordx4 v113, s[72:75], s92 offen offset:2048 sc1 lds\n"
"v_mfma_f32_16x16x32_bf16 v[72:75], v[140:143], v[32:35], v[72:75]\n"
"s_waitcnt lgkmcnt(5)\n"
"v_mfma_f32_16x16x32_bf16 v[68:71], v[144:147], v[36:39], v[68:71]\n"
"s_add_u32 s54, s54, 3670016\n"
"ds_read_b128 v[32:35], v118 offset:9216\n"
"v_cvt_pk_bf16_f32 v24, v60, v61\n"
"v_mfma_f32_16x16x32_bf16 v[72:75], v[148:151], v[36:39], v[72:75]\n"
"v_cvt_pk_bf16_f32 v25, v62, v63\n"
"v_cvt_pk_bf16_f32 v26, v64, v65\n"
"v_mfma_f32_16x16x32_bf16 v[68:71], v[152:155], v[40:43], v[68:71]\n"
"ds_read_b128 v[36:39], v118 offset:10240\n"
"v_cvt_pk_bf16_f32 v27, v66, v67\n"
"v_mfma_f32_16x16x32_bf16 v[72:75], v[156:159], v[40:43], v[72:75]\n"
"s_waitcnt lgkmcnt(5)\n"
"v_and_or_b32 v24, v24, v23, s37\n"
"v_and_or_b32 v25, v25, v23, s37\n"
"v_mfma_f32_16x16x32_bf16 v[68:71], v[160:163], v[44:47], v[68:71]\n"
"ds_read_b128 v[40:43], v118 offset:11264\n"
"v_and_or_b32 v26, v26, v23, s37\n"
"v_mfma_f32_16x16x32_bf16 v[72:75], v[164:167], v[44:47], v[72:75]\n"
"v_and_or_b32 v27, v27, v23, s37\n"
"v_mfma_f32_16x16x32_bf16 v[68:71], v[168:171], v[48:51], v[68:71]\n"
"buffer_store_dwordx4 v[24:27], v113, s[72:75], s57 offen sc1\n"
"ds_read_b128 v[44:47], v118 offset:12288\n"
"v_mfma_f32_16x16x32_bf16 v[72:75], v[172:175], v[48:51], v[72:75]\n"
"s_waitcnt lgkmcnt(5)\n"
"v_mfma_f32_16x16x32_bf16 v[68:71], v[176:179], v[52:55], v[68:71]\n"
"s_add_u32 s57, s57, 3145728\n"
"ds_read_b128 v[48:51], v118 offset:13312\n"
"v_mfma_f32_16x16x32_bf16 v[72:75], v[180:183], v[52:55], v[72:75]\n"
"v_mfma_f32_16x16x32_bf16 v[68:71], v[184:187], v[56:59], v[68:71]\n"
"ds_read_b128 v[52:55], v118 offset:14336\n"
"v_mfma_f32_16x16x32_bf16 v[72:75], v[188:191], v[56:59], v[72:75]\n"
"s_waitcnt lgkmcnt(5)\n"
"v_mfma_f32_16x16x32_bf16 v[68:71], v[192:195], v[28:31], v[68:71]\n"
"s_waitcnt vmcnt(13)\n"
"ds_read_b128 v[56:59], v118 offset:15360\n"
"v_mfma_f32_16x16x32_bf16 v[72:75], v[196:199], v[28:31], v[72:75]\n"
"s_barrier\n"
"v_mfma_f32_16x16x32_bf16 v[68:71], v[200:203], v[32:35], v[68:71]\n"
"s_add_u32 s92, s54, s44\n"
"ds_read_b128 v[28:31], v113 offset:0\n"
"v_mfma_f32_16x16x32_bf16 v[72:75], v[204:207], v[32:35], v[72:75]\n"
"s_waitcnt lgkmcnt(5)\n"
"v_mfma_f32_16x16x32_bf16 v[68:71], v[208:211], v[36:39], v[68:71]\n"
"s_add_u32 m0, s44, 49152\n"
"ds_read_b128 v[32:35], v113 offset:1024\n"
"v_mfma_f32_16x16x32_bf16 v[72:75], v[212:215], v[36:39], v[72:75]\n"
"v_mfma_f32_16x16x32_bf16 v[68:71], v[216:219], v[40:43], v[68:71]\n"
"ds_read_b128 v[36:39], v113 offset:2048\n"
"buffer_load_dwordx4 v113, s[72:75], s92 offen offset:0 sc1 lds\n"
"v_mfma_f32_16x16x32_bf16 v[72:75], v[220:223], v[40:43], v[72:75]\n"
"s_waitcnt lgkmcnt(5)\n"
"v_mfma_f32_16x16x32_bf16 v[68:71], v[224:227], v[44:47], v[68:71]\n"
"ds_read_b128 v[40:43], v113 offset:3072\n"
"v_mfma_f32_16x16x32_bf16 v[72:75], v[228:231], v[44:47], v[72:75]\n"
"v_mfma_f32_16x16x32_bf16 v[68:71], v[232:235], v[48:51], v[68:71]\n"
"ds_read_b128 v[44:47], v113 offset:4096\n"
"buffer_load_dwordx4 v113, s[72:75], s92 offen offset:1024 sc1 lds\n"
"v_mfma_f32_16x16x32_bf16 v[72:75], v[236:239], v[48:51], v[72:75]\n"
"s_waitcnt lgkmcnt(5)\n"
"v_mfma_f32_16x16x32_bf16 v[68:71], v[240:243], v[52:55], v[68:71]\n"
"ds_read_b128 v[48:51], v113 offset:5120\n"
"v_mfma_f32_16x16x32_bf16 v[72:75], v[244:247], v[52:55], v[72:75]\n"
"v_mfma_f32_16x16x32_bf16 v[68:71], v[248:251], v[56:59], v[68:71]\n"
"ds_read_b128 v[52:55], v113 offset:6144\n"
"buffer_load_dwordx4 v113, s[72:75], s92 offen offset:2048 sc1 lds\n"
"v_mfma_f32_16x16x32_bf16 v[72:75], v[252:255], v[56:59], v[72:75]\n"
"s_waitcnt lgkmcnt(5)\n"
"v_mfma_f32_16x16x32_bf16 v[68:71], a[0:3], v[28:31], v[68:71]\n"
"ds_read_b128 v[56:59], v113 offset:7168\n"
"v_mfma_f32_16x16x32_bf16 v[72:75], a[4:7], v[28:31], v[72:75]\n"
"v_mfma_f32_16x16x32_bf16 v[68:71], a[8:11], v[32:35], v[68:71]\n"
"ds_read_b128 v[28:31], v113 offset:8192\n"
"buffer_load_dwordx4 v113, s[72:75], s92 offen offset:3072 sc1 lds\n"
"v_mfma_f32_16x16x32_bf16 v[72:75], a[12:15], v[32:35], v[72:75]\n"
"s_waitcnt lgkmcnt(5)\n"
"v_mfma_f32_16x16x32_bf16 v[68:71], a[16:19], v[36:39], v[68:71]\n"
"ds_read_b128 v[32:35], v113 offset:9216\n"
"v_mfma_f32_16x16x32_bf16 v[72:75], a[20:23], v[36:39], v[72:75]\n"
"v_mfma_f32_16x16x32_bf16 v[68:71], a[24:27], v[40:43], v[68:71]\n"
"ds_read_b128 v[36:39], v113 offset:10240\n"
"v_mfma_f32_16x16x32_bf16 v[72:75], a[28:31], v[40:43], v[72:75]\n"
"s_waitcnt lgkmcnt(5)\n"
"v_mfma_f32_16x16x32_bf16 v[68:71], a[32:35], v[44:47], v[68:71]\n"
"s_waitcnt vmcnt(8)\n"
"ds_read_b128 v[40:43], v113 offset:11264\n"
"v_mfma_f32_16x16x32_bf16 v[72:75], a[36:39], v[44:47], v[72:75]\n"
"s_barrier\n"
"v_mfma_f32_16x16x32_bf16 v[68:71], a[40:43], v[48:51], v[68:71]\n"
"s_add_u32 s92, s54, s45\n"
"ds_read_b128 v[44:47], v113 offset:16384\n"
"v_mfma_f32_16x16x32_bf16 v[72:75], a[44:47], v[48:51], v[72:75]\n"
"s_waitcnt lgkmcnt(5)\n"
"v_mfma_f32_16x16x32_bf16 v[68:71], a[48:51], v[52:55], v[68:71]\n"
"s_add_u32 m0, s45, 49152\n"
"ds_read_b128 v[48:51], v113 offset:17408\n"
"v_mfma_f32_16x16x32_bf16 v[72:75], a[52:55], v[52:55], v[72:75]\n"
"v_mfma_f32_16x16x32_bf16 v[68:71], a[56:59], v[56:59], v[68:71]\n"
"ds_read_b128 v[52:55], v113 offset:18432\n"
"buffer_load_dwordx4 v113, s[72:75], s92 offen offset:0 sc1 lds\n"
"v_mfma_f32_16x16x32_bf16 v[72:75], a[60:63], v[56:59], v[72:75]\n"
"s_waitcnt lgkmcnt(5)\n"
"v_mfma_f32_16x16x32_bf16 v[68:71], a[64:67], v[28:31], v[68:71]\n"
"ds_read_b128 v[56:59], v113 offset:19456\n"
"v_mfma_f32_16x16x32_bf16 v[72:75], a[68:71], v[28:31], v[72:75]\n"
"v_mfma_f32_16x16x32_bf16 v[68:71], a[72:75], v[32:35], v[68:71]\n"
"ds_read_b128 v[28:31], v113 offset:20480\n"
"v_mfma_f32_16x16x32_bf16 v[72:75], a[76:79], v[32:35], v[72:75]\n"
"s_waitcnt lgkmcnt(5)\n"
"v_mfma_f32_16x16x32_bf16 v[68:71], a[80:83], v[36:39], v[68:71]\n"
"ds_read_b128 v[32:35], v113 offset:21504\n"
"buffer_load_dwordx4 v113, s[72:75], s92 offen offset:1024 sc1 lds\n"
"v_mfma_f32_16x16x32_bf16 v[72:75], a[84:87], v[36:39], v[72:75]\n"
"v_mfma_f32_16x16x32_bf16 v[68:71], a[88:91], v[40:43], v[68:71]\n"
"ds_read_b128 v[36:39], v113 offset:22528\n"
"v_mfma_f32_16x16x32_bf16 v[72:75], a[92:95], v[40:43], v[72:75]\n"
"s_waitcnt lgkmcnt(5)\n"
"v_mfma_f32_16x16x32_bf16 v[60:63], v[128:131], v[44:47], v[96:99]\n"
"ds_read_b128 v[40:43], v113 offset:23552\n"
"v_xor_b32 v4, s37, v4\n"
"v_mfma_f32_16x16x32_bf16 v[64:67], v[132:135], v[44:47], v[100:103]\n"
"v_xor_b32 v5, s37, v5\n"
"v_xor_b32 v6, s37, v6\n"
"v_mfma_f32_16x16x32_bf16 v[60:63], v[136:139], v[48:51], v[60:63]\n"
"ds_read_b128 v[44:47], v113 offset:24576\n"
"buffer_load_dwordx4 v113, s[72:75], s92 offen offset:2048 sc1 lds\n"
"v_mfma_f32_16x16x32_bf16 v[64:67], v[140:143], v[48:51], v[64:67]\n"
"s_waitcnt lgkmcnt(5)\n"
"v_xor_b32 v7, s37, v7\n"
"v_xor_b32 v8, s37, v8\n"
"v_mfma_f32_16x16x32_bf16 v[60:63], v[144:147], v[52:55], v[60:63]\n"
"s_add_u32 s54, s54, 3670016\n"
"ds_read_b128 v[48:51], v113 offset:25600\n"
"v_fmac_f32 v76, v104, v68\n"
"v_mfma_f32_16x16x32_bf16 v[64:67], v[148:151], v[52:55], v[64:67]\n"
"v_fmac_f32 v77, v105, v69\n"
"v_fmac_f32 v78, v106, v70\n"
"v_mfma_f32_16x16x32_bf16 v[60:63], v[152:155], v[56:59], v[60:63]\n"
"ds_read_b128 v[52:55], v113 offset:26624\n"
"v_fmac_f32 v79, v107, v71\n"
"v_mfma_f32_16x16x32_bf16 v[64:67], v[156:159], v[56:59], v[64:67]\n"
"s_waitcnt lgkmcnt(5)\n"
"v_fmac_f32 v80, v108, v72\n"
"v_fmac_f32 v81, v109, v73\n"
"v_mfma_f32_16x16x32_bf16 v[60:63], v[160:163], v[28:31], v[60:63]\n"
"ds_read_b128 v[56:59], v113 offset:27648\n"
"v_fmac_f32 v82, v110, v74\n"
"v_mfma_f32_16x16x32_bf16 v[64:67], v[164:167], v[28:31], v[64:67]\n"
"v_fmac_f32 v83, v111, v75\n"
"v_cvt_pk_bf16_f32 v84, v76, v77\n"
"v_mfma_f32_16x16x32_bf16 v[60:63], v[168:171], v[32:35], v[60:63]\n"
"ds_read_b128 v[28:31], v113 offset:28672\n"
"v_cvt_pk_bf16_f32 v85, v78, v79\n"
"v_mfma_f32_16x16x32_bf16 v[64:67], v[172:175], v[32:35], v[64:67]\n"
"s_waitcnt lgkmcnt(5)\n"
"v_cvt_pk_bf16_f32 v86, v80, v81\n"
"v_cvt_pk_bf16_f32 v87, v82, v83\n"
"v_mfma_f32_16x16x32_bf16 v[60:63], v[176:179], v[36:39], v[60:63]\n"
"buffer_store_dwordx4 v[84:87], v127, s[84:87], s48 offen\n"
"ds_read_b128 v[32:35], v113 offset:29696\n"
"v_mfma_f32_16x16x32_bf16 v[64:67], v[180:183], v[36:39], v[64:67]\n"
"s_add_u32 s48, s48, 131072\n"
"v_xor_b32 v9, s37, v9\n"
"v_xor_b32 v10, s37, v10\n"
"v_mfma_f32_16x16x32_bf16 v[60:63], v[184:187], v[40:43], v[60:63]\n"
"ds_read_b128 v[36:39], v113 offset:30720\n"
"v_xor_b32 v11, s37, v11\n"
"v_mfma_f32_16x16x32_bf16 v[64:67], v[188:191], v[40:43], v[64:67]\n"
"s_waitcnt lgkmcnt(5)\n"
"v_xor_b32 v12, s37, v12\n"
"v_xor_b32 v13, s37, v13\n"
"v_mfma_f32_16x16x32_bf16 v[60:63], v[192:195], v[44:47], v[60:63]\n"
"s_waitcnt vmcnt(9)\n"
"ds_read_b128 v[40:43], v113 offset:31744\n"
"v_xor_b32 v14, s37, v14\n"
"v_mfma_f32_16x16x32_bf16 v[64:67], v[196:199], v[44:47], v[64:67]\n"
"s_barrier\n"
"v_xor_b32 v15, s37, v15\n"
"v_or3_b32 v3, v4, v5, v6\n"
"v_mfma_f32_16x16x32_bf16 v[60:63], v[200:203], v[48:51], v[60:63]\n"
"s_add_u32 s92, s54, s44\n"
"ds_read_b128 v[44:47], v113 offset:32768\n"
"v_or3_b32 v3, v3, v7, v8\n"
"v_mfma_f32_16x16x32_bf16 v[64:67], v[204:207], v[48:51], v[64:67]\n"
"s_waitcnt lgkmcnt(5)\n"
"v_or3_b32 v3, v3, v9, v10\n"
"v_or3_b32 v3, v3, v11, v12\n"
"v_mfma_f32_16x16x32_bf16 v[60:63], v[208:211], v[52:55], v[60:63]\n"
"s_add_u32 m0, s44, 0\n"
"ds_read_b128 v[48:51], v113 offset:33792\n"
"v_or3_b32 v3, v3, v13, v14\n"
"v_mfma_f32_16x16x32_bf16 v[64:67], v[212:215], v[52:55], v[64:67]\n"
"v_bitop3_b32 v3, v3, v15, v23 bitop3:0x54\n"
"v_cmp_ne_u32 vcc, 0, v3\n"
"v_mfma_f32_16x16x32_bf16 v[60:63], v[216:219], v[56:59], v[60:63]\n"
"ds_read_b128 v[52:55], v113 offset:34816\n"
"buffer_load_dwordx4 v113, s[72:75], s92 offen offset:0 sc1 lds\n"
"v_mfma_f32_16x16x32_bf16 v[64:67], v[220:223], v[56:59], v[64:67]\n"
"s_waitcnt lgkmcnt(5)\n"
"v_lshlrev_b32 v92, 16, v88\n"
"v_and_b32 v93, 0xffff0000, v88\n"
"v_mfma_f32_16x16x32_bf16 v[60:63], v[224:227], v[28:31], v[60:63]\n"
"s_cbranch_vccnz Lslow_p2_K\n"
"Lret_p2_K:\n"
"ds_read_b128 v[56:59], v113 offset:35840\n"
"v_lshlrev_b32 v94, 16, v89\n"
"v_mfma_f32_16x16x32_bf16 v[64:67], v[228:231], v[28:31], v[64:67]\n"
"s_add_u32 s46, s46, 3145728\n"
"v_and_b32 v95, 0xffff0000, v89\n"
"v_lshlrev_b32 v112, 16, v90\n"
"v_mfma_f32_16x16x32_bf16 v[60:63], v[232:235], v[32:35], v[60:63]\n"
"ds_read_b128 v[28:31], v113 offset:36864\n"
"buffer_load_dwordx4 v113, s[72:75], s92 offen offset:1024 sc1 lds\n"
"v_mfma_f32_16x16x32_bf16 v[64:67], v[236:239], v[32:35], v[64:67]\n"
"s_waitcnt lgkmcnt(5)\n"
"v_and_b32 v18, 0xffff0000, v90\n"
"v_lshlrev_b32 v120, 16, v91\n"
"v_mfma_f32_16x16x32_bf16 v[60:63], v[240:243], v[36:39], v[60:63]\n"
"ds_read_b128 v[32:35], v113 offset:37888\n"
"v_and_b32 v121, 0xffff0000, v91\n"
"v_mfma_f32_16x16x32_bf16 v[64:67], v[244:247], v[36:39], v[64:67]\n"
"v_lshl_or_b32 v3, v4, 16, v16\n"
"v_and_or_b32 v4, v4, v17, v16\n"
"v_mfma_f32_16x16x32_bf16 v[60:63], v[248:251], v[40:43], v[60:63]\n"
"ds_read_b128 v[36:39], v113 offset:38912\n"
"buffer_load_dwordx4 v113, s[72:75], s92 offen offset:2048 sc1 lds\n"
"v_mfma_f32_16x16x32_bf16 v[64:67], v[252:255], v[40:43], v[64:67]\n"
"s_waitcnt lgkmcnt(5)\n"
"v_fmac_f32 v92, v104, v3\n"
"v_fmac_f32 v93, v105, v4\n"
"v_mfma_f32_16x16x32_bf16 v[60:63], a[0:3], v[44:47], v[60:63]\n"
"ds_read_b128 v[40:43], v113 offset:39936\n"
"v_lshl_or_b32 v3, v5, 16, v16\n"
"v_mfma_f32_16x16x32_bf16 v[64:67], a[4:7], v[44:47], v[64:67]\n"
"v_and_or_b32 v5, v5, v17, v16\n"
"v_fmac_f32 v94, v106, v3\n"
"v_mfma_f32_16x16x32_bf16 v[60:63], a[8:11], v[48:51], v[60:63]\n"
"ds_read_b128 v[44:47], v113 offset:40960\n"
"buffer_load_dwordx4 v113, s[72:75], s92 offen offset:3072 sc1 lds\n"
"v_mfma_f32_16x16x32_bf16 v[64:67], a[12:15], v[48:51], v[64:67]\n"
"s_waitcnt lgkmcnt(5)\n"
"v_fmac_f32 v95, v107, v5\n"
"v_lshl_or_b32 v3, v6, 16, v16\n"
"v_mfma_f32_16x16x32_bf16 v[60:63], a[16:19], v[52:55], v[60:63]\n"
"ds_read_b128 v[48:51], v113 offset:41984\n"
"v_and_or_b32 v6, v6, v17, v16\n"
"v_mfma_f32_16x16x32_bf16 v[64:67], a[20:23], v[52:55], v[64:67]\n"
"v_fmac_f32 v112, v108, v3\n"
"v_fmac_f32 v18, v109, v6\n"
"v_mfma_f32_16x16x32_bf16 v[60:63], a[24:27], v[56:59], v[60:63]\n"
"ds_read_b128 v[52:55], v113 offset:43008\n"
"v_lshl_or_b32 v3, v7, 16, v16\n"
"v_mfma_f32_16x16x32_bf16 v[64:67], a[28:31], v[56:59], v[64:67]\n"
"s_waitcnt lgkmcnt(5)\n"
"v_and_or_b32 v7, v7, v17, v16\n"
"v_fmac_f32 v120, v110, v3\n"
"v_mfma_f32_16x16x32_bf16 v[60:63], a[32:35], v[28:31], v[60:63]\n"
"s_waitcnt vmcnt(8)\n"
"ds_read_b128 v[56:59], v113 offset:44032\n"
"v_fmac_f32 v121, v111, v7\n"
"v_mfma_f32_16x16x32_bf16 v[64:67], a[36:39], v[28:31], v[64:67]\n"
"s_barrier\n"
"v_lshl_or_b32 v3, v8, 16, v16\n"
"v_and_or_b32 v8, v8, v17, v16\n"
"v_mfma_f32_16x16x32_bf16 v[60:63], a[40:43], v[32:35], v[60:63]\n"
"s_add_u32 s92, s54, s45\n"
"ds_read_b128 v[28:31], v113 offset:49152\n"
"v_fmac_f32 v92, v104, v3\n"
"v_mfma_f32_16x16x32_bf16 v[64:67], a[44:47], v[32:35], v[64:67]\n"
"s_waitcnt lgkmcnt(5)\n"
"v_fmac_f32 v93, v105, v8\n"
"v_lshl_or_b32 v3, v9, 16, v16\n"
"v_mfma_f32_16x16x32_bf16 v[60:63], a[48:51], v[36:39], v[60:63]\n"
"s_add_u32 m0, s45, 0\n"
"ds_read_b128 v[32:35], v113 offset:50176\n"
"v_and_or_b32 v9, v9, v17, v16\n"
"v_mfma_f32_16x16x32_bf16 v[64:67], a[52:55], v[36:39], v[64:67]\n"
"v_fmac_f32 v94, v106, v3\n"
"v_fmac_f32 v95, v107, v9\n"
"v_mfma_f32_16x16x32_bf16 v[60:63], a[56:59], v[40:43], v[60:63]\n"
"ds_read_b128 v[36:39], v113 offset:51200\n"
"buffer_load_dwordx4 v113, s[72:75], s92 offen offset:0 sc1 lds\n"
"v_mfma_f32_16x16x32_bf16 v[64:67], a[60:63], v[40:43], v[64:67]\n"
"s_waitcnt lgkmcnt(5)\n"
"v_lshl_or_b32 v3, v10, 16, v16\n"
"v_and_or_b32 v10, v10, v17, v16\n"
"v_mfma_f32_16x16x32_bf16 v[60:63], a[64:67], v[44:47], v[60:63]\n"
"ds_read_b128 v[40:43], v113 offset:52224\n"
"v_fmac_f32 v112, v108, v3\n"
"v_mfma_f32_16x16x32_bf16 v[64:67], a[68:71], v[44:47], v[64:67]\n"
"v_fmac_f32 v18, v109, v10\n"
"v_lshl_or_b32 v3, v11, 16, v16\n"
"v_mfma_f32_16x16x32_bf16 v[60:63], a[72:75], v[48:51], v[60:63]\n"
"ds_read_b128 v[44:47], v113 offset:53248\n"
"v_and_or_b32 v11, v11, v17, v16\n"
"v_mfma_f32_16x16x32_bf16 v[64:67], a[76:79], v[48:51], v[64:67]\n"
"s_waitcnt lgkmcnt(5)\n"
"v_fmac_f32 v120, v110, v3\n"
"v_fmac_f32 v121, v111, v11\n"
"v_mfma_f32_16x16x32_bf16 v[60:63], a[80:83], v[52:55], v[60:63]\n"
"ds_read_b128 v[48:51], v113 offset:54272\n"
"buffer_load_dwordx4 v113, s[72:75], s92 offen offset:1024 sc1 lds\n"
"v_mfma_f32_16x16x32_bf16 v[64:67], a[84:87], v[52:55], v[64:67]\n"
"v_lshl_or_b32 v3, v12, 16, v16\n"
"v_and_or_b32 v12, v12, v17, v16\n"
"v_mfma_f32_16x16x32_bf16 v[60:63], a[88:91], v[56:59], v[60:63]\n"
"ds_read_b128 v[52:55], v113 offset:55296\n"
"v_fmac_f32 v92, v104, v3\n"
"v_mfma_f32_16x16x32_bf16 v[64:67], a[92:95], v[56:59], v[64:67]\n"
"s_waitcnt lgkmcnt(5)\n"
"v_fmac_f32 v93, v105, v12\n"
"v_lshl_or_b32 v3, v13, 16, v16\n"
"v_mfma_f32_16x16x32_bf16 v[68:71], v[128:131], v[28:31], v[96:99]\n"
"ds_read_b128 v[56:59], v113 offset:56320\n"
"v_and_or_b32 v13, v13, v17, v16\n"
"v_mfma_f32_16x16x32_bf16 v[72:75], v[132:135], v[28:31], v[100:103]\n"
"v_fmac_f32 v94, v106, v3\n"
"v_fmac_f32 v95, v107, v13\n"
"v_mfma_f32_16x16x32_bf16 v[68:71], v[136:139], v[32:35], v[68:71]\n"
"ds_read_b128 v[28:31], v113 offset:57344\n"
"buffer_load_dwordx4 v113, s[72:75], s92 offen offset:2048 sc1 lds\n"
"v_mfma_f32_16x16x32_bf16 v[72:75], v[140:143], v[32:35], v[72:75]\n"
"s_waitcnt lgkmcnt(5)\n"
"v_lshl_or_b32 v3, v14, 16, v16\n"
"v_and_or_b32 v14, v14, v17, v16\n"
"v_mfma_f32_16x16x32_bf16 v[68:71], v[144:147], v[36:39], v[68:71]\n"
"s_add_u32 s54, s54, 3670016\n"
"ds_read_b128 v[32:35], v113 offset:58368\n"
"v_fmac_f32 v114, v104, v60\n"
"v_mfma_f32_16x16x32_bf16 v[72:75], v[148:151], v[36:39], v[72:75]\n"
"v_fmac_f32 v115, v105, v61\n"
"v_fmac_f32 v116, v106, v62\n"
"v_mfma_f32_16x16x32_bf16 v[68:71], v[152:155], v[40:43], v[68:71]\n"
"ds_read_b128 v[36:39], v113 offset:59392\n"
"v_fmac_f32 v117, v107, v63\n"
"v_mfma_f32_16x16x32_bf16 v[72:75], v[156:159], v[40:43], v[72:75]\n"
"s_waitcnt lgkmcnt(5)\n"
"v_fmac_f32 v122, v108, v64\n"
"v_fmac_f32 v123, v109, v65\n"
"v_mfma_f32_16x16x32_bf16 v[68:71], v[160:163], v[44:47], v[68:71]\n"
"ds_read_b128 v[40:43], v113 offset:60416\n"
"v_fmac_f32 v124, v110, v66\n"
"v_mfma_f32_16x16x32_bf16 v[72:75], v[164:167], v[44:47], v[72:75]\n"
"v_fmac_f32 v125, v111, v67\n"
"v_cvt_pk_bf16_f32 v84, v114, v115\n"
"v_mfma_f32_16x16x32_bf16 v[68:71], v[168:171], v[48:51], v[68:71]\n"
"ds_read_b128 v[44:47], v113 offset:61440\n"
"v_cvt_pk_bf16_f32 v85, v116, v117\n"
"v_mfma_f32_16x16x32_bf16 v[72:75], v[172:175], v[48:51], v[72:75]\n"
"s_waitcnt lgkmcnt(5)\n"
"v_cvt_pk_bf16_f32 v86, v122, v123\n"
"v_cvt_pk_bf16_f32 v87, v124, v125\n"
"v_mfma_f32_16x16x32_bf16 v[68:71], v[176:179], v[52:55], v[68:71]\n"
"buffer_store_dwordx4 v[84:87], v127, s[84:87], s48 offen\n"
"ds_read_b128 v[48:51], v113 offset:62464\n"
"v_mfma_f32_16x16x32_bf16 v[72:75], v[180:183], v[52:55], v[72:75]\n"
"s_add_u32 s48, s48, 131072\n"
"v_fmac_f32 v112, v108, v3\n"
"v_fmac_f32 v18, v109, v14\n"
"v_mfma_f32_16x16x32_bf16 v[68:71], v[184:187], v[56:59], v[68:71]\n"
"ds_read_b128 v[52:55], v113 offset:63488\n"
"v_lshl_or_b32 v3, v15, 16, v16\n"
"v_mfma_f32_16x16x32_bf16 v[72:75], v[188:191], v[56:59], v[72:75]\n"
"s_waitcnt lgkmcnt(5)\n"
"v_and_or_b32 v15, v15, v17, v16\n"
"v_fmac_f32 v120, v110, v3\n"
"v_mfma_f32_16x16x32_bf16 v[68:71], v[192:195], v[28:31], v[68:71]\n"
"s_waitcnt vmcnt(9)\n"
"ds_read_b128 v[56:59], v113 offset:64512\n"
"v_fmac_f32 v121, v111, v15\n"
"v_mfma_f32_16x16x32_bf16 v[72:75], v[196:199], v[28:31], v[72:75]\n"
"s_barrier\n"
"v_mfma_f32_16x16x32_bf16 v[68:71], v[200:203], v[32:35], v[68:71]\n"
"ds_read_b128 v[28:31], v118 offset:0\n"
"buffer_load_dwordx4 v[88:91], v126, s[72:75], s47 offen sc1\n"
"v_mfma_f32_16x16x32_bf16 v[72:75], v[204:207], v[32:35], v[72:75]\n"
"s_waitcnt lgkmcnt(5)\n"
"v_mfma_f32_16x16x32_bf16 v[68:71], v[208:211], v[36:39], v[68:71]\n"
"s_add_u32 s47, s47, 1048576\n"
"ds_read_b128 v[32:35], v118 offset:1024\n"
"v_mfma_f32_16x16x32_bf16 v[72:75], v[212:215], v[36:39], v[72:75]\n"
"s_add_u32 s95, s46, 1048576\n"
"v_mfma_f32_16x16x32_bf16 v[68:71], v[216:219], v[40:43], v[68:71]\n"
"s_add_u32 s61, s46, 2097152\n"
"ds_read_b128 v[36:39], v118 offset:2048\n"
"v_mfma_f32_16x16x32_bf16 v[72:75], v[220:223], v[40:43], v[72:75]\n"
"s_waitcnt lgkmcnt(5)\n"
"v_mfma_f32_16x16x32_bf16 v[68:71], v[224:227], v[44:47], v[68:71]\n"
"ds_read_b128 v[40:43], v118 offset:3072\n"
"buffer_load_dwordx4 v[4:7], v113, s[72:75], s46 offen sc1\n"
"v_mfma_f32_16x16x32_bf16 v[72:75], v[228:231], v[44:47], v[72:75]\n"
"v_mfma_f32_16x16x32_bf16 v[68:71], v[232:235], v[48:51], v[68:71]\n"
"ds_read_b128 v[44:47], v118 offset:4096\n"
"buffer_load_dwordx4 v[8:11], v113, s[72:75], s95 offen sc1\n"
"v_mfma_f32_16x16x32_bf16 v[72:75], v[236:239], v[48:51], v[72:75]\n"
"s_waitcnt lgkmcnt(5)\n"
"v_mfma_f32_16x16x32_bf16 v[68:71], v[240:243], v[52:55], v[68:71]\n"
"ds_read_b128 v[48:51], v118 offset:5120\n"
"buffer_load_dwordx4 v[12:15], v113, s[72:75], s61 offen sc1\n"
"v_mfma_f32_16x16x32_bf16 v[72:75], v[244:247], v[52:55], v[72:75]\n"
"v_mfma_f32_16x16x32_bf16 v[68:71], v[248:251], v[56:59], v[68:71]\n"
"ds_read_b128 v[52:55], v118 offset:6144\n"
"v_mfma_f32_16x16x32_bf16 v[72:75], v[252:255], v[56:59], v[72:75]\n"
"s_waitcnt lgkmcnt(5)\n"
"v_mfma_f32_16x16x32_bf16 v[68:71], a[0:3], v[28:31], v[68:71]\n"
"ds_read_b128 v[56:59], v118 offset:7168\n"
"v_mfma_f32_16x16x32_bf16 v[72:75], a[4:7], v[28:31], v[72:75]\n"
"v_mfma_f32_16x16x32_bf16 v[68:71], a[8:11], v[32:35], v[68:71]\n"
"ds_read_b128 v[28:31], v118 offset:8192\n"
"v_mfma_f32_16x16x32_bf16 v[72:75], a[12:15], v[32:35], v[72:75]\n"
"s_waitcnt lgkmcnt(5)\n"
"v_mfma_f32_16x16x32_bf16 v[68:71], a[16:19], v[36:39], v[68:71]\n"
"ds_read_b128 v[32:35], v118 offset:9216\n"
"v_mfma_f32_16x16x32_bf16 v[72:75], a[20:23], v[36:39], v[72:75]\n"
"v_mfma_f32_16x16x32_bf16 v[68:71], a[24:27], v[40:43], v[68:71]\n"
"ds_read_b128 v[36:39], v118 offset:10240\n"
"v_mfma_f32_16x16x32_bf16 v[72:75], a[28:31], v[40:43], v[72:75]\n"
"s_waitcnt lgkmcnt(5)\n"
"v_mfma_f32_16x16x32_bf16 v[68:71], a[32:35], v[44:47], v[68:71]\n"
"s_waitcnt vmcnt(8)\n"
"ds_read_b128 v[40:43], v118 offset:11264\n"
"v_mfma_f32_16x16x32_bf16 v[72:75], a[36:39], v[44:47], v[72:75]\n"
"s_barrier\n"
"v_mfma_f32_16x16x32_bf16 v[68:71], a[40:43], v[48:51], v[68:71]\n"
"ds_read_b128 v[44:47], v113 offset:0\n"
"v_mfma_f32_16x16x32_bf16 v[72:75], a[44:47], v[48:51], v[72:75]\n"
"s_waitcnt lgkmcnt(5)\n"
"v_mfma_f32_16x16x32_bf16 v[68:71], a[48:51], v[52:55], v[68:71]\n"
"ds_read_b128 v[48:51], v113 offset:1024\n"
"v_mfma_f32_16x16x32_bf16 v[72:75], a[52:55], v[52:55], v[72:75]\n"
"v_mfma_f32_16x16x32_bf16 v[68:71], a[56:59], v[56:59], v[68:71]\n"
"ds_read_b128 v[52:55], v113 offset:2048\n"
"v_mfma_f32_16x16x32_bf16 v[72:75], a[60:63], v[56:59], v[72:75]\n"
"s_waitcnt lgkmcnt(5)\n"
"v_mfma_f32_16x16x32_bf16 v[68:71], a[64:67], v[28:31], v[68:71]\n"
"ds_read_b128 v[56:59], v113 offset:3072\n"
"v_mfma_f32_16x16x32_bf16 v[72:75], a[68:71], v[28:31], v[72:75]\n"
"v_mfma_f32_16x16x32_bf16 v[68:71], a[72:75], v[32:35], v[68:71]\n"
"ds_read_b128 v[28:31], v113 offset:4096\n"
"v_mfma_f32_16x16x32_bf16 v[72:75], a[76:79], v[32:35], v[72:75]\n"
"s_waitcnt lgkmcnt(5)\n"
"v_mfma_f32_16x16x32_bf16 v[68:71], a[80:83], v[36:39], v[68:71]\n"
"ds_read_b128 v[32:35], v113 offset:5120\n"
"v_mfma_f32_16x16x32_bf16 v[72:75], a[84:87], v[36:39], v[72:75]\n"
"v_mfma_f32_16x16x32_bf16 v[68:71], a[88:91], v[40:43], v[68:71]\n"
"ds_read_b128 v[36:39], v113 offset:6144\n"
"v_mfma_f32_16x16x32_bf16 v[72:75], a[92:95], v[40:43], v[72:75]\n"
"s_waitcnt lgkmcnt(5)\n"
"v_mfma_f32_16x16x32_bf16 v[60:63], v[128:131], v[44:47], v[96:99]\n"
"ds_read_b128 v[40:43], v113 offset:7168\n"
"v_mfma_f32_16x16x32_bf16 v[64:67], v[132:135], v[44:47], v[100:103]\n"
"v_mfma_f32_16x16x32_bf16 v[60:63], v[136:139], v[48:51], v[60:63]\n"
"ds_read_b128 v[44:47], v113 offset:8192\n"
"v_mfma_f32_16x16x32_bf16 v[64:67], v[140:143], v[48:51], v[64:67]\n"
"s_waitcnt lgkmcnt(5)\n"
"v_mfma_f32_16x16x32_bf16 v[60:63], v[144:147], v[52:55], v[60:63]\n"
"ds_read_b128 v[48:51], v113 offset:9216\n"
"v_fmac_f32 v92, v104, v68\n"
"v_mfma_f32_16x16x32_bf16 v[64:67], v[148:151], v[52:55], v[64:67]\n"
"v_fmac_f32 v93, v105, v69\n"
"v_fmac_f32 v94, v106, v70\n"
"v_mfma_f32_16x16x32_bf16 v[60:63], v[152:155], v[56:59], v[60:63]\n"
"ds_read_b128 v[52:55], v113 offset:10240\n"
"v_fmac_f32 v95, v107, v71\n"
"v_mfma_f32_16x16x32_bf16 v[64:67], v[156:159], v[56:59], v[64:67]\n"
"s_waitcnt lgkmcnt(5)\n"
"v_fmac_f32 v112, v108, v72\n"
"v_fmac_f32 v18, v109, v73\n"
"v_mfma_f32_16x16x32_bf16 v[60:63], v[160:163], v[28:31], v[60:63]\n"
"ds_read_b128 v[56:59], v113 offset:11264\n"
"v_fmac_f32 v120, v110, v74\n"
"v_mfma_f32_16x16x32_bf16 v[64:67], v[164:167], v[28:31], v[64:67]\n"
"v_fmac_f32 v121, v111, v75\n"
"v_cvt_pk_bf16_f32 v84, v92, v93\n"
"v_mfma_f32_16x16x32_bf16 v[60:63], v[168:171], v[32:35], v[60:63]\n"
"ds_read_b128 v[28:31], v113 offset:12288\n"
"v_cvt_pk_bf16_f32 v85, v94, v95\n"
"v_mfma_f32_16x16x32_bf16 v[64:67], v[172:175], v[32:35], v[64:67]\n"
"s_waitcnt lgkmcnt(5)\n"
"v_cvt_pk_bf16_f32 v86, v112, v18\n"
"v_cvt_pk_bf16_f32 v87, v120, v121\n"
"v_mfma_f32_16x16x32_bf16 v[60:63], v[176:179], v[36:39], v[60:63]\n"
"buffer_store_dwordx4 v[84:87], v127, s[84:87], s48 offen\n"
"ds_read_b128 v[32:35], v113 offset:13312\n"
"v_mfma_f32_16x16x32_bf16 v[64:67], v[180:183], v[36:39], v[64:67]\n"
"s_add_u32 s48, s48, 131072\n"
"v_mfma_f32_16x16x32_bf16 v[60:63], v[184:187], v[40:43], v[60:63]\n"
"ds_read_b128 v[36:39], v113 offset:14336\n"
"v_mfma_f32_16x16x32_bf16 v[64:67], v[188:191], v[40:43], v[64:67]\n"
"s_waitcnt lgkmcnt(5)\n"
"v_mfma_f32_16x16x32_bf16 v[60:63], v[192:195], v[44:47], v[60:63]\n"
"s_waitcnt vmcnt(6)\n"
"ds_read_b128 v[40:43], v113 offset:15360\n"
"v_mfma_f32_16x16x32_bf16 v[64:67], v[196:199], v[44:47], v[64:67]\n"
"s_barrier\n"
"v_mfma_f32_16x16x32_bf16 v[60:63], v[200:203], v[48:51], v[60:63]\n"
"ds_read_b128 v[44:47], v113 offset:16384\n"
"v_mfma_f32_16x16x32_bf16 v[64:67], v[204:207], v[48:51], v[64:67]\n"
"s_waitcnt lgkmcnt(5)\n"
"v_mfma_f32_16x16x32_bf16 v[60:63], v[208:211], v[52:55], v[60:63]\n"
"ds_read_b128 v[48:51], v113 offset:17408\n"
"v_mfma_f32_16x16x32_bf16 v[64:67], v[212:215], v[52:55], v[64:67]\n"
"v_mfma_f32_16x16x32_bf16 v[60:63], v[216:219], v[56:59], v[60:63]\n"
"ds_read_b128 v[52:55], v113 offset:18432\n"
"v_mfma_f32_16x16x32_bf16 v[64:67], v[220:223], v[56:59], v[64:67]\n"
"s_waitcnt lgkmcnt(5)\n"
"v_mfma_f32_16x16x32_bf16 v[60:63], v[224:227], v[28:31], v[60:63]\n"
"ds_read_b128 v[56:59], v113 offset:19456\n"
"v_mfma_f32_16x16x32_bf16 v[64:67], v[228:231], v[28:31], v[64:67]\n"
"v_mfma_f32_16x16x32_bf16 v[60:63], v[232:235], v[32:35], v[60:63]\n"
"ds_read_b128 v[28:31], v113 offset:20480\n"
"v_mfma_f32_16x16x32_bf16 v[64:67], v[236:239], v[32:35], v[64:67]\n"
"s_waitcnt lgkmcnt(5)\n"
"v_mfma_f32_16x16x32_bf16 v[60:63], v[240:243], v[36:39], v[60:63]\n"
"s_waitcnt vmcnt(1)\n"
"ds_read_b128 v[32:35], v113 offset:21504\n"
"v_xor_b32 v4, s37, v4\n"
"v_mfma_f32_16x16x32_bf16 v[64:67], v[244:247], v[36:39], v[64:67]\n"
"v_xor_b32 v5, s37, v5\n"
"v_xor_b32 v6, s37, v6\n"
"v_mfma_f32_16x16x32_bf16 v[60:63], v[248:251], v[40:43], v[60:63]\n"
"ds_read_b128 v[36:39], v113 offset:22528\n"
"v_xor_b32 v7, s37, v7\n"
"v_mfma_f32_16x16x32_bf16 v[64:67], v[252:255], v[40:43], v[64:67]\n"
"s_waitcnt lgkmcnt(5)\n"
"v_xor_b32 v8, s37, v8\n"
"v_xor_b32 v9, s37, v9\n"
"v_mfma_f32_16x16x32_bf16 v[60:63], a[0:3], v[44:47], v[60:63]\n"
"ds_read_b128 v[40:43], v113 offset:23552\n"
"v_xor_b32 v10, s37, v10\n"
"v_mfma_f32_16x16x32_bf16 v[64:67], a[4:7], v[44:47], v[64:67]\n"
"v_xor_b32 v11, s37, v11\n"
"v_xor_b32 v12, s37, v12\n"
"v_mfma_f32_16x16x32_bf16 v[60:63], a[8:11], v[48:51], v[60:63]\n"
"ds_read_b128 v[44:47], v113 offset:24576\n"
"v_xor_b32 v13, s37, v13\n"
"v_mfma_f32_16x16x32_bf16 v[64:67], a[12:15], v[48:51], v[64:67]\n"
"s_waitcnt lgkmcnt(5)\n"
"v_xor_b32 v14, s37, v14\n"
"v_xor_b32 v15, s37, v15\n"
"v_mfma_f32_16x16x32_bf16 v[60:63], a[16:19], v[52:55], v[60:63]\n"
"ds_read_b128 v[48:51], v113 offset:25600\n"
"v_or3_b32 v3, v4, v5, v6\n"
"v_mfma_f32_16x16x32_bf16 v[64:67], a[20:23], v[52:55], v[64:67]\n"
"v_or3_b32 v3, v3, v7, v8\n"
"v_or3_b32 v3, v3, v9, v10\n"
"v_mfma_f32_16x16x32_bf16 v[60:63], a[24:27], v[56:59], v[60:63]\n"
"ds_read_b128 v[52:55], v113 offset:26624\n"
"v_or3_b32 v3, v3, v11, v12\n"
"v_mfma_f32_16x16x32_bf16 v[64:67], a[28:31], v[56:59], v[64:67]\n"
"s_waitcnt lgkmcnt(5)\n"
"v_or3_b32 v3, v3, v13, v14\n"
"v_bitop3_b32 v3, v3, v15, v23 bitop3:0x54\n"
"v_mfma_f32_16x16x32_bf16 v[60:63], a[32:35], v[28:31], v[60:63]\n"
"ds_read_b128 v[56:59], v113 offset:27648\n"
"v_cmp_ne_u32 vcc, 0, v3\n"
"v_mfma_f32_16x16x32_bf16 v[64:67], a[36:39], v[28:31], v[64:67]\n"
"v_lshlrev_b32 v76, 16, v88\n"
"v_and_b32 v77, 0xffff0000, v88\n"
"v_mfma_f32_16x16x32_bf16 v[60:63], a[40:43], v[32:35], v[60:63]\n"
"s_cbranch_vccnz Lslow_p3_K\n"
"Lret_p3_K:\n"
"v_lshlrev_b32 v78, 16, v89\n"
"v_and_b32 v79, 0xffff0000, v89\n"
"v_mfma_f32_16x16x32_bf16 v[64:67], a[44:47], v[32:35], v[64:67]\n"
"s_waitcnt lgkmcnt(4)\n"
"v_lshlrev_b32 v80, 16, v90\n"
"v_and_b32 v81, 0xffff0000, v90\n"
"v_mfma_f32_16x16x32_bf16 v[60:63], a[48:51], v[36:39], v[60:63]\n"
"s_add_u32 s46, s46, 3145728\n"
"v_lshlrev_b32 v82, 16, v91\n"
"v_and_b32 v83, 0xffff0000, v91\n"
"v_mfma_f32_16x16x32_bf16 v[64:67], a[52:55], v[36:39], v[64:67]\n"
"v_lshl_or_b32 v3, v4, 16, v16\n"
"v_and_or_b32 v4, v4, v17, v16\n"
"v_mfma_f32_16x16x32_bf16 v[60:63], a[56:59], v[40:43], v[60:63]\n"
"v_fmac_f32 v76, v104, v3\n"
"v_fmac_f32 v77, v105, v4\n"
"v_mfma_f32_16x16x32_bf16 v[64:67], a[60:63], v[40:43], v[64:67]\n"
"s_waitcnt lgkmcnt(2)\n"
"v_lshl_or_b32 v3, v5, 16, v16\n"
"v_and_or_b32 v5, v5, v17, v16\n"
"v_mfma_f32_16x16x32_bf16 v[60:63], a[64:67], v[44:47], v[60:63]\n"
"v_fmac_f32 v78, v106, v3\n"
"v_fmac_f32 v79, v107, v5\n"
"v_mfma_f32_16x16x32_bf16 v[64:67], a[68:71], v[44:47], v[64:67]\n"
"v_lshl_or_b32 v3, v6, 16, v16\n"
"v_and_or_b32 v6, v6, v17, v16\n"
"v_mfma_f32_16x16x32_bf16 v[60:63], a[72:75], v[48:51], v[60:63]\n"
"v_fmac_f32 v80, v108, v3\n"
"v_fmac_f32 v81, v109, v6\n"
"v_mfma_f32_16x16x32_bf16 v[64:67], a[76:79], v[48:51], v[64:67]\n"
"s_waitcnt lgkmcnt(0)\n"
"v_lshl_or_b32 v3, v7, 16, v16\n"
"v_and_or_b32 v7, v7, v17, v16\n"
"v_mfma_f32_16x16x32_bf16 v[60:63], a[80:83], v[52:55], v[60:63]\n"
"v_fmac_f32 v82, v110, v3\n"
"v_fmac_f32 v83, v111, v7\n"
"v_mfma_f32_16x16x32_bf16 v[64:67], a[84:87], v[52:55], v[64:67]\n"
"v_lshl_or_b32 v3, v8, 16, v16\n"
"v_and_or_b32 v8, v8, v17, v16\n"
"v_mfma_f32_16x16x32_bf16 v[60:63], a[88:91], v[56:59], v[60:63]\n"
"v_fmac_f32 v76, v104, v3\n"
"v_fmac_f32 v77, v105, v8\n"
"v_mfma_f32_16x16x32_bf16 v[64:67], a[92:95], v[56:59], v[64:67]\n"
"v_lshl_or_b32 v3, v9, 16, v16\n"
"v_and_or_b32 v9, v9, v17, v16\n"
"v_fmac_f32 v78, v106, v3\n"
"v_fmac_f32 v79, v107, v9\n"
"v_lshl_or_b32 v3, v10, 16, v16\n"
"v_and_or_b32 v10, v10, v17, v16\n"
"v_fmac_f32 v80, v108, v3\n"
"v_fmac_f32 v81, v109, v10\n"
"v_lshl_or_b32 v3, v11, 16, v16\n"
"v_and_or_b32 v11, v11, v17, v16\n"
"v_fmac_f32 v82, v110, v3\n"
"v_fmac_f32 v83, v111, v11\n"
"v_lshl_or_b32 v3, v12, 16, v16\n"
"v_and_or_b32 v12, v12, v17, v16\n"
"v_fmac_f32 v76, v104, v3\n"
"v_fmac_f32 v77, v105, v12\n"
"v_lshl_or_b32 v3, v13, 16, v16\n"
"v_and_or_b32 v13, v13, v17, v16\n"
"v_fmac_f32 v78, v106, v3\n"
"v_fmac_f32 v79, v107, v13\n"
"v_lshl_or_b32 v3, v14, 16, v16\n"
"v_and_or_b32 v14, v14, v17, v16\n"
"v_fmac_f32 v80, v108, v3\n"
"v_fmac_f32 v81, v109, v14\n"
"v_lshl_or_b32 v3, v15, 16, v16\n"
"v_and_or_b32 v15, v15, v17, v16\n"
"v_fmac_f32 v82, v110, v3\n"
"v_fmac_f32 v83, v111, v15\n"
"s_nop 7\n"
"v_fmac_f32 v76, v104, v60\n"
"v_fmac_f32 v77, v105, v61\n"
"v_fmac_f32 v78, v106, v62\n"
"v_fmac_f32 v79, v107, v63\n"
"v_fmac_f32 v80, v108, v64\n"
"v_fmac_f32 v81, v109, v65\n"
"v_fmac_f32 v82, v110, v66\n"
"v_fmac_f32 v83, v111, v67\n"
"v_cvt_pk_bf16_f32 v84, v76, v77\n"
"v_cvt_pk_bf16_f32 v85, v78, v79\n"
"v_cvt_pk_bf16_f32 v86, v80, v81\n"
"v_cvt_pk_bf16_f32 v87, v82, v83\n"
"buffer_store_dwordx4 v[84:87], v127, s[84:87], s48 offen\n"
"s_branch Lend_K\n"
"Lslow_h0_K:\n"
"s_sleep 1\n"
"buffer_load_dword v2, v0, s[72:75], 0 offen offset:0 sc1\n"
"s_waitcnt vmcnt(0)\n"
"v_cmp_gt_u32 vcc, s49, v2\n"
"s_nop 1\n"
"s_and_b64 vcc, vcc, 1\n"
"s_add_u32 s94, s94, 1\n"
"s_cmp_gt_u32 s94, 0x20000\n"
"s_cbranch_scc1 Lret_h0_K\n"
"s_nop 1\n"
"s_cbranch_vccnz Lslow_h0_K\n"
"s_branch Lret_h0_K\n"
"Lslow_h1_K:\n"
"s_sleep 1\n"
"buffer_load_dword v2, v0, s[72:75], 0 offen offset:256 sc1\n"
"s_waitcnt vmcnt(0)\n"
"v_cmp_gt_u32 vcc, s49, v2\n"
"s_nop 1\n"
"s_and_b64 vcc, vcc, 1\n"
"s_add_u32 s94, s94, 1\n"
"s_cmp_gt_u32 s94, 0x20000\n"
"s_cbranch_scc1 Lret_h1_K\n"
"s_nop 1\n"
"s_cbranch_vccnz Lslow_h1_K\n"
"s_branch Lret_h1_K\n"
"Lslow_h2_K:\n"
"s_sleep 1\n"
"buffer_load_dword v2, v0, s[72:75], 0 offen offset:512 sc1\n"
"s_waitcnt vmcnt(0)\n"
"v_cmp_gt_u32 vcc, s49, v2\n"
"s_nop 1\n"
"s_and_b64 vcc, vcc, 1\n"
"s_add_u32 s94, s94, 1\n"
"s_cmp_gt_u32 s94, 0x20000\n"
"s_cbranch_scc1 Lret_h2_K\n"
"s_nop 1\n"
"s_cbranch_vccnz Lslow_h2_K\n"
"s_branch Lret_h2_K\n"
"Lslow_h3_K:\n"
"s_sleep 1\n"
"buffer_load_dword v2, v0, s[72:75], 0 offen offset:768 sc1\n"
"s_waitcnt vmcnt(0)\n"
"v_cmp_gt_u32 vcc, s49, v2\n"
"s_nop 1\n"
"s_and_b64 vcc, vcc, 1\n"
"s_add_u32 s94, s94, 1\n"
"s_cmp_gt_u32 s94, 0x20000\n"
"s_cbranch_scc1 Lret_h3_K\n"
"s_nop 1\n"
"s_cbranch_vccnz Lslow_h3_K\n"
"s_branch Lret_h3_K\n"
"Lslow_x1_K:\n"
"s_sleep 1\n"
"buffer_load_dword v21, v20, s[72:75], s93 offen sc1\n"
"s_waitcnt vmcnt(0)\n"
"v_cmp_ne_u32 vcc, s50, v21\n"
"s_add_u32 s94, s94, 1\n"
"s_cmp_gt_u32 s94, 0x20000\n"
"s_cbranch_scc1 Lret_x1_K\n"
"s_nop 1\n"
"s_cbranch_vccnz Lslow_x1_K\n"
"s_branch Lret_x1_K\n"
"Lslow_p0_K:\n"
"s_sleep 1\n"
"buffer_load_dwordx4 v[4:7], v113, s[72:75], s46 offen sc1\n"
"buffer_load_dwordx4 v[8:11], v113, s[72:75], s95 offen sc1\n"
"buffer_load_dwordx4 v[12:15], v113, s[72:75], s61 offen sc1\n"
"s_waitcnt vmcnt(0)\n"
"v_xor_b32 v4, s37, v4\n"
"v_xor_b32 v5, s37, v5\n"
"v_xor_b32 v6, s37, v6\n"
"v_xor_b32 v7, s37, v7\n"
"v_xor_b32 v8, s37, v8\n"
"v_xor_b32 v9, s37, v9\n"
"v_xor_b32 v10, s37, v10\n"
"v_xor_b32 v11, s37, v11\n"
"v_xor_b32 v12, s37, v12\n"
"v_xor_b32 v13, s37, v13\n"
"v_xor_b32 v14, s37, v14\n"
"v_xor_b32 v15, s37, v15\n"
"v_or3_b32 v3, v4, v5, v6\n"
"v_or3_b32 v3, v3, v7, v8\n"
"v_or3_b32 v3, v3, v9, v10\n"
"v_or3_b32 v3, v3, v11, v12\n"
"v_or3_b32 v3, v3, v13, v14\n"
"v_bitop3_b32 v3, v3, v15, v23 bitop3:0x54\n"
"v_cmp_ne_u32 vcc, 0, v3\n"
"s_add_u32 s94, s94, 1\n"
"s_cmp_gt_u32 s94, 0x20000\n"
"s_cbranch_scc1 Lret_p0_K\n"
"s_nop 1\n"
"s_cbranch_vccnz Lslow_p0_K\n"
"s_branch Lret_p0_K\n"
"Lslow_p1_K:\n"
"s_sleep 1\n"
"buffer_load_dwordx4 v[4:7], v113, s[72:75], s46 offen sc1\n"
"buffer_load_dwordx4 v[8:11], v113, s[72:75], s95 offen sc1\n"
"buffer_load_dwordx4 v[12:15], v113, s[72:75], s61 offen sc1\n"
"s_waitcnt vmcnt(0)\n"
"v_xor_b32 v4, s37, v4\n"
"v_xor_b32 v5, s37, v5\n"
"v_xor_b32 v6, s37, v6\n"
"v_xor_b32 v7, s37, v7\n"
"v_xor_b32 v8, s37, v8\n"
"v_xor_b32 v9, s37, v9\n"
"v_xor_b32 v10, s37, v10\n"
"v_xor_b32 v11, s37, v11\n"
"v_xor_b32 v12, s37, v12\n"
"v_xor_b32 v13, s37, v13\n"
"v_xor_b32 v14, s37, v14\n"
"v_xor_b32 v15, s37, v15\n"
"v_or3_b32 v3, v4, v5, v6\n"
"v_or3_b32 v3, v3, v7, v8\n"
"v_or3_b32 v3, v3, v9, v10\n"
"v_or3_b32 v3, v3, v11, v12\n"
"v_or3_b32 v3, v3, v13, v14\n"
"v_bitop3_b32 v3, v3, v15, v23 bitop3:0x54\n"
"v_cmp_ne_u32 vcc, 0, v3\n"
"s_add_u32 s94, s94, 1\n"
"s_cmp_gt_u32 s94, 0x20000\n"
"s_cbranch_scc1 Lret_p1_K\n"
"s_nop 1\n"
"s_cbranch_vccnz Lslow_p1_K\n"
"s_branch Lret_p1_K\n"
"Lslow_p2_K:\n"
"s_sleep 1\n"
"buffer_load_dwordx4 v[4:7], v113, s[72:75], s46 offen sc1\n"
"buffer_load_dwordx4 v[8:11], v113, s[72:75], s95 offen sc1\n"
"buffer_load_dwordx4 v[12:15], v113, s[72:75], s61 offen sc1\n"
"s_waitcnt vmcnt(0)\n"
"v_xor_b32 v4, s37, v4\n"
"v_xor_b32 v5, s37, v5\n"
"v_xor_b32 v6, s37, v6\n"
"v_xor_b32 v7, s37, v7\n"
"v_xor_b32 v8, s37, v8\n"
"v_xor_b32 v9, s37, v9\n"
"v_xor_b32 v10, s37, v10\n"
"v_xor_b32 v11, s37, v11\n"
"v_xor_b32 v12, s37, v12\n"
"v_xor_b32 v13, s37, v13\n"
"v_xor_b32 v14, s37, v14\n"
"v_xor_b32 v15, s37, v15\n"
"v_or3_b32 v3, v4, v5, v6\n"
"v_or3_b32 v3, v3, v7, v8\n"
"v_or3_b32 v3, v3, v9, v10\n"
"v_or3_b32 v3, v3, v11, v12\n"
"v_or3_b32 v3, v3, v13, v14\n"
"v_bitop3_b32 v3, v3, v15, v23 bitop3:0x54\n"
"v_cmp_ne_u32 vcc, 0, v3\n"
"s_add_u32 s94, s94, 1\n"
"s_cmp_gt_u32 s94, 0x20000\n"
"s_cbranch_scc1 Lret_p2_K\n"
"s_nop 1\n"
"s_cbranch_vccnz Lslow_p2_K\n"
"s_branch Lret_p2_K\n"
"Lslow_p3_K:\n"
"s_sleep 1\n"
"buffer_load_dwordx4 v[4:7], v113, s[72:75], s46 offen sc1\n"
"buffer_load_dwordx4 v[8:11], v113, s[72:75], s95 offen sc1\n"
"buffer_load_dwordx4 v[12:15], v113, s[72:75], s61 offen sc1\n"
"s_waitcnt vmcnt(0)\n"
"v_xor_b32 v4, s37, v4\n"
"v_xor_b32 v5, s37, v5\n"
"v_xor_b32 v6, s37, v6\n"
"v_xor_b32 v7, s37, v7\n"
"v_xor_b32 v8, s37, v8\n"
"v_xor_b32 v9, s37, v9\n"
"v_xor_b32 v10, s37, v10\n"
"v_xor_b32 v11, s37, v11\n"
"v_xor_b32 v12, s37, v12\n"
"v_xor_b32 v13, s37, v13\n"
"v_xor_b32 v14, s37, v14\n"
"v_xor_b32 v15, s37, v15\n"
"v_or3_b32 v3, v4, v5, v6\n"
"v_or3_b32 v3, v3, v7, v8\n"
"v_or3_b32 v3, v3, v9, v10\n"
"v_or3_b32 v3, v3, v11, v12\n"
"v_or3_b32 v3, v3, v13, v14\n"
"v_bitop3_b32 v3, v3, v15, v23 bitop3:0x54\n"
"v_cmp_ne_u32 vcc, 0, v3\n"
"s_add_u32 s94, s94, 1\n"
"s_cmp_gt_u32 s94, 0x20000\n"
"s_cbranch_scc1 Lret_p3_K\n"
"s_nop 1\n"
"s_cbranch_vccnz Lslow_p3_K\n"
"s_branch Lret_p3_K\n"
"Lk2five_K:\n"
"s_add_u32 m0, s44, 65536\n"
"s_nop 0\n"
"buffer_load_dwordx4 v122, s[76:79], s42 offen offset:0 lds\n"
"v_and_b32 v144, 0xffff, v128\n"
"v_and_b32 v145, 0xffff, v132\n"
"v_and_b32 v146, 0xffff, v136\n"
"v_and_b32 v147, 0xffff, v140\n"
"s_nop 1\n"
"v_mfma_f32_16x16x32_f16 v[64:67], v[144:147], v[0:3], 0\n"
"v_mov_b32 v148, v129\n"
"v_mov_b32 v149, v133\n"
"v_mfma_f32_16x16x32_f16 v[68:71], v[144:147], v[4:7], 0\n"
"v_mov_b32 v150, v137\n"
"v_mov_b32 v151, v141\n"
"s_nop 1\n"
"v_mfma_f32_16x16x32_f16 v[72:75], v[148:151], v[0:3], 0\n"
"v_mov_b32 v152, v130\n"
"v_mov_b32 v153, v134\n"
"v_mfma_f32_16x16x32_f16 v[76:79], v[148:151], v[4:7], 0\n"
"v_mov_b32 v154, v138\n"
"v_mov_b32 v155, v142\n"
"s_nop 1\n"
"v_mfma_f32_16x16x32_f16 v[80:83], v[152:155], v[0:3], 0\n"
"buffer_load_dwordx4 v123, s[76:79], s42 offen offset:1024 lds\n"
"v_mov_b32 v156, v131\n"
"v_mov_b32 v157, v135\n"
"v_mfma_f32_16x16x32_f16 v[84:87], v[152:155], v[4:7], 0\n"
"v_mov_b32 v158, v139\n"
"v_mov_b32 v159, v143\n"
"s_nop 1\n"
"v_mfma_f32_16x16x32_f16 v[88:91], v[156:159], v[0:3], 0\n"
"v_lshrrev_b32 v212, 16, v128\n"
"v_lshrrev_b32 v213, 16, v132\n"
"v_mfma_f32_16x16x32_f16 v[92:95], v[156:159], v[4:7], 0\n"
"v_lshrrev_b32 v214, 16, v136\n"
"v_lshrrev_b32 v215, 16, v140\n"
"s_nop 1\n"
"v_mfma_f32_16x16x32_f16 v[184:187], v[212:215], v[0:3], 0\n"
"v_mfma_f32_16x16x32_f16 v[188:191], v[212:215], v[4:7], 0\n"
"v_pk_mul_f32 v[144:145], v[64:65], v[216:217]\n"
"v_pk_mul_f32 v[146:147], v[66:67], v[218:219]\n"
"v_pk_fma_f32 v[144:145], v[68:69], v[220:221], v[144:145]\n"
"v_pk_fma_f32 v[146:147], v[70:71], v[222:223], v[146:147]\n"
"buffer_load_dwordx4 v124, s[76:79], s42 offen offset:2048 lds\n"
"v_pk_mul_f32 v[68:69], v[68:69], v[216:217]\n"
"v_pk_mul_f32 v[70:71], v[70:71], v[218:219]\n"
"v_pk_fma_f32 v[68:69], v[64:65], v[220:221], v[68:69] neg_lo:[1,0,0] neg_hi:[1,0,0]\n"
"v_pk_fma_f32 v[70:71], v[66:67], v[222:223], v[70:71] neg_lo:[1,0,0] neg_hi:[1,0,0]\n"
"v_pk_mul_f32 v[148:149], v[72:73], v[216:217]\n"
"v_pk_mul_f32 v[150:151], v[74:75], v[218:219]\n"
"v_pk_fma_f32 v[148:149], v[76:77], v[220:221], v[148:149]\n"
"v_pk_fma_f32 v[150:151], v[78:79], v[222:223], v[150:151]\n"
"v_pk_mul_f32 v[76:77], v[76:77], v[216:217]\n"
"v_pk_mul_f32 v[78:79], v[78:79], v[218:219]\n"
"v_pk_fma_f32 v[76:77], v[72:73], v[220:221], v[76:77] neg_lo:[1,0,0] neg_hi:[1,0,0]\n"
"v_pk_fma_f32 v[78:79], v[74:75], v[222:223], v[78:79] neg_lo:[1,0,0] neg_hi:[1,0,0]\n"
"v_pk_mul_f32 v[152:153], v[80:81], v[216:217]\n"
"v_pk_mul_f32 v[154:155], v[82:83], v[218:219]\n"
"v_pk_fma_f32 v[152:153], v[84:85], v[220:221], v[152:153]\n"
"v_pk_fma_f32 v[154:155], v[86:87], v[222:223], v[154:155]\n"
"v_pk_mul_f32 v[84:85], v[84:85], v[216:217]\n"
"v_pk_mul_f32 v[86:87], v[86:87], v[218:219]\n"
"v_pk_fma_f32 v[84:85], v[80:81], v[220:221], v[84:85] neg_lo:[1,0,0] neg_hi:[1,0,0]\n"
"v_pk_fma_f32 v[86:87], v[82:83], v[222:223], v[86:87] neg_lo:[1,0,0] neg_hi:[1,0,0]\n"
"v_pk_mul_f32 v[156:157], v[88:89], v[216:217]\n"
"v_pk_mul_f32 v[158:159], v[90:91], v[218:219]\n"
"v_pk_fma_f32 v[156:157], v[92:93], v[220:221], v[156:157]\n"
"v_pk_fma_f32 v[158:159], v[94:95], v[222:223], v[158:159]\n"
"v_pk_mul_f32 v[92:93], v[92:93], v[216:217]\n"
"v_pk_mul_f32 v[94:95], v[94:95], v[218:219]\n"
"buffer_load_dwordx4 v125, s[76:79], s42 offen offset:3072 lds\n"
"s_add_u32 s42, s42, 458752\n"
"v_pk_fma_f32 v[92:93], v[88:89], v[220:221], v[92:93] neg_lo:[1,0,0] neg_hi:[1,0,0]\n"
"v_pk_fma_f32 v[94:95], v[90:91], v[222:223], v[94:95] neg_lo:[1,0,0] neg_hi:[1,0,0]\n"
"v_pk_mul_f32 v[212:213], v[184:185], v[216:217]\n"
"v_pk_mul_f32 v[214:215], v[186:187], v[218:219]\n"
"v_pk_fma_f32 v[212:213], v[188:189], v[220:221], v[212:213]\n"
"v_pk_fma_f32 v[214:215], v[190:191], v[222:223], v[214:215]\n"
"v_pk_mul_f32 v[188:189], v[188:189], v[216:217]\n"
"v_pk_mul_f32 v[190:191], v[190:191], v[218:219]\n"
"v_pk_fma_f32 v[188:189], v[184:185], v[220:221], v[188:189] neg_lo:[1,0,0] neg_hi:[1,0,0]\n"
"v_pk_fma_f32 v[190:191], v[186:187], v[222:223], v[190:191] neg_lo:[1,0,0] neg_hi:[1,0,0]\n"
"v_cvt_pk_f16_f32 v144, v144, v68\n"
"v_cvt_pk_f16_f32 v145, v145, v69\n"
"v_cvt_pk_f16_f32 v146, v146, v70\n"
"v_cvt_pk_f16_f32 v147, v147, v71\n"
"s_nop 1\n"
"v_mfma_f32_16x16x32_f16 v[64:67], v[8:11], v[144:147], 0\n"
"v_cvt_pk_f16_f32 v148, v148, v76\n"
"v_cvt_pk_f16_f32 v149, v149, v77\n"
"v_mfma_f32_16x16x32_f16 v[68:71], v[12:15], v[144:147], 0\n"
"v_cvt_pk_f16_f32 v150, v150, v78\n"
"v_cvt_pk_f16_f32 v151, v151, v79\n"
"s_nop 1\n"
"v_mfma_f32_16x16x32_f16 v[72:75], v[8:11], v[148:151], 0\n"
"s_add_u32 m0, s44, 81920\n"
"s_nop 0\n"
"buffer_load_dwordx4 v122, s[76:79], s42 offen offset:0 lds\n"
"v_cvt_pk_f16_f32 v152, v152, v84\n"
"v_cvt_pk_f16_f32 v153, v153, v85\n"
"v_mfma_f32_16x16x32_f16 v[76:79], v[12:15], v[148:151], 0\n"
"v_cvt_pk_f16_f32 v154, v154, v86\n"
"v_cvt_pk_f16_f32 v155, v155, v87\n"
"s_nop 1\n"
"v_mfma_f32_16x16x32_f16 v[80:83], v[8:11], v[152:155], 0\n"
"v_cvt_pk_f16_f32 v156, v156, v92\n"
"v_cvt_pk_f16_f32 v157, v157, v93\n"
"v_mfma_f32_16x16x32_f16 v[84:87], v[12:15], v[152:155], 0\n"
"v_cvt_pk_f16_f32 v158, v158, v94\n"
"v_cvt_pk_f16_f32 v159, v159, v95\n"
"s_nop 1\n"
"v_mfma_f32_16x16x32_f16 v[88:91], v[8:11], v[156:159], 0\n"
"v_cvt_pk_f16_f32 v212, v212, v188\n"
"v_cvt_pk_f16_f32 v213, v213, v189\n"
"v_mfma_f32_16x16x32_f16 v[92:95], v[12:15], v[156:159], 0\n"
"v_cvt_pk_f16_f32 v214, v214, v190\n"
"v_cvt_pk_f16_f32 v215, v215, v191\n"
"buffer_load_dwordx4 v123, s[76:79], s42 offen offset:1024 lds\n"
"s_nop 0\n"
"v_mfma_f32_16x16x32_f16 v[184:187], v[8:11], v[212:215], 0\n"
"v_mfma_f32_16x16x32_f16 v[188:191], v[12:15], v[212:215], 0\n"
"v_pk_mul_f32 v[144:145], v[64:65], v[32:33]\n"
"v_pk_mul_f32 v[146:147], v[66:67], v[34:35]\n"
"v_pk_fma_f32 v[144:145], v[68:69], v[36:37], v[144:145] neg_lo:[1,0,0] neg_hi:[1,0,0]\n"
"v_pk_fma_f32 v[146:147], v[70:71], v[38:39], v[146:147] neg_lo:[1,0,0] neg_hi:[1,0,0]\n"
"v_pk_mul_f32 v[68:69], v[68:69], v[32:33]\n"
"v_pk_mul_f32 v[70:71], v[70:71], v[34:35]\n"
"v_pk_fma_f32 v[68:69], v[64:65], v[36:37], v[68:69]\n"
"v_pk_fma_f32 v[70:71], v[66:67], v[38:39], v[70:71]\n"
"v_pk_mul_f32 v[148:149], v[72:73], v[40:41]\n"
"v_pk_mul_f32 v[150:151], v[74:75], v[42:43]\n"
"v_pk_fma_f32 v[148:149], v[76:77], v[44:45], v[148:149] neg_lo:[1,0,0] neg_hi:[1,0,0]\n"
"v_pk_fma_f32 v[150:151], v[78:79], v[46:47], v[150:151] neg_lo:[1,0,0] neg_hi:[1,0,0]\n"
"v_pk_mul_f32 v[76:77], v[76:77], v[40:41]\n"
"v_pk_mul_f32 v[78:79], v[78:79], v[42:43]\n"
"v_pk_fma_f32 v[76:77], v[72:73], v[44:45], v[76:77]\n"
"v_pk_fma_f32 v[78:79], v[74:75], v[46:47], v[78:79]\n"
"v_pk_mul_f32 v[152:153], v[80:81], v[48:49]\n"
"v_pk_mul_f32 v[154:155], v[82:83], v[50:51]\n"
"v_pk_fma_f32 v[152:153], v[84:85], v[52:53], v[152:153] neg_lo:[1,0,0] neg_hi:[1,0,0]\n"
"v_pk_fma_f32 v[154:155], v[86:87], v[54:55], v[154:155] neg_lo:[1,0,0] neg_hi:[1,0,0]\n"
"buffer_load_dwordx4 v124, s[76:79], s42 offen offset:2048 lds\n"
"v_pk_mul_f32 v[84:85], v[84:85], v[48:49]\n"
"v_pk_mul_f32 v[86:87], v[86:87], v[50:51]\n"
"v_pk_fma_f32 v[84:85], v[80:81], v[52:53], v[84:85]\n"
"v_pk_fma_f32 v[86:87], v[82:83], v[54:55], v[86:87]\n"
"v_pk_mul_f32 v[156:157], v[88:89], v[56:57]\n"
"v_pk_mul_f32 v[158:159], v[90:91], v[58:59]\n"
"v_pk_fma_f32 v[156:157], v[92:93], v[60:61], v[156:157] neg_lo:[1,0,0] neg_hi:[1,0,0]\n"
"v_pk_fma_f32 v[158:159], v[94:95], v[62:63], v[158:159] neg_lo:[1,0,0] neg_hi:[1,0,0]\n"
"v_pk_mul_f32 v[92:93], v[92:93], v[56:57]\n"
"v_pk_mul_f32 v[94:95], v[94:95], v[58:59]\n"
"v_pk_fma_f32 v[92:93], v[88:89], v[60:61], v[92:93]\n"
"v_pk_fma_f32 v[94:95], v[90:91], v[62:63], v[94:95]\n"
"v_pk_mul_f32 v[212:213], v[184:185], v[176:177]\n"
"v_pk_mul_f32 v[214:215], v[186:187], v[178:179]\n"
"v_pk_fma_f32 v[212:213], v[188:189], v[180:181], v[212:213] neg_lo:[1,0,0] neg_hi:[1,0,0]\n"
"v_pk_fma_f32 v[214:215], v[190:191], v[182:183], v[214:215] neg_lo:[1,0,0] neg_hi:[1,0,0]\n"
"v_pk_mul_f32 v[188:189], v[188:189], v[176:177]\n"
"v_pk_mul_f32 v[190:191], v[190:191], v[178:179]\n"
"v_pk_fma_f32 v[188:189], v[184:185], v[180:181], v[188:189]\n"
"v_pk_fma_f32 v[190:191], v[186:187], v[182:183], v[190:191]\n"
"v_cvt_pk_f16_f32 v144, v144, v68\n"
"v_cvt_pk_f16_f32 v145, v145, v69\n"
"v_cvt_pk_f16_f32 v146, v146, v70\n"
"v_cvt_pk_f16_f32 v147, v147, v71\n"
"s_nop 1\n"
"v_mfma_f32_16x16x32_f16 v[64:67], v[144:147], v[16:19], 0\n"
"buffer_load_dwordx4 v125, s[76:79], s42 offen offset:3072 lds\n"
"s_add_u32 s42, s42, 458752\n"
"v_cvt_pk_f16_f32 v148, v148, v76\n"
"v_cvt_pk_f16_f32 v149, v149, v77\n"
"v_mfma_f32_16x16x32_f16 v[68:71], v[144:147], v[20:23], 0\n"
"v_cvt_pk_f16_f32 v150, v150, v78\n"
"v_cvt_pk_f16_f32 v151, v151, v79\n"
"s_nop 1\n"
"v_mfma_f32_16x16x32_f16 v[72:75], v[148:151], v[16:19], 0\n"
"v_cvt_pk_f16_f32 v152, v152, v84\n"
"v_cvt_pk_f16_f32 v153, v153, v85\n"
"v_mfma_f32_16x16x32_f16 v[76:79], v[148:151], v[20:23], 0\n"
"v_cvt_pk_f16_f32 v154, v154, v86\n"
"v_cvt_pk_f16_f32 v155, v155, v87\n"
"s_nop 1\n"
"v_mfma_f32_16x16x32_f16 v[80:83], v[152:155], v[16:19], 0\n"
"v_cvt_pk_f16_f32 v156, v156, v92\n"
"v_cvt_pk_f16_f32 v157, v157, v93\n"
"v_mfma_f32_16x16x32_f16 v[84:87], v[152:155], v[20:23], 0\n"
"v_cvt_pk_f16_f32 v158, v158, v94\n"
"v_cvt_pk_f16_f32 v159, v159, v95\n"
"s_add_u32 m0, s44, 98304\n"
"s_nop 0\n"
"buffer_load_dwordx4 v122, s[76:79], s42 offen offset:0 lds\n"
"v_mfma_f32_16x16x32_f16 v[88:91], v[156:159], v[16:19], 0\n"
"v_cvt_pk_f16_f32 v212, v212, v188\n"
"v_cvt_pk_f16_f32 v213, v213, v189\n"
"v_mfma_f32_16x16x32_f16 v[92:95], v[156:159], v[20:23], 0\n"
"v_cvt_pk_f16_f32 v214, v214, v190\n"
"v_cvt_pk_f16_f32 v215, v215, v191\n"
"s_nop 1\n"
"v_mfma_f32_16x16x32_f16 v[184:187], v[212:215], v[16:19], 0\n"
"v_mfma_f32_16x16x32_f16 v[188:191], v[212:215], v[20:23], 0\n"
"v_pk_mul_f32 v[144:145], v[64:65], v[224:225]\n"
"v_pk_mul_f32 v[146:147], v[66:67], v[226:227]\n"
"v_pk_fma_f32 v[144:145], v[68:69], v[228:229], v[144:145] neg_lo:[1,0,0] neg_hi:[1,0,0]\n"
"v_pk_fma_f32 v[146:147], v[70:71], v[230:231], v[146:147] neg_lo:[1,0,0] neg_hi:[1,0,0]\n"
"v_pk_mul_f32 v[68:69], v[68:69], v[224:225]\n"
"v_pk_mul_f32 v[70:71], v[70:71], v[226:227]\n"
"v_pk_fma_f32 v[68:69], v[64:65], v[228:229], v[68:69]\n"
"v_pk_fma_f32 v[70:71], v[66:67], v[230:231], v[70:71]\n"
"v_pk_mul_f32 v[148:149], v[72:73], v[224:225]\n"
"v_pk_mul_f32 v[150:151], v[74:75], v[226:227]\n"
"buffer_load_dwordx4 v123, s[76:79], s42 offen offset:1024 lds\n"
"v_pk_fma_f32 v[148:149], v[76:77], v[228:229], v[148:149] neg_lo:[1,0,0] neg_hi:[1,0,0]\n"
"v_pk_fma_f32 v[150:151], v[78:79], v[230:231], v[150:151] neg_lo:[1,0,0] neg_hi:[1,0,0]\n"
"v_pk_mul_f32 v[76:77], v[76:77], v[224:225]\n"
"v_pk_mul_f32 v[78:79], v[78:79], v[226:227]\n"
"v_pk_fma_f32 v[76:77], v[72:73], v[228:229], v[76:77]\n"
"v_pk_fma_f32 v[78:79], v[74:75], v[230:231], v[78:79]\n"
"v_pk_mul_f32 v[152:153], v[80:81], v[224:225]\n"
"v_pk_mul_f32 v[154:155], v[82:83], v[226:227]\n"
"v_pk_fma_f32 v[152:153], v[84:85], v[228:229], v[152:153] neg_lo:[1,0,0] neg_hi:[1,0,0]\n"
"v_pk_fma_f32 v[154:155], v[86:87], v[230:231], v[154:155] neg_lo:[1,0,0] neg_hi:[1,0,0]\n"
"v_pk_mul_f32 v[84:85], v[84:85], v[224:225]\n"
"v_pk_mul_f32 v[86:87], v[86:87], v[226:227]\n"
"v_pk_fma_f32 v[84:85], v[80:81], v[228:229], v[84:85]\n"
"v_pk_fma_f32 v[86:87], v[82:83], v[230:231], v[86:87]\n"
"v_pk_mul_f32 v[156:157], v[88:89], v[224:225]\n"
"v_pk_mul_f32 v[158:159], v[90:91], v[226:227]\n"
"v_pk_fma_f32 v[156:157], v[92:93], v[228:229], v[156:157] neg_lo:[1,0,0] neg_hi:[1,0,0]\n"
"v_pk_fma_f32 v[158:159], v[94:95], v[230:231], v[158:159] neg_lo:[1,0,0] neg_hi:[1,0,0]\n"
"v_pk_mul_f32 v[92:93], v[92:93], v[224:225]\n"
"v_pk_mul_f32 v[94:95], v[94:95], v[226:227]\n"
"v_pk_fma_f32 v[92:93], v[88:89], v[228:229], v[92:93]\n"
"v_pk_fma_f32 v[94:95], v[90:91], v[230:231], v[94:95]\n"
"v_pk_mul_f32 v[212:213], v[184:185], v[224:225]\n"
"v_pk_mul_f32 v[214:215], v[186:187], v[226:227]\n"
"v_pk_fma_f32 v[212:213], v[188:189], v[228:229], v[212:213] neg_lo:[1,0,0] neg_hi:[1,0,0]\n"
"v_pk_fma_f32 v[214:215], v[190:191], v[230:231], v[214:215] neg_lo:[1,0,0] neg_hi:[1,0,0]\n"
"buffer_load_dwordx4 v124, s[76:79], s42 offen offset:2048 lds\n"
"v_pk_mul_f32 v[188:189], v[188:189], v[224:225]\n"
"v_pk_mul_f32 v[190:191], v[190:191], v[226:227]\n"
"v_pk_fma_f32 v[188:189], v[184:185], v[228:229], v[188:189]\n"
"v_pk_fma_f32 v[190:191], v[186:187], v[230:231], v[190:191]\n"
"v_cvt_pk_f16_f32 v144, v144, v68\n"
"v_cvt_pk_f16_f32 v145, v145, v69\n"
"v_cvt_pk_f16_f32 v146, v146, v70\n"
"v_cvt_pk_f16_f32 v147, v147, v71\n"
"s_nop 1\n"
"v_mfma_f32_16x16x32_f16 v[64:67], v[24:27], v[144:147], 0\n"
"v_cvt_pk_f16_f32 v148, v148, v76\n"
"v_cvt_pk_f16_f32 v149, v149, v77\n"
"v_mfma_f32_16x16x32_f16 v[68:71], v[28:31], v[144:147], 0\n"
"v_cvt_pk_f16_f32 v150, v150, v78\n"
"v_cvt_pk_f16_f32 v151, v151, v79\n"
"s_nop 1\n"
"v_mfma_f32_16x16x32_f16 v[72:75], v[24:27], v[148:151], 0\n"
"v_cvt_pk_f16_f32 v152, v152, v84\n"
"v_cvt_pk_f16_f32 v153, v153, v85\n"
"v_mfma_f32_16x16x32_f16 v[76:79], v[28:31], v[148:151], 0\n"
"v_cvt_pk_f16_f32 v154, v154, v86\n"
"buffer_load_dwordx4 v125, s[76:79], s42 offen offset:3072 lds\n"
"s_add_u32 s42, s42, 458752\n"
"v_cvt_pk_f16_f32 v155, v155, v87\n"
"s_nop 1\n"
"v_mfma_f32_16x16x32_f16 v[80:83], v[24:27], v[152:155], 0\n"
"v_cvt_pk_f16_f32 v156, v156, v92\n"
"v_cvt_pk_f16_f32 v157, v157, v93\n"
"v_mfma_f32_16x16x32_f16 v[84:87], v[28:31], v[152:155], 0\n"
"v_cvt_pk_f16_f32 v158, v158, v94\n"
"v_cvt_pk_f16_f32 v159, v159, v95\n"
"s_nop 1\n"
"v_mfma_f32_16x16x32_f16 v[88:91], v[24:27], v[156:159], 0\n"
"v_cvt_pk_f16_f32 v212, v212, v188\n"
"v_cvt_pk_f16_f32 v213, v213, v189\n"
"v_mfma_f32_16x16x32_f16 v[92:95], v[28:31], v[156:159], 0\n"
"v_cvt_pk_f16_f32 v214, v214, v190\n"
"v_cvt_pk_f16_f32 v215, v215, v191\n"
"s_nop 1\n"
"v_mfma_f32_16x16x32_f16 v[184:187], v[24:27], v[212:215], 0\n"
"v_mfma_f32_16x16x32_f16 v[188:191], v[28:31], v[212:215], 0\n"
"v_cvt_pk_f16_f32 v129, v72, v76\n"
"v_cvt_pk_f16_f32 v130, v80, v84\n"
"v_cvt_pk_f16_f32 v131, v88, v92\n"
"s_nop 3\n"
"v_cvt_pk_f16_f32 v128, v64, v184\n"
"v_and_or_b32 v128, v128, v236, s37\n"
"v_and_or_b32 v129, v129, v236, s37\n"
"v_and_or_b32 v130, v130, v236, s37\n"
"v_and_or_b32 v131, v131, v236, s37\n"
"ds_write_b128 v101, v[128:131] offset:0\n"
"v_cvt_pk_f16_f32 v133, v73, v77\n"
"v_cvt_pk_f16_f32 v134, v81, v85\n"
"v_cvt_pk_f16_f32 v135, v89, v93\n"
"v_cvt_pk_f16_f32 v132, v65, v185\n"
"v_and_or_b32 v132, v132, v236, s37\n"
"v_and_or_b32 v133, v133, v236, s37\n"
"v_and_or_b32 v134, v134, v236, s37\n"
"v_and_or_b32 v135, v135, v236, s37\n"
"ds_write_b128 v101, v[132:135] offset:1024\n"
"v_cvt_pk_f16_f32 v137, v74, v78\n"
"v_cvt_pk_f16_f32 v138, v82, v86\n"
"v_cvt_pk_f16_f32 v139, v90, v94\n"
"v_cvt_pk_f16_f32 v136, v66, v186\n"
"v_and_or_b32 v136, v136, v236, s37\n"
"v_and_or_b32 v137, v137, v236, s37\n"
"v_and_or_b32 v138, v138, v236, s37\n"
"v_and_or_b32 v139, v139, v236, s37\n"
"ds_write_b128 v101, v[136:139] offset:2048\n"
"v_cvt_pk_f16_f32 v141, v75, v79\n"
"v_cvt_pk_f16_f32 v142, v83, v87\n"
"v_cvt_pk_f16_f32 v143, v91, v95\n"
"v_cvt_pk_f16_f32 v140, v67, v187\n"
"v_and_or_b32 v140, v140, v236, s37\n"
"v_and_or_b32 v141, v141, v236, s37\n"
"v_and_or_b32 v142, v142, v236, s37\n"
"v_and_or_b32 v143, v143, v236, s37\n"
"ds_write_b128 v101, v[140:143] offset:3072\n"
"s_waitcnt lgkmcnt(0)\n"
"s_branch Lk2done_K\n"
"Lslow_g1_K:\n"
"s_sleep 6\n"
"s_mov_b64 exec, s[28:29]\n"
"s_mov_b32 s36, 196608\n"
"buffer_load_dwordx4 v[128:131], v97, s[72:75], s36 offen sc1\n"
"s_mov_b32 s36, 200704\n"
"buffer_load_dwordx4 v[132:135], v97, s[72:75], s36 offen sc1\n"
"s_mov_b64 exec, s[30:31]\n"
"s_mov_b32 s36, 221184\n"
"buffer_load_dwordx4 v[136:139], v97, s[72:75], s36 offen sc1\n"
"s_mov_b32 s36, 225280\n"
"buffer_load_dwordx4 v[140:143], v97, s[72:75], s36 offen sc1\n"
"s_mov_b64 exec, s[32:33]\n"
"s_mov_b32 s36, 245760\n"
"buffer_load_dwordx4 v[144:147], v97, s[72:75], s36 offen sc1\n"
"s_mov_b32 s36, 249856\n"
"buffer_load_dwordx4 v[148:151], v97, s[72:75], s36 offen sc1\n"
"s_mov_b64 exec, s[34:35]\n"
"s_mov_b32 s36, 270336\n"
"buffer_load_dwordx4 v[152:155], v97, s[72:75], s36 offen sc1\n"
"s_mov_b32 s36, 274432\n"
"buffer_load_dwordx4 v[156:159], v97, s[72:75], s36 offen sc1\n"
"s_mov_b64 exec, -1\n"
"s_waitcnt vmcnt(0)\n"
"s_add_u32 s94, s94, 1\n"
"s_cmp_gt_u32 s94, 0x20000\n"
"s_cbranch_scc1 Lret_g1_K\n"
"s_mov_b64 exec, s[28:29]\n"
"v_xor_b32 v128, s38, v128\n"
"v_xor_b32 v129, s38, v129\n"
"v_xor_b32 v130, s38, v130\n"
"v_xor_b32 v131, s38, v131\n"
"v_xor_b32 v132, s38, v132\n"
"v_xor_b32 v133, s38, v133\n"
"v_xor_b32 v134, s38, v134\n"
"v_xor_b32 v135, s38, v135\n"
"s_mov_b64 exec, s[30:31]\n"
"v_xor_b32 v136, s38, v136\n"
"v_xor_b32 v137, s38, v137\n"
"v_xor_b32 v138, s38, v138\n"
"v_xor_b32 v139, s38, v139\n"
"v_xor_b32 v140, s38, v140\n"
"v_xor_b32 v141, s38, v141\n"
"v_xor_b32 v142, s38, v142\n"
"v_xor_b32 v143, s38, v143\n"
"s_mov_b64 exec, s[32:33]\n"
"v_xor_b32 v144, s38, v144\n"
"v_xor_b32 v145, s38, v145\n"
"v_xor_b32 v146, s38, v146\n"
"v_xor_b32 v147, s38, v147\n"
"v_xor_b32 v148, s38, v148\n"
"v_xor_b32 v149, s38, v149\n"
"v_xor_b32 v150, s38, v150\n"
"v_xor_b32 v151, s38, v151\n"
"s_mov_b64 exec, s[34:35]\n"
"v_xor_b32 v152, s38, v152\n"
"v_xor_b32 v153, s38, v153\n"
"v_xor_b32 v154, s38, v154\n"
"v_xor_b32 v155, s38, v155\n"
"v_xor_b32 v156, s38, v156\n"
"v_xor_b32 v157, s38, v157\n"
"v_xor_b32 v158, s38, v158\n"
"v_xor_b32 v159, s38, v159\n"
"s_mov_b64 exec, -1\n"
"v_or3_b32 v239, v128, v129, v130\n"
"v_or3_b32 v239, v239, v131, v132\n"
"v_or3_b32 v239, v239, v133, v134\n"
"v_bitop3_b32 v239, v239, v135, v247 bitop3:0x54\n"
"v_cmp_ne_u32 s[28:29], 0, v239\n"
"v_or3_b32 v239, v136, v137, v138\n"
"v_or3_b32 v239, v239, v139, v140\n"
"v_or3_b32 v239, v239, v141, v142\n"
"v_bitop3_b32 v239, v239, v143, v247 bitop3:0x54\n"
"v_cmp_ne_u32 s[30:31], 0, v239\n"
"v_or3_b32 v239, v144, v145, v146\n"
"v_or3_b32 v239, v239, v147, v148\n"
"v_or3_b32 v239, v239, v149, v150\n"
"v_bitop3_b32 v239, v239, v151, v247 bitop3:0x54\n"
"v_cmp_ne_u32 s[32:33], 0, v239\n"
"v_or3_b32 v239, v152, v153, v154\n"
"v_or3_b32 v239, v239, v155, v156\n"
"v_or3_b32 v239, v239, v157, v158\n"
"v_bitop3_b32 v239, v239, v159, v247 bitop3:0x54\n"
"v_cmp_ne_u32 s[34:35], 0, v239\n"
"s_nop 1\n"
"s_or_b64 vcc, s[28:29], s[30:31]\n"
"s_or_b64 vcc, vcc, s[32:33]\n"
"s_or_b64 vcc, vcc, s[34:35]\n"
"s_cbranch_scc1 Lslow_g1_K\n"
"s_branch Lret_g1_K\n"
"Lslow_wx_K:\n"
"s_sleep 6\n"
"s_wqm_b64 s[28:29], s[28:29]\n"
"s_wqm_b64 s[30:31], s[30:31]\n"
"s_wqm_b64 s[32:33], s[32:33]\n"
"s_wqm_b64 s[34:35], s[34:35]\n"
"s_mov_b64 exec, s[28:29]\n"
"buffer_load_dwordx4 v[64:67], v118, s[72:75], s8 offen sc1\n"
"s_add_u32 s36, s8, 32768\n"
"s_mov_b64 exec, s[30:31]\n"
"buffer_load_dwordx4 v[68:71], v118, s[72:75], s36 offen sc1\n"
"s_add_u32 s36, s8, 65536\n"
"s_mov_b64 exec, s[32:33]\n"
"buffer_load_dwordx4 v[72:75], v118, s[72:75], s36 offen sc1\n"
"s_add_u32 s36, s8, 98304\n"
"s_mov_b64 exec, s[34:35]\n"
"buffer_load_dwordx4 v[76:79], v118, s[72:75], s36 offen sc1\n"
"s_mov_b64 exec, -1\n"
"s_waitcnt vmcnt(0)\n"
"s_add_u32 s94, s94, 1\n"
"s_cmp_gt_u32 s94, 0x20000\n"
"s_cbranch_scc1 Lret_wx_K\n"
"s_mov_b64 exec, s[28:29]\n"
"v_xor_b32 v64, s39, v64\n"
"v_xor_b32 v65, s39, v65\n"
"v_xor_b32 v66, s39, v66\n"
"v_xor_b32 v67, s39, v67\n"
"s_mov_b64 exec, s[30:31]\n"
"v_xor_b32 v68, s39, v68\n"
"v_xor_b32 v69, s39, v69\n"
"v_xor_b32 v70, s39, v70\n"
"v_xor_b32 v71, s39, v71\n"
"s_mov_b64 exec, s[32:33]\n"
"v_xor_b32 v72, s39, v72\n"
"v_xor_b32 v73, s39, v73\n"
"v_xor_b32 v74, s39, v74\n"
"v_xor_b32 v75, s39, v75\n"
"s_mov_b64 exec, s[34:35]\n"
"v_xor_b32 v76, s39, v76\n"
"v_xor_b32 v77, s39, v77\n"
"v_xor_b32 v78, s39, v78\n"
"v_xor_b32 v79, s39, v79\n"
"s_mov_b64 exec, -1\n"
"v_or3_b32 v238, v64, v65, v66\n"
"v_bitop3_b32 v238, v238, v67, v236 bitop3:0x54\n"
"v_cmp_ne_u32 s[28:29], 0, v238\n"
"v_or3_b32 v238, v68, v69, v70\n"
"v_bitop3_b32 v238, v238, v71, v236 bitop3:0x54\n"
"v_cmp_ne_u32 s[30:31], 0, v238\n"
"v_or3_b32 v238, v72, v73, v74\n"
"v_bitop3_b32 v238, v238, v75, v236 bitop3:0x54\n"
"v_cmp_ne_u32 s[32:33], 0, v238\n"
"v_or3_b32 v238, v76, v77, v78\n"
"v_bitop3_b32 v238, v238, v79, v236 bitop3:0x54\n"
"v_cmp_ne_u32 s[34:35], 0, v238\n"
"s_nop 1\n"
"s_or_b64 vcc, s[28:29], s[30:31]\n"
"s_or_b64 vcc, vcc, s[32:33]\n"
"s_or_b64 vcc, vcc, s[34:35]\n"
"s_cbranch_scc1 Lslow_wx_K\n"
"s_branch Lret_wx_K\n"
"Lslow_nw_K:\n"
"s_sleep 0\n"
"s_lshl_b32 s28, s18, 8\n"
"s_add_u32 s28, s28, 122880\n"
"buffer_load_dwordx2 v[64:65], v127, s[72:75], s28 offen offset:0 sc1\n"
"buffer_load_dwordx2 v[66:67], v127, s[72:75], s28 offen offset:128 sc1\n"
"buffer_load_dwordx2 v[68:69], v127, s[72:75], s28 offen offset:256 sc1\n"
"buffer_load_dwordx2 v[70:71], v127, s[72:75], s28 offen offset:384 sc1\n"
"s_waitcnt vmcnt(0)\n"
"v_xor_b32 v238, v65, v117\n"
"v_bitop3_b32 v238, v238, v67, v117 bitop3:0xf6\n"
"v_bitop3_b32 v238, v238, v69, v117 bitop3:0xf6\n"
"v_bitop3_b32 v238, v238, v71, v117 bitop3:0xf6\n"
"v_cmp_ne_u32 vcc, 0, v238\n"
"v_cvt_f32_f16 v176, v64\n"
"v_cvt_f32_f16_sdwa v180, v64 dst_sel:DWORD dst_unused:UNUSED_PAD src0_sel:WORD_1\n"
"v_cvt_f32_f16 v177, v66\n"
"v_cvt_f32_f16_sdwa v181, v66 dst_sel:DWORD dst_unused:UNUSED_PAD src0_sel:WORD_1\n"
"v_cvt_f32_f16 v178, v68\n"
"v_cvt_f32_f16_sdwa v182, v68 dst_sel:DWORD dst_unused:UNUSED_PAD src0_sel:WORD_1\n"
"v_cvt_f32_f16 v179, v70\n"
"v_cvt_f32_f16_sdwa v183, v70 dst_sel:DWORD dst_unused:UNUSED_PAD src0_sel:WORD_1\n"
"s_add_u32 s94, s94, 1\n"
"s_cmp_gt_u32 s94, 0x20000\n"
"s_cbranch_scc1 Lret_nw_K\n"
"s_nop 1\n"
"s_cbranch_vccnz Lslow_nw_K\n"
"s_branch Lret_nw_K\n"
"Lslow_i2_K:\n"
"s_sleep 6\n"
"s_wqm_b64 s[28:29], s[28:29]\n"
"s_wqm_b64 s[30:31], s[30:31]\n"
"s_wqm_b64 s[32:33], s[32:33]\n"
"s_wqm_b64 s[34:35], s[34:35]\n"
"s_mov_b64 exec, s[28:29]\n"
"buffer_load_dwordx4 v[128:131], v100, s[72:75], s13 offen sc1\n"
"s_add_u32 s36, s13, 264192\n"
"s_mov_b64 exec, s[30:31]\n"
"buffer_load_dwordx4 v[132:135], v100, s[72:75], s36 offen sc1\n"
"s_add_u32 s36, s13, 528384\n"
"s_mov_b64 exec, s[32:33]\n"
"buffer_load_dwordx4 v[136:139], v100, s[72:75], s36 offen sc1\n"
"s_add_u32 s36, s13, 792576\n"
"s_mov_b64 exec, s[34:35]\n"
"buffer_load_dwordx4 v[140:143], v100, s[72:75], s36 offen sc1\n"
"s_mov_b64 exec, -1\n"
"s_waitcnt vmcnt(0)\n"
"s_add_u32 s94, s94, 1\n"
"s_cmp_gt_u32 s94, 0x20000\n"
"s_cbranch_scc1 Lret_i2_K\n"
"s_mov_b64 exec, s[28:29]\n"
"v_xor_b32 v128, s37, v128\n"
"v_xor_b32 v129, s37, v129\n"
"v_xor_b32 v130, s37, v130\n"
"v_xor_b32 v131, s37, v131\n"
"s_mov_b64 exec, s[30:31]\n"
"v_xor_b32 v132, s37, v132\n"
"v_xor_b32 v133, s37, v133\n"
"v_xor_b32 v134, s37, v134\n"
"v_xor_b32 v135, s37, v135\n"
"s_mov_b64 exec, s[32:33]\n"
"v_xor_b32 v136, s37, v136\n"
"v_xor_b32 v137, s37, v137\n"
"v_xor_b32 v138, s37, v138\n"
"v_xor_b32 v139, s37, v139\n"
"s_mov_b64 exec, s[34:35]\n"
"v_xor_b32 v140, s37, v140\n"
"v_xor_b32 v141, s37, v141\n"
"v_xor_b32 v142, s37, v142\n"
"v_xor_b32 v143, s37, v143\n"
"s_mov_b64 exec, -1\n"
"v_or3_b32 v238, v128, v129, v130\n"
"v_bitop3_b32 v238, v238, v131, v236 bitop3:0x54\n"
"v_cmp_ne_u32 s[28:29], 0, v238\n"
"v_or3_b32 v238, v132, v133, v134\n"
"v_bitop3_b32 v238, v238, v135, v236 bitop3:0x54\n"
"v_cmp_ne_u32 s[30:31], 0, v238\n"
"v_or3_b32 v238, v136, v137, v138\n"
"v_bitop3_b32 v238, v238, v139, v236 bitop3:0x54\n"
"v_cmp_ne_u32 s[32:33], 0, v238\n"
"v_or3_b32 v238, v140, v141, v142\n"
"v_bitop3_b32 v238, v238, v143, v236 bitop3:0x54\n"
"v_cmp_ne_u32 s[34:35], 0, v238\n"
"s_nop 1\n"
"s_or_b64 vcc, s[28:29], s[30:31]\n"
"s_or_b64 vcc, vcc, s[32:33]\n"
"s_or_b64 vcc, vcc, s[34:35]\n"
"s_cbranch_scc1 Lslow_i2_K\n"
"s_branch Lret_i2_K\n"
"Lslow_hop_K:\n"
"s_sleep 4\n"
"s_add_u32 s36, s101, 204800\n"
"buffer_load_dwordx4 v[208:211], v233, s[72:75], s36 offen sc1\n"
"s_waitcnt vmcnt(0)\n"
"v_xor_b32 v208, s38, v208\n"
"v_xor_b32 v209, s38, v209\n"
"v_xor_b32 v210, s38, v210\n"
"v_xor_b32 v211, s38, v211\n"
"v_or3_b32 v238, v208, v209, v210\n"
"v_bitop3_b32 v238, v238, v211, v247 bitop3:0x54\n"
"v_cmp_ne_u32 vcc, 0, v238\n"
"s_add_u32 s94, s94, 1\n"
"s_cmp_gt_u32 s94, 0x20000\n"
"s_cbranch_scc1 Lret_hop_K\n"
"s_nop 1\n"
"s_cbranch_vccnz Lslow_hop_K\n"
"s_branch Lret_hop_K\n"
"Lslow_pq_K:\n"
"s_sleep 4\n"
"s_mov_b32 s36, 294912\n"
"buffer_load_dwordx4 v[64:67], v97, s[72:75], s36 offen sc1\n"
"s_mov_b32 s36, 299008\n"
"buffer_load_dwordx4 v[68:71], v97, s[72:75], s36 offen sc1\n"
"s_mov_b32 s36, 303104\n"
"buffer_load_dwordx4 v[72:75], v97, s[72:75], s36 offen sc1\n"
"s_waitcnt vmcnt(0)\n"
"v_xor_b32 v238, v64, v117\n"
"v_bitop3_b32 v238, v238, v65, v117 bitop3:0xf6\n"
"v_bitop3_b32 v238, v238, v66, v117 bitop3:0xf6\n"
"v_bitop3_b32 v238, v238, v67, v117 bitop3:0xf6\n"
"v_bitop3_b32 v238, v238, v68, v117 bitop3:0xf6\n"
"v_bitop3_b32 v238, v238, v69, v117 bitop3:0xf6\n"
"v_bitop3_b32 v238, v238, v70, v117 bitop3:0xf6\n"
"v_bitop3_b32 v238, v238, v71, v117 bitop3:0xf6\n"
"v_bitop3_b32 v238, v238, v72, v117 bitop3:0xf6\n"
"v_bitop3_b32 v238, v238, v73, v117 bitop3:0xf6\n"
"v_bitop3_b32 v238, v238, v74, v117 bitop3:0xf6\n"
"v_bitop3_b32 v238, v238, v75, v117 bitop3:0xf6\n"
"v_and_b32 v238, v238, v237\n"
"v_cmp_ne_u32 vcc, 0, v238\n"
"s_add_u32 s94, s94, 1\n"
"s_cmp_gt_u32 s94, 0x20000\n"
"s_cbranch_scc1 Lret_pq_K\n"
"s_nop 1\n"
"s_cbranch_vccnz Lslow_pq_K\n"
"s_branch Lret_pq_K\n"
"Lslow_i3_K:\n"
"s_sleep 6\n"
"s_wqm_b64 s[28:29], s[28:29]\n"
"s_wqm_b64 s[30:31], s[30:31]\n"
"s_wqm_b64 s[32:33], s[32:33]\n"
"s_wqm_b64 s[34:35], s[34:35]\n"
"s_mov_b64 exec, s[28:29]\n"
"buffer_load_dwordx4 v[128:131], v103, s[72:75], s16 offen sc1\n"
"s_add_u32 s36, s16, 262144\n"
"s_mov_b64 exec, s[30:31]\n"
"buffer_load_dwordx4 v[132:135], v103, s[72:75], s36 offen sc1\n"
"s_add_u32 s36, s16, 524288\n"
"s_mov_b64 exec, s[32:33]\n"
"buffer_load_dwordx4 v[136:139], v103, s[72:75], s36 offen sc1\n"
"s_add_u32 s36, s16, 786432\n"
"s_mov_b64 exec, s[34:35]\n"
"buffer_load_dwordx4 v[140:143], v103, s[72:75], s36 offen sc1\n"
"s_mov_b64 exec, -1\n"
"s_waitcnt vmcnt(0)\n"
"s_add_u32 s94, s94, 1\n"
"s_cmp_gt_u32 s94, 0x20000\n"
"s_cbranch_scc1 Lret_i3_K\n"
"s_mov_b64 exec, s[28:29]\n"
"v_xor_b32 v128, s37, v128\n"
"v_xor_b32 v129, s37, v129\n"
"v_xor_b32 v130, s37, v130\n"
"v_xor_b32 v131, s37, v131\n"
"s_mov_b64 exec, s[30:31]\n"
"v_xor_b32 v132, s37, v132\n"
"v_xor_b32 v133, s37, v133\n"
"v_xor_b32 v134, s37, v134\n"
"v_xor_b32 v135, s37, v135\n"
"s_mov_b64 exec, s[32:33]\n"
"v_xor_b32 v136, s37, v136\n"
"v_xor_b32 v137, s37, v137\n"
"v_xor_b32 v138, s37, v138\n"
"v_xor_b32 v139, s37, v139\n"
"s_mov_b64 exec, s[34:35]\n"
"v_xor_b32 v140, s37, v140\n"
"v_xor_b32 v141, s37, v141\n"
"v_xor_b32 v142, s37, v142\n"
"v_xor_b32 v143, s37, v143\n"
"s_mov_b64 exec, -1\n"
"v_or3_b32 v238, v128, v129, v130\n"
"v_bitop3_b32 v238, v238, v131, v236 bitop3:0x54\n"
"v_cmp_ne_u32 s[28:29], 0, v238\n"
"v_or3_b32 v238, v132, v133, v134\n"
"v_bitop3_b32 v238, v238, v135, v236 bitop3:0x54\n"
"v_cmp_ne_u32 s[30:31], 0, v238\n"
"v_or3_b32 v238, v136, v137, v138\n"
"v_bitop3_b32 v238, v238, v139, v236 bitop3:0x54\n"
"v_cmp_ne_u32 s[32:33], 0, v238\n"
"v_or3_b32 v238, v140, v141, v142\n"
"v_bitop3_b32 v238, v238, v143, v236 bitop3:0x54\n"
"v_cmp_ne_u32 s[34:35], 0, v238\n"
"s_nop 1\n"
"s_or_b64 vcc, s[28:29], s[30:31]\n"
"s_or_b64 vcc, vcc, s[32:33]\n"
"s_or_b64 vcc, vcc, s[34:35]\n"
"s_cbranch_scc1 Lslow_i3_K\n"
"s_branch Lret_i3_K\n"
"Lslow_a1_K:\n"
"s_sleep 1\n"
"buffer_load_dword v2, v20, s[72:75], 0 offen sc1\n"
"s_waitcnt vmcnt(0)\n"
"v_cmp_ne_u32 vcc, s50, v2\n"
"s_add_u32 s94, s94, 1\n"
"s_cmp_gt_u32 s94, 0x20000\n"
"s_cbranch_scc1 Lret_a1_K\n"
"s_nop 1\n"
"s_cbranch_vccnz Lslow_a1_K\n"
"s_branch Lret_a1_K\n"
"Lslow_gb_K:\n"
"s_sleep 1\n"
"buffer_load_dwordx4 v[4:7], v104, s[72:75], 0 offen offset:0 sc1\n"
"buffer_load_dwordx4 v[8:11], v104, s[72:75], 0 offen offset:16 sc1\n"
"s_waitcnt vmcnt(0)\n"
"v_xor_b32 v3, s50, v4\n"
"v_bitop3_b32 v3, v3, v5, s50 bitop3:0xf6\n"
"v_bitop3_b32 v3, v3, v6, s50 bitop3:0xf6\n"
"v_bitop3_b32 v3, v3, v7, s50 bitop3:0xf6\n"
"v_bitop3_b32 v3, v3, v8, s50 bitop3:0xf6\n"
"v_bitop3_b32 v3, v3, v9, s50 bitop3:0xf6\n"
"v_bitop3_b32 v3, v3, v10, s50 bitop3:0xf6\n"
"v_bitop3_b32 v3, v3, v11, s50 bitop3:0xf6\n"
"v_and_b32 v3, 0xffff, v3\n"
"v_cmp_ne_u32 vcc, 0, v3\n"
"s_add_u32 s94, s94, 1\n"
"s_cmp_gt_u32 s94, 0x20000\n"
"s_cbranch_scc1 Lret_gb_K\n"
"s_nop 1\n"
"s_cbranch_vccnz Lslow_gb_K\n"
"s_branch Lret_gb_K\n"
"Lend_K:\n"
        : "+{v[112:127]}"(vin), "+{v[96:111]}"(fin), "+{s[40:55]}"(s0), "+{s[56:63]}"(s1), "+{s[8:23]}"(fs), "+{v[16:31]}"(xr), "+{v[40:47]}"(cr), "+{s[0:7]}"(p0), "+{s[96:99]}"(am),
          "+{s[100:101]}"(fx), "+{s[72:79]}"(d0), "+{s[80:87]}"(d1), "+{s[88:91]}"(pp)
        :
        : "memory", "scc", "vcc", "v0", "v95", "v128", "v255", "a0", "a255", "s24", "s39", "s64", "s71", "s92", "s95");
  }
}

extern "C" void kernel_launch(void* const* d_in, const int* in_sizes, int n_in, void* d_out, int out_size, void* d_ws, size_t ws_size, hipStream_t stream) {
  const u16* x = (const u16*)d_in[0]; const u16* wr = (const u16*)d_in[1]; const u16* wi = (const u16*)d_in[2];
  const u16* w1 = (const u16*)d_in[4]; const u16* b1 = (const u16*)d_in[5]; const u16* w2 = (const u16*)d_in[6]; const u16* b2 = (const u16*)d_in[7];
  const u16* c = (const u16*)d_in[8]; const u16* wmod = (const u16*)d_in[9];
  char* ws = (char*)d_ws + WSOFF;
  hipFuncSetAttribute((const void*)kmega, hipFuncAttributeMaxDynamicSharedMemorySize, 163840);
  kmega<<<256, 256, 163840, stream>>>(x, wr, wi, c, wmod, w1, b1, w2, b2, ws, (u16*)d_out);
}
```
